```python
import torch

def _softmax(x, axis=-1):
    return torch.softmax(x, dim=axis)
def _gelu(x):
    return torch.nn.functional.gelu(x, approximate="tanh")
def _silu(x):
    return torch.nn.functional.silu(x)
def _sigmoid(x):
    return torch.sigmoid(x)
def _layernorm(x, axis=-1, eps=1e-5):
    m = x.mean(dim=axis, keepdim=True)
    v = x.var(dim=axis, keepdim=True, unbiased=False)
    return (x - m) / torch.sqrt(v + eps)
def _scatter_add(tgt, idx, src, axis=0):
    return tgt.clone().index_add_(axis, idx, src)
def _segment_sum(src, seg, n):
    out = torch.zeros((n,) + src.shape[1:], dtype=src.dtype)
    return out.index_add_(0, seg, src)
def _pad(x, pw):
    flat = tuple(v for pair in reversed(pw) for v in pair)
    return torch.nn.functional.pad(x, flat)

kernel_name = 'recurrent_scan__spectral_gfnet__dense_activation_squared_relu__prompt'

def setup_inputs():

    _w = (lambda a: (lambda t: t.to(torch.bfloat16) if t.is_floating_point() else t)(torch.as_tensor(a)))
    def _A_setup():
        B, S, D = 1, 2048, 1024
        Df = D // 2 + 1
        return {
            "x": _w((lambda *s: torch.randn(*s, dtype=torch.bfloat16) * 0.05)(B, S, D)),
            "w_r": _w((lambda *s: torch.randn(*s, dtype=torch.bfloat16) * 0.05)(S, Df) * 0.02),
            "w_i": _w((lambda *s: torch.randn(*s, dtype=torch.bfloat16) * 0.05)(S, Df) * 0.02),
        }
    _A_in = _A_setup()

    def _B_setup():
        B, S, D, F = 1, 2048, 1024, 3584
        return {
            "x": _w((lambda *s: torch.randn(*s, dtype=torch.bfloat16) * 0.05)(B, S, D)),
            "w1": _w((lambda *s: torch.randn(*s, dtype=torch.bfloat16) * 0.05)(D, F)),
            "b1": _w((lambda *s: torch.randn(*s, dtype=torch.bfloat16) * 0.4)(F)),
            "w2": _w((lambda *s: torch.randn(*s, dtype=torch.bfloat16) * 0.05)(F, D)),
            "b2": _w((lambda *s: torch.randn(*s, dtype=torch.bfloat16) * 3.2)(D)),
        }
    _B_in = _B_setup()

    return {
        "A_x": _A_in["x"],
        "A_w_r": _A_in["w_r"],
        "A_w_i": _A_in["w_i"],
        "B_x": _B_in["x"],
        "B_w1": _B_in["w1"],
        "B_b1": _B_in["b1"],
        "B_w2": _B_in["w2"],
        "B_b2": _B_in["b2"],
    }

def reference(A_x, A_w_r, A_w_i, B_x, B_w1, B_b1, B_w2, B_b2):
    def _A_ref(x, w_r, w_i):
        B, S, D = x.shape
        X = (lambda x, axes=(-2,-1): torch.fft.rfft2(x.float(), dim=axes))(x, axes=(-2, -1))
        W = w_r + 1j * w_i
        X = X * W
        y = (lambda x, s, axes=(-2,-1): torch.fft.irfft2(x, s=s, dim=axes).to(torch.bfloat16))(X, (S, D), axes=(-2, -1))
        return y

    def _B_ref(x, w1, b1, w2, b2):
        h = torch.einsum("bsd,df->bsf", x, w1) + b1
        zero = torch.zeros_like(h)
        r = torch.where(h > zero, h, zero)
        h = r * r
        out = torch.einsum("bsf,fd->bsd", h, w2) + b2
        return out

    x = A_x
    xn = _layernorm(x, axis=-1)
    s = _A_ref(xn, A_w_r, A_w_i)
    g = _silu(_B_ref(xn, B_w1, B_b1, B_w2, B_b2))
    return x + s * g
```

```cpp
#include <hip/hip_runtime.h>
#include <hip/hip_bf16.h>
#include <stdint.h>

#define DI __device__ __forceinline__
#define LAS __attribute__((address_space(3)))
typedef short s16x4 __attribute__((ext_vector_type(4)));
typedef short s16x8 __attribute__((ext_vector_type(8)));
typedef _Float16 h16x2 __attribute__((ext_vector_type(2)));
typedef _Float16 h16x8 __attribute__((ext_vector_type(8)));
typedef __bf16 b16x2 __attribute__((ext_vector_type(2)));
typedef __bf16 b16x8 __attribute__((ext_vector_type(8)));
typedef float f32x2 __attribute__((ext_vector_type(2)));
typedef float f32x4 __attribute__((ext_vector_type(4)));
typedef float f32x16 __attribute__((ext_vector_type(16)));
typedef unsigned u32x2 __attribute__((ext_vector_type(2)));
typedef unsigned u32x4 __attribute__((ext_vector_type(4)));
typedef unsigned short u16;

constexpr int S = 2048, D = 1024, F = 3584, DF = 513;
constexpr size_t WS_XN = 0;
constexpr size_t WS_Z = WS_XN + (4u << 20);
constexpr size_t WS_Q = WS_Z + (4u << 20);
constexpr size_t WS_WT = WS_Q + (4u << 20);
constexpr size_t WS_ZN = WS_WT + (5u << 20);
constexpr size_t WS_QN = WS_ZN + (64u << 10);
constexpr size_t WS_H = WS_QN + (64u << 10);
constexpr size_t WS_P = WS_H + (16u << 20);

DI unsigned pk_bf16(float lo, float hi) { f32x2 v = {lo, hi}; return __builtin_bit_cast(unsigned, __builtin_convertvector(v, b16x2)); }
DI unsigned pk_f16(float lo, float hi) { f32x2 v = {lo, hi}; return __builtin_bit_cast(unsigned, __builtin_convertvector(v, h16x2)); }
DI float bf_lo(unsigned u) { return __uint_as_float(u << 16); }
DI float bf_hi(unsigned u) { return __uint_as_float(u & 0xffff0000u); }
DI float rbf(float x) { return bf_lo(pk_bf16(x, x)); }
DI f32x16 mfma_h(u32x4 a, u32x4 b, f32x16 c) { return __builtin_amdgcn_mfma_f32_32x32x16_f16(__builtin_bit_cast(h16x8, a), __builtin_bit_cast(h16x8, b), c, 0, 0, 0); }
DI f32x16 mfma_b(u32x4 a, u32x4 b, f32x16 c) { return __builtin_amdgcn_mfma_f32_32x32x16_bf16(__builtin_bit_cast(b16x8, a), __builtin_bit_cast(b16x8, b), c, 0, 0, 0); }
DI u32x2 tr_read(LAS char* p) { return __builtin_bit_cast(u32x2, __builtin_amdgcn_ds_read_tr16_b64_v4i16((LAS s16x4*)p)); }
DI int crow(int r, int h) { return (r & 3) + 8 * (r >> 2) + 4 * h; }
DI float sinr(float x) { return __builtin_amdgcn_sinf(x); }
DI float cosr(float x) { return __builtin_amdgcn_cosf(x); }
template <bool HALF> DI unsigned pk16(float a, float b) { return HALF ? pk_f16(a, b) : pk_bf16(a, b); }
template <bool HALF> DI void pack16(const float (&v)[16], u32x4 (&o)[2]) {
#pragma unroll
  for (int s = 0; s < 2; ++s)
#pragma unroll
    for (int i = 0; i < 4; ++i) o[s][i] = pk16<HALF>(v[8 * s + 2 * i], v[8 * s + 2 * i + 1]);
}
DI u32x4 neg16(u32x4 v) { return v ^ 0x80008000u; }
DI float wave_sum(float v) {
#pragma unroll
  for (int o = 32; o >= 1; o >>= 1) v += __shfl_xor(v, o);
  return v;
}

__global__ __launch_bounds__(512) void k1_ln_rowfft(const u16* __restrict__ x, const u16* __restrict__ wr, const u16* __restrict__ wi, char* __restrict__ ws) {
  __shared__ __attribute__((aligned(16))) char smem_[16384 + 16384 + 16384];
  LAS char* smem = (LAS char*)smem_;
  const int tid = threadIdx.x, w = __builtin_amdgcn_readfirstlane(tid >> 6), l = tid & 63, c = l & 31, h = l >> 5;
  const int blk = blockIdx.x, q = blk & 7, n2 = blk >> 3;
  const int e = w & 3, p = w >> 2;
  const int sp = 128 * q + 32 * e + n2;
  const int row = sp + 1024 * p;
  const u32x4 x0 = *(const u32x4*)(x + (size_t)row * D + 8 * l), x1 = *(const u32x4*)(x + (size_t)row * D + 512 + 8 * l);
  unsigned wg[2][4];
  {
    const int gp = n2 >> 2, mo = n2 & 3, k = 64 * q + l;
#pragma unroll
    for (int pp = 0; pp < 2; ++pp)
#pragma unroll
      for (int ee = 0; ee < 4; ++ee) {
        const int u = 16 * mo + 2 * w + pp + 64 * (4 * gp + ee);
        wg[pp][ee] = (unsigned)wr[(size_t)u * DF + k] | ((unsigned)wi[(size_t)u * DF + k] << 16);
      }
    if (q == 7 && l < 2) {
      u32x4 g;
#pragma unroll
      for (int ee = 0; ee < 4; ++ee) { const int u = 16 * mo + 2 * w + l + 64 * (4 * gp + ee); g[ee] = (unsigned)wr[(size_t)u * DF + 512] | ((unsigned)wi[(size_t)u * DF + 512] << 16); }
      *(u32x4*)(ws + WS_WT + (size_t)(512 * 2 + l) * 4096 + (gp >> 1) * 1024 + ((gp & 1) * 32 + 8 * mo + w) * 16) = g;
    }
  }
  u32x4 C1[2], nS1[2], M1[2], M2[2]; float tc[16], ts[16];
  {
    float a[16], b[16];
#pragma unroll
    for (int r = 0; r < 16; ++r) { const float an = (float)(crow(r, h) * c) * (1.f / 32); a[r] = cosr(an); b[r] = -sinr(an); }
    pack16<true>(a, C1); pack16<true>(b, nS1);
#pragma unroll
    for (int r = 0; r < 16; ++r) { const float an = (float)(crow(r, h) * (c >> 1)) * (1.f / 32) + 0.25f * (c & 1); a[r] = cosr(an); b[r] = sinr(an); }
    pack16<true>(a, M1); pack16<true>(b, M2);
    const float rot = (float)(p * sp) * (1.f / 2048);
#pragma unroll
    for (int r = 0; r < 16; ++r) { const float an = (float)(crow(r, h) * c) * (1.f / 1024) + rot; tc[r] = cosr(an) * (1.f / 32); ts[r] = sinr(an) * (1.f / 32); }
  }
  float v[16];
#pragma unroll
  for (int i = 0; i < 4; ++i) { v[2 * i] = bf_lo(x0[i]); v[2 * i + 1] = bf_hi(x0[i]); v[8 + 2 * i] = bf_lo(x1[i]); v[9 + 2 * i] = bf_hi(x1[i]); }
  float sm = 0;
#pragma unroll
  for (int i = 0; i < 16; ++i) sm += v[i];
  const float mean = wave_sum(sm) * (1.f / D);
  float sq = 0;
#pragma unroll
  for (int i = 0; i < 16; ++i) { const float d = v[i] - mean; sq += d * d; }
  const float var = wave_sum(sq) * (1.f / D);
  const float mb = rbf(mean), sd = rbf(sqrtf(rbf(rbf(var) + 1e-5f))), rs = 1.f / sd;
  u32x4 xb[2], xh[2];
#pragma unroll
  for (int s = 0; s < 2; ++s)
#pragma unroll
    for (int i = 0; i < 4; ++i) {
      const unsigned d = pk_bf16(v[8 * s + 2 * i] - mb, v[8 * s + 2 * i + 1] - mb);
      const unsigned n = pk_bf16(bf_lo(d) * rs, bf_hi(d) * rs);
      xb[s][i] = n; xh[s][i] = pk_f16(bf_lo(n), bf_hi(n));
    }
  *(u32x4*)(ws + WS_XN + ((size_t)row * D + 8 * l) * 2) = xb[0];
  *(u32x4*)(ws + WS_XN + ((size_t)row * D + 512 + 8 * l) * 2) = xb[1];
  *(LAS u32x4*)(smem + w * 2048 + l * 16) = xh[0];
  *(LAS u32x4*)(smem + w * 2048 + 1024 + l * 16) = xh[1];
#pragma unroll
  for (int pp = 0; pp < 2; ++pp) *(LAS u32x4*)(smem + 32768 + pp * 8192 + l * 128 + ((w ^ (l & 7)) * 16)) = u32x4{wg[pp][0], wg[pp][1], wg[pp][2], wg[pp][3]};
  __syncthreads();
  {
    const int gp = n2 >> 2, mo = n2 & 3;
#pragma unroll
    for (int pp = 0; pp < 2; ++pp) {
      const int kk = tid >> 3, ww = tid & 7;
      const u32x4 g = *(LAS u32x4*)(smem + 32768 + pp * 8192 + kk * 128 + ((ww ^ (kk & 7)) * 16));
      *(u32x4*)(ws + WS_WT + (size_t)((64 * q + kk) * 2 + pp) * 4096 + (gp >> 1) * 1024 + ((gp & 1) * 32 + 8 * mo + ww) * 16) = g;
    }
  }
  u32x4 A[2];
  {
    const int qq = (l & 15) >> 2, pp = l & 3, bl = (l >> 4) & 1;
    LAS char* bt = smem + e * 2048 + (4 * h + qq) * 64 + bl * 32 + pp * 8;
#pragma unroll
    for (int ks = 0; ks < 2; ++ks) {
      const u32x2 t0 = tr_read(bt + ks * 1024), t1 = tr_read(bt + ks * 1024 + 512);
      const u32x2 b0 = tr_read(bt + 8192 + ks * 1024), b1 = tr_read(bt + 8192 + ks * 1024 + 512);
      const h16x8 T = __builtin_bit_cast(h16x8, u32x4{t0[0], t0[1], t1[0], t1[1]});
      const u32x4 Bu = u32x4{b0[0], b0[1], b1[0], b1[1]} ^ (p ? 0x80008000u : 0u);
      A[ks] = __builtin_bit_cast(u32x4, T + __builtin_bit_cast(h16x8, Bu));
    }
  }
  f32x16 Dr = {}, Di = {};
  Dr = mfma_h(A[0], C1[0], Dr); Di = mfma_h(A[0], nS1[0], Di);
  Dr = mfma_h(A[1], C1[1], Dr); Di = mfma_h(A[1], nS1[1], Di);
  float er[16], ei[16];
#pragma unroll
  for (int r = 0; r < 16; ++r) { er[r] = Dr[r] * tc[r] + Di[r] * ts[r]; ei[r] = Di[r] * tc[r] - Dr[r] * ts[r]; }
  {
    float nr = 0, ni = 0;
#pragma unroll
    for (int r = 0; r < 16; r += 2) { nr += er[r] - er[r + 1]; ni += ei[r] - ei[r + 1]; }
    nr += __shfl_xor(nr, 32); ni += __shfl_xor(ni, 32);
    if (l == 0) *(unsigned*)(ws + WS_ZN + (p * 1024 + sp) * 4) = pk_f16(nr, ni);
  }
  u32x4 Er[2], Ei[2]; pack16<true>(er, Er); pack16<true>(ei, Ei);
  f32x16 Y = {};
  Y = mfma_h(M1[0], Er[0], Y); Y = mfma_h(M2[0], Ei[0], Y); Y = mfma_h(M1[1], Er[1], Y); Y = mfma_h(M2[1], Ei[1], Y);
#pragma unroll
  for (int g = 0; g < 4; ++g) *(LAS u32x2*)(smem + 16384 + w * 2048 + ((2 * g + h) * 32 + c) * 8) = u32x2{pk_f16(Y[4 * g], Y[4 * g + 1]), pk_f16(Y[4 * g + 2], Y[4 * g + 3])};
  __syncthreads();
  {
    const int qq = (l & 15) >> 2, pp = l & 3;
#pragma unroll
    for (int i = 0; i < 2; ++i) {
      const int cons0 = (w * 2 + i) * 16 + (l >> 4) * 4;
      LAS char* a = smem + 16384 + qq * 2048 + (cons0 + pp) * 8;
      const u32x2 p0 = tr_read(a), p1 = tr_read(a + 8192);
      *(u32x4*)(ws + WS_Z + (size_t)(q * 32 + n2) * 16384 + (w * 2 + i) * 1024 + l * 16) = u32x4{p0[0], p0[1], p1[0], p1[1]};
    }
  }
}

template <bool FH>
__global__ __launch_bounds__(512) void k2_colfft(char* __restrict__ ws) {
  __shared__ __attribute__((aligned(16))) char smem_[16384 + 16384 + 512];
  LAS char* smem = (LAS char*)smem_;
  const int tid = threadIdx.x, w = __builtin_amdgcn_readfirstlane(tid >> 6), l = tid & 63, c = l & 31, h = l >> 5;
  const int blk = blockIdx.x, j = blk & 7, k1 = blk >> 3, cons = j * 32 + k1;
#pragma unroll
  for (int i = 0; i < 2; ++i) {
    const int prod = (w * 2 + i) * 16 + (l >> 2), slot = (l & 3) ^ ((prod >> 2) & 3);
    __builtin_amdgcn_global_load_lds((const unsigned*)(ws + WS_Z + (size_t)prod * 16384 + cons * 64 + slot * 16), (LAS unsigned*)(smem + (w * 2 + i) * 1024), 16, 0, 0);
  }
  const bool nyq = (w >= 4);
  const bool active = (w < 4) || (blk == 0 && w < 6);
  const int b = (w >> 1) & 1, p = w & 1;
  const int k = nyq ? 512 : k1 + 32 * (2 * j + b);
  u32x4 W[4]; unsigned zn[16];
  u32x4 Ch[2], Sh[2], Cb[2], Sb[2]; float tc[16], ts[16];
  if (active) {
#pragma unroll
    for (int g = 0; g < 4; ++g) W[g] = *(const u32x4*)(ws + WS_WT + (size_t)(k * 2 + p) * 4096 + g * 1024 + l * 16);
    if (nyq) {
#pragma unroll
      for (int r = 0; r < 16; ++r) zn[r] = *(const unsigned*)(ws + WS_ZN + (p * 1024 + 32 * crow(r, h) + c) * 4);
    }
    float a[16], s_[16];
#pragma unroll
    for (int r = 0; r < 16; ++r) { const float an = (float)(crow(r, h) * c) * (1.f / 32); a[r] = cosr(an); s_[r] = sinr(an); }
    pack16<FH>(a, Ch); pack16<FH>(s_, Sh); pack16<false>(a, Cb); pack16<false>(s_, Sb);
#pragma unroll
    for (int r = 0; r < 16; ++r) { const float an = (float)(crow(r, h) * c) * (1.f / 1024); tc[r] = cosr(an) * (1.f / 32); ts[r] = sinr(an) * (1.f / 32); }
  }
  asm volatile("s_waitcnt vmcnt(0)" ::: "memory");
  __syncthreads();
  if (active) {
    u32x4 Ar[2], Ai[2];
    if (!nyq) {
#pragma unroll
      for (int ks = 0; ks < 2; ++ks)
#pragma unroll
        for (int t = 0; t < 2; ++t) {
          const int prod = (4 * ks + 2 * t + h) * 32 + c, sw = (c >> 2) & 3;
          const u32x2 re = *(LAS u32x2*)(smem + prod * 64 + (((2 * b) ^ sw) * 16) + p * 8);
          const u32x2 im = *(LAS u32x2*)(smem + prod * 64 + (((2 * b + 1) ^ sw) * 16) + p * 8);
          Ar[ks][2 * t] = re[0]; Ar[ks][2 * t + 1] = re[1]; Ai[ks][2 * t] = im[0]; Ai[ks][2 * t + 1] = im[1];
        }
    } else {
#pragma unroll
      for (int i = 0; i < 8; ++i) { Ar[i >> 2][i & 3] = (zn[2 * i] & 0xffffu) | (zn[2 * i + 1] << 16); Ai[i >> 2][i & 3] = (zn[2 * i] >> 16) | (zn[2 * i + 1] & 0xffff0000u); }
    }
    f32x16 Dr = {}, Di = {};
#pragma unroll
    for (int ks = 0; ks < 2; ++ks) {
      Dr = mfma_h(Ar[ks], Ch[ks], Dr); Di = mfma_h(Ai[ks], Ch[ks], Di);
      Dr = mfma_h(Ai[ks], Sh[ks], Dr); Di = mfma_h(Ar[ks], neg16(Sh[ks]), Di);
    }
    float er[16], ei[16]; u32x4 Er[2], Ei[2];
#pragma unroll
    for (int r = 0; r < 16; ++r) { er[r] = Dr[r] * tc[r] + Di[r] * ts[r]; ei[r] = Di[r] * tc[r] - Dr[r] * ts[r]; }
    pack16<true>(er, Er); pack16<true>(ei, Ei);
    Dr = f32x16{}; Di = f32x16{};
#pragma unroll
    for (int ks = 0; ks < 2; ++ks) {
      Dr = mfma_h(Ch[ks], Er[ks], Dr); Di = mfma_h(Ch[ks], Ei[ks], Di);
      Dr = mfma_h(Sh[ks], Ei[ks], Dr); Di = mfma_h(neg16(Sh[ks]), Er[ks], Di);
    }
#pragma unroll
    for (int r = 0; r < 16; ++r) { const float a = bf_lo(W[r >> 2][r & 3]), bb = bf_hi(W[r >> 2][r & 3]); er[r] = Dr[r] * a - Di[r] * bb; ei[r] = Di[r] * a + Dr[r] * bb; }
    pack16<false>(er, Er); pack16<false>(ei, Ei);
    Dr = f32x16{}; Di = f32x16{};
#pragma unroll
    for (int ks = 0; ks < 2; ++ks) {
      Dr = mfma_b(Er[ks], Cb[ks], Dr); Di = mfma_b(Ei[ks], Cb[ks], Di);
      Dr = mfma_b(Ei[ks], neg16(Sb[ks]), Dr); Di = mfma_b(Er[ks], Sb[ks], Di);
    }
#pragma unroll
    for (int r = 0; r < 16; ++r) { er[r] = Dr[r] * tc[r] - Di[r] * ts[r]; ei[r] = Di[r] * tc[r] + Dr[r] * ts[r]; }
    pack16<false>(er, Er); pack16<false>(ei, Ei);
    Dr = f32x16{}; Di = f32x16{};
#pragma unroll
    for (int ks = 0; ks < 2; ++ks) {
      Dr = mfma_b(Cb[ks], Er[ks], Dr); Di = mfma_b(Cb[ks], Ei[ks], Di);
      Dr = mfma_b(neg16(Sb[ks]), Ei[ks], Dr); Di = mfma_b(Sb[ks], Er[ks], Di);
    }
    if (!nyq) {
      const float sc = (k == 0) ? 0.5f : 1.f;
#pragma unroll
      for (int r = 0; r < 16; ++r) *(LAS unsigned*)(smem + 16384 + (r & 3) * (4096 + 64) + ((2 * (r >> 2) + h) * 32 + c) * 16 + w * 4) = pk_bf16(Dr[r] * sc, Di[r] * sc);
    } else {
#pragma unroll
      for (int r = 0; r < 16; ++r) *(f32x2*)(ws + WS_QN + (p * 1024 + 32 * crow(r, h) + c) * 8) = f32x2{Dr[r], Di[r]};
    }
  }
  __syncthreads();
#pragma unroll
  for (int i = 0; i < 2; ++i) {
    const int cc = (w * 2 + i) * 16 + (l >> 2);
    const u32x4 g = *(LAS u32x4*)(smem + 16384 + (l & 3) * (4096 + 64) + cc * 16);
    *(u32x4*)(ws + WS_Q + (size_t)cons * 16384 + (w * 2 + i) * 1024 + l * 16) = g;
  }
}

__global__ __launch_bounds__(512) void k4_final(const u16* __restrict__ x, const u16* __restrict__ b2, char* __restrict__ ws, u16* __restrict__ out) {
  __shared__ __attribute__((aligned(16))) char smem_[16384];
  LAS char* smem = (LAS char*)smem_;
  const int tid = threadIdx.x, w = __builtin_amdgcn_readfirstlane(tid >> 6), l = tid & 63, c = l & 31, h = l >> 5;
  const int blk = blockIdx.x, q = blk & 7, n2 = blk >> 3, cons = q * 32 + n2;
  const int e = w & 3, tb = w >> 2, sp = 128 * q + 32 * e + n2, row = sp + 1024 * tb;
#pragma unroll
  for (int i = 0; i < 2; ++i) {
    const int prod = (w * 2 + i) * 16 + (l >> 2), slot = (l & 3) ^ ((prod >> 2) & 3);
    __builtin_amdgcn_global_load_lds((const unsigned*)(ws + WS_Q + (size_t)prod * 16384 + cons * 64 + slot * 16), (LAS unsigned*)(smem + (w * 2 + i) * 1024), 16, 0, 0);
  }
  u32x2 xv[4], bv[4]; f32x4 p0[4], p1[4];
#pragma unroll
  for (int g = 0; g < 4; ++g) {
    const int d = 32 * c + 8 * g + 4 * h;
    xv[g] = *(const u32x2*)(x + (size_t)row * D + d); bv[g] = *(const u32x2*)(b2 + d);
    p0[g] = *(const f32x4*)(ws + WS_P + ((size_t)row * D + d) * 4); p1[g] = *(const f32x4*)(ws + WS_P + ((size_t)(S + row) * D + d) * 4);
  }
  const f32x2 na = *(const f32x2*)(ws + WS_QN + sp * 8), nb = *(const f32x2*)(ws + WS_QN + (1024 + sp) * 8);
  const float rot = (float)sp * (1.f / 2048) + 0.5f * tb;
  u32x4 Mr[4], Mi[4], C2[2], nS2[2]; float tc[16], ts[16];
#pragma unroll
  for (int st = 0; st < 4; ++st)
#pragma unroll
    for (int i = 0; i < 4; ++i) {
      const float an = (float)((2 * (2 * st + h) + (i >> 1)) * c) * (1.f / 32) + ((i & 1) ? rot : 0.f);
      const float cs = cosr(an), sn = sinr(an);
      Mr[st][i] = pk_bf16(cs, -sn); Mi[st][i] = pk_bf16(sn, cs);
    }
  {
    float a[16], b[16];
#pragma unroll
    for (int r = 0; r < 16; ++r) { const float an = (float)(crow(r, h) * c) * (1.f / 32); a[r] = cosr(an); b[r] = -sinr(an); }
    pack16<false>(a, C2); pack16<false>(b, nS2);
#pragma unroll
    for (int r = 0; r < 16; ++r) { const float an = (float)(crow(r, h) * c) * (1.f / 1024); tc[r] = cosr(an) * (1.f / 32); ts[r] = sinr(an) * (1.f / 32); }
  }
  asm volatile("s_waitcnt vmcnt(0)" ::: "memory");
  __syncthreads();
  f32x16 Dr = {}, Di = {};
#pragma unroll
  for (int st = 0; st < 4; ++st) {
    const int prod = (2 * st + h) * 32 + c;
    const u32x4 A = *(LAS u32x4*)(smem + prod * 64 + ((e ^ ((c >> 2) & 3)) * 16));
    Dr = mfma_b(A, Mr[st], Dr); Di = mfma_b(A, Mi[st], Di);
  }
  float er[16], ei[16]; u32x4 Er[2], Ei[2];
#pragma unroll
  for (int r = 0; r < 16; ++r) { er[r] = Dr[r] * tc[r] - Di[r] * ts[r]; ei[r] = Di[r] * tc[r] + Dr[r] * ts[r]; }
  pack16<false>(er, Er); pack16<false>(ei, Ei);
  const float ny = (na[0] + nb[0] * cosr(rot) - nb[1] * sinr(rot)) * (1.f / 64);
  f32x16 Y;
#pragma unroll
  for (int r = 0; r < 16; ++r) Y[r] = (r & 1) ? -ny : ny;
  Y = mfma_b(Er[0], C2[0], Y); Y = mfma_b(Ei[0], nS2[0], Y); Y = mfma_b(Er[1], C2[1], Y); Y = mfma_b(Ei[1], nS2[1], Y);
#pragma unroll
  for (int g = 0; g < 4; ++g) {
    float o[4];
#pragma unroll
    for (int i = 0; i < 4; ++i) {
      const float bb = (i & 1) ? bf_hi(bv[g][i >> 1]) : bf_lo(bv[g][i >> 1]), xx = (i & 1) ? bf_hi(xv[g][i >> 1]) : bf_lo(xv[g][i >> 1]);
      const float z = p0[g][i] + p1[g][i] + bb;
      const float gg = z * __builtin_amdgcn_rcpf(1.f + __expf(-z));
      o[i] = xx + Y[4 * g + i] * gg;
    }
    *(u32x2*)(out + (size_t)row * D + 32 * c + 8 * g + 4 * h) = u32x2{pk_bf16(o[0], o[1]), pk_bf16(o[2], o[3])};
  }
}

template <int MODE>
__global__ __launch_bounds__(256) void k_gemm(const u16* __restrict__ A, const u16* __restrict__ B, const u16* __restrict__ bias, void* __restrict__ C, int lda, int ldb, int KT, int nN) {
  __shared__ __attribute__((aligned(16))) char smem_[65536];
  LAS char* smem = (LAS char*)smem_;
  const int tid = threadIdx.x, w = __builtin_amdgcn_readfirstlane(tid >> 6), l = tid & 63, c = l & 31, h = l >> 5;
  const int bm = blockIdx.x / nN, bn = blockIdx.x % nN, kz = blockIdx.y;
  const int wm = w >> 1, wn = w & 1;
  const u16* Ab = A + (size_t)bm * 128 * lda + (size_t)kz * KT * 64;
  const u16* Bb = B + (size_t)kz * KT * 64 * ldb + bn * 128;
  auto stage = [&](int buf, int kt) {
#pragma unroll
    for (int i = 0; i < 4; ++i) {
      const int pc = w * 4 + i;
      const int ar = pc * 8 + (l >> 3), ach = (l & 7) ^ ((ar >> 1) & 7);
      __builtin_amdgcn_global_load_lds((const unsigned*)(Ab + (size_t)ar * lda + kt * 64 + ach * 8), (LAS unsigned*)(smem + buf * 32768 + pc * 1024), 16, 0, 0);
      const int br = pc * 4 + (l >> 4), bch = (l & 15) ^ (((br & 3) << 2) | ((br >> 2) & 3));
      __builtin_amdgcn_global_load_lds((const unsigned*)(Bb + (size_t)(kt * 64 + br) * ldb + bch * 8), (LAS unsigned*)(smem + buf * 32768 + 16384 + pc * 1024), 16, 0, 0);
    }
  };
  f32x16 acc[2][2] = {};
  stage(0, 0);
  for (int kt = 0; kt < KT; ++kt) {
    asm volatile("s_waitcnt vmcnt(0)" ::: "memory");
    __syncthreads();
    if (kt + 1 < KT) stage((kt + 1) & 1, kt + 1);
    LAS char* sa = smem + (kt & 1) * 32768; LAS char* sb = sa + 16384;
#pragma unroll
    for (int ks = 0; ks < 4; ++ks) {
      u32x4 a[2], b[2];
#pragma unroll
      for (int m = 0; m < 2; ++m) { const int r = wm * 64 + m * 32 + c; a[m] = *(LAS u32x4*)(sa + r * 128 + (((2 * ks + h) ^ ((r >> 1) & 7)) * 16)); }
#pragma unroll
      for (int n = 0; n < 2; ++n) {
        const int qq = (l & 15) >> 2, pp = l & 3, bl = (l >> 4) & 1;
        u32x2 t[2];
#pragma unroll
        for (int tt = 0; tt < 2; ++tt) { const int r = 16 * ks + 8 * h + 4 * tt + qq, ch = 4 * (wn * 2 + n) + 2 * bl + (pp >> 1); t[tt] = tr_read(sb + r * 256 + ((ch ^ (((r & 3) << 2) | ((r >> 2) & 3))) * 16) + 8 * (pp & 1)); }
        b[n] = u32x4{t[0][0], t[0][1], t[1][0], t[1][1]};
      }
#pragma unroll
      for (int m = 0; m < 2; ++m)
#pragma unroll
        for (int n = 0; n < 2; ++n) acc[m][n] = mfma_b(b[n], a[m], acc[m][n]);
    }
  }
#pragma unroll
  for (int m = 0; m < 2; ++m)
#pragma unroll
    for (int n = 0; n < 2; ++n)
#pragma unroll
      for (int g = 0; g < 4; ++g) {
        const int r = bm * 128 + wm * 64 + m * 32 + c, col = bn * 128 + wn * 64 + n * 32 + 8 * g + 4 * h;
        if (MODE == 0) {
          const u32x2 bv = *(const u32x2*)(bias + col);
          float o[4];
#pragma unroll
          for (int i = 0; i < 4; ++i) { const float z = fmaxf(acc[m][n][4 * g + i] + ((i & 1) ? bf_hi(bv[i >> 1]) : bf_lo(bv[i >> 1])), 0.f); o[i] = z * z; }
          *(u32x2*)((u16*)C + (size_t)r * ldb + col) = u32x2{pk_bf16(o[0], o[1]), pk_bf16(o[2], o[3])};
        } else {
          *(f32x4*)((float*)C + ((size_t)kz * S + r) * ldb + col) = f32x4{acc[m][n][4 * g], acc[m][n][4 * g + 1], acc[m][n][4 * g + 2], acc[m][n][4 * g + 3]};
        }
      }
}

extern "C" void kernel_launch(void* const* d_in, const int* in_sizes, int n_in, void* d_out, int out_size, void* d_ws, size_t ws_size, hipStream_t stream) {
  const u16* x = (const u16*)d_in[0]; const u16* wr = (const u16*)d_in[1]; const u16* wi = (const u16*)d_in[2];
  const u16* w1 = (const u16*)d_in[4]; const u16* b1 = (const u16*)d_in[5]; const u16* w2 = (const u16*)d_in[6]; const u16* b2 = (const u16*)d_in[7];
  char* ws = (char*)d_ws;
  k1_ln_rowfft<<<256, 512, 0, stream>>>(x, wr, wi, ws);
  k2_colfft<true><<<256, 512, 0, stream>>>(ws);
  k_gemm<0><<<dim3(16 * 28, 1), 256, 0, stream>>>((const u16*)(ws + WS_XN), w1, b1, ws + WS_H, D, F, D / 64, 28);
  k_gemm<1><<<dim3(16 * 8, 2), 256, 0, stream>>>((const u16*)(ws + WS_H), w2, nullptr, ws + WS_P, F, D, F / 128, 8);
  k4_final<<<256, 512, 0, stream>>>(x, b2, ws, (u16*)d_out);
}
```

```cpp
#include <hip/hip_runtime.h>

static __device__ const unsigned k_tab[6144] __attribute__((aligned(1024))) = {
0x3d000000,0x3d000000,0x3d000000,0x3d000000,0x3d000000,0x3cfffec4,0x3cfffb11,0x3cfff4e6,0x3d000000,0x3cfffb11,0x3cffec43,0x3cffd397,0x3d000000,0x3cfff4e6,0x3cffd397,0x3cff9c18,
0x3d000000,0x3cffec43,0x3cffb10f,0x3cff4e6d,0x3d000000,0x3cffe129,0x3cff84ab,0x3cfeea9d,0x3d000000,0x3cffd397,0x3cff4e6d,0x3cfe70b0,0x3d000000,0x3cffc38f,0x3cff0e58,0x3cfde0b1,
0x3d000000,0x3cffb10f,0x3cfec46d,0x3cfd3aac,0x3d000000,0x3cff9c18,0x3cfe70b0,0x3cfc7eb0,0x3d000000,0x3cff84ab,0x3cfe1324,0x3cfbaccd,0x3d000000,0x3cff6ac7,0x3cfdabcc,0x3cfac516,
0x3d000000,0x3cff4e6d,0x3cfd3aac,0x3cf9c79d,0x3d000000,0x3cff2f9d,0x3cfcbfc9,0x3cf8b47b,0x3d000000,0x3cff0e58,0x3cfc3b28,0x3cf78bc5,0x3d000000,0x3cfeea9d,0x3cfbaccd,0x3cf64d97,
0x3d000000,0x3cfec46d,0x3cfb14be,0x3cf4fa0b,0x3d000000,0x3cfe9bc9,0x3cfa7302,0x3cf3913f,0x3d000000,0x3cfe70b0,0x3cf9c79d,0x3cf21352,0x3d000000,0x3cfe4323,0x3cf91298,0x3cf08066,
0x3d000000,0x3cfe1324,0x3cf853f8,0x3ceed89e,0x3d000000,0x3cfde0b1,0x3cf78bc5,0x3ced1c1d,0x3d000000,0x3cfdabcc,0x3cf6ba07,0x3ceb4b0c,0x3d000000,0x3cfd7474,0x3cf5dec6,0x3ce96591,
0x3d000000,0x3cfd3aac,0x3cf4fa0b,0x3ce76bd8,0x3d000000,0x3cfcfe73,0x3cf40bdd,0x3ce55e0b,0x3d000000,0x3cfcbfc9,0x3cf31447,0x3ce33c5a,0x3d000000,0x3cfc7eb0,0x3cf21352,0x3ce106f2,
0x3d000000,0x3cfc3b28,0x3cf10908,0x3cdebe05,0x3d000000,0x3cfbf531,0x3ceff573,0x3cdc61c7,0x3d000000,0x3cfbaccd,0x3ceed89e,0x3cd9f26a,0x3d000000,0x3cfb61fc,0x3cedb293,0x3cd77026,
0x3d000000,0x3d000000,0x3d000000,0x3d000000,0x3cffec43,0x3cffe129,0x3cffd397,0x3cffc38f,0x3cffb10f,0x3cff84ab,0x3cff4e6d,0x3cff0e58,0x3cff4e6d,0x3cfeea9d,0x3cfe70b0,0x3cfde0b1,
0x3cfec46d,0x3cfe1324,0x3cfd3aac,0x3cfc3b28,0x3cfe1324,0x3cfcfe73,0x3cfbaccd,0x3cfa1e84,0x3cfd3aac,0x3cfbaccd,0x3cf9c79d,0x3cf78bc5,0x3cfc3b28,0x3cfa1e84,0x3cf78bc5,0x3cf48422,
0x3cfb14be,0x3cf853f8,0x3cf4fa0b,0x3cf10908,0x3cf9c79d,0x3cf64d97,0x3cf21352,0x3ced1c1d,0x3cf853f8,0x3cf40bdd,0x3ceed89e,0x3ce8bf3c,0x3cf6ba07,0x3cf18f57,0x3ceb4b0c,0x3ce3f473,
0x3cf4fa0b,0x3ceed89e,0x3ce76bd8,0x3cdebe05,0x3cf31447,0x3cebe858,0x3ce33c5a,0x3cd91e6a,0x3cf10908,0x3ce8bf3c,0x3cdebe05,0x3cd31849,0x3ceed89e,0x3ce55e0b,0x3cd9f26a,0x3cccae79,
0x3cec835e,0x3ce1c598,0x3cd4db31,0x3cc5e403,0x3cea09a7,0x3cddf6be,0x3ccf7a1f,0x3cbebc1b,0x3ce76bd8,0x3cd9f26a,0x3cc9d112,0x3cb73a23,0x3ce4aa59,0x3cd5b993,0x3cc3e200,0x3caf61a5,
0x3ce1c598,0x3cd14d3d,0x3cbdaef9,0x3ca73656,0x3cdebe05,0x3cccae79,0x3cb73a23,0x3c9ebc12,0x3cdb941a,0x3cc7de65,0x3cb085bb,0x3c95f6d9,0x3cd84853,0x3cc2de29,0x3ca99415,0x3c8cead0,
0x3cd4db31,0x3cbdaef9,0x3ca26799,0x3c839c3d,0x3cd14d3d,0x3cb85216,0x3c9b02c6,0x3c741f07,0x3ccd9f02,0x3cb2c8c9,0x3c93682a,0x3c60924f,0x3cc9d112,0x3cad1469,0x3c8b9a6b,0x3c4c9b8b,
0x3cc5e403,0x3ca73656,0x3c839c3d,0x3c38442a,0x3cc1d870,0x3ca12ff9,0x3c76e0cb,0x3c2395c5,0x3cbdaef9,0x3c9b02c6,0x3c663375,0x3c0e9a22,0x3cb96842,0x3c94b039,0x3c553641,0x3bf2b651,
0x3d000000,0x3d000000,0x3d000000,0x3d000000,0x3cffb10f,0x3cff9c18,0x3cff84ab,0x3cff6ac7,0x3cfec46d,0x3cfe70b0,0x3cfe1324,0x3cfdabcc,0x3cfd3aac,0x3cfc7eb0,0x3cfbaccd,0x3cfac516,
0x3cfb14be,0x3cf9c79d,0x3cf853f8,0x3cf6ba07,0x3cf853f8,0x3cf64d97,0x3cf40bdd,0x3cf18f57,0x3cf4fa0b,0x3cf21352,0x3ceed89e,0x3ceb4b0c,0x3cf10908,0x3ced1c1d,0x3ce8bf3c,0x3ce3f473,
0x3cec835e,0x3ce76bd8,0x3ce1c598,0x3cdb941a,0x3ce76bd8,0x3ce106f2,0x3cd9f26a,0x3cd233c6,0x3ce1c598,0x3cd9f26a,0x3cd14d3d,0x3cc7de65,0x3cdb941a,0x3cd233c6,0x3cc7de65,0x3cbca003,
0x3cd4db31,0x3cc9d112,0x3cbdaef9,0x3cb085bb,0x3ccd9f02,0x3cc0d0da,0x3cb2c8c9,0x3ca39da9,0x3cc5e403,0x3cb73a23,0x3ca73656,0x3c95f6d9,0x3cbdaef9,0x3cad1469,0x3c9b02c6,0x3c87a136,
0x3cb504f3,0x3ca26799,0x3c8e39da,0x3c715aea,0x3cabeb4a,0x3c973c07,0x3c80e7e4,0x3c525a09,0x3ca26799,0x3c8b9a6b,0x3c663375,0x3c3263ef,0x3c987fc0,0x3c7f17b2,0x3c49b953,0x3c119ddd,
0x3c8e39da,0x3c663375,0x3c2c7cd4,0x3be05c13,0x3c839c3d,0x3c4c9b8b,0x3c0e9a22,0x3b9c76de,0x3c715aea,0x3c3263ef,0x3be05c13,0x3b2fb680,0x3c5ae880,0x3c17a117,0x3ba2abb6,0x3a16c9b6,
0x3c43ef15,0x3bf8cfcc,0x3b48bd36,0xbac8fb30,0x3c2c7cd4,0x3bc19b37,0x3a96c32c,0xbb6e3876,0x3c14a031,0x3b89cf86,0xba490ab0,0xbbbb6ecf,0x3bf8cfcc,0x3b23308c,0xbb2fb680,0xbbfee6e1,
0x3bc7c5c2,0x3a490ab0,0xbb964083,0xbc209ae5,0x3b964083,0xba7b49ba,0xbbd41501,0xbc41071e,0x3b48bd36,0xbb2fb680,0xbc088e93,0xbc60924f,0x3ac8fb30,0xbb9008b7,0xbc268f12,0xbc7f17b2,
0x3d000000,0x3d000000,0x3d000000,0x3d000000,0x3cff4e6d,0x3cff2f9d,0x3cff0e58,0x3cfeea9d,0x3cfd3aac,0x3cfcbfc9,0x3cfc3b28,0x3cfbaccd,0x3cf9c79d,0x3cf8b47b,0x3cf78bc5,0x3cf64d97,
0x3cf4fa0b,0x3cf31447,0x3cf10908,0x3ceed89e,0x3ceed89e,0x3cebe858,0x3ce8bf3c,0x3ce55e0b,0x3ce76bd8,0x3ce33c5a,0x3cdebe05,0x3cd9f26a,0x3cdebe05,0x3cd91e6a,0x3cd31849,0x3cccae79,
0x3cd4db31,0x3ccd9f02,0x3cc5e403,0x3cbdaef9,0x3cc9d112,0x3cc0d0da,0x3cb73a23,0x3cad1469,0x3cbdaef9,0x3cb2c8c9,0x3ca73656,0x3c9b02c6,0x3cb085bb,0x3ca39da9,0x3c95f6d9,0x3c87a136,
0x3ca26799,0x3c93682a,0x3c839c3d,0x3c663375,0x3c93682a,0x3c8242b1,0x3c60924f,0x3c3b31a0,0x3c839c3d,0x3c60924f,0x3c38442a,0x3c0e9a22,0x3c663375,0x3c3b31a0,0x3c0e9a22,0x3bc19b37,
0x3c43ef15,0x3c14a031,0x3bc7c5c2,0x3b48bd36,0x3c209ae5,0x3bda3997,0x3b61bc2e,0x39490f88,0x3bf8cfcc,0x3b89cf86,0x3a490ab0,0xbb2fb680,0x3baf10a2,0x3ae21469,0xbafb2b74,0xbbb54098,
0x3b48bd36,0xba490ab0,0xbb964083,0xbc088e93,0x3a490ab0,0xbb553db9,0xbbec9a7f,0xbc3554ec,0xbac8fb30,0xbbbb6ecf,0xbc209ae5,0xbc60924f,0xbb7ab273,0xbc0586ce,0xbc49b953,0xbc84f484,
0xbbc7c5c2,0xbc2c7cd4,0xbc715aea,0xbc987fc0,0xbc088e93,0xbc525a09,0xbc8b9a6b,0xbcaac082,0xbc2c7cd4,0xbc76e0cb,0xbc9d7fd1,0xbcbb8f3b,0xbc4f7bca,0xbc8cead0,0xbcae3bde,0xbccac77f,
0xbc715aea,0xbc9d7fd1,0xbcbdaef9,0xbcd84853,0xbc88f59b,0xbcad1469,0xbccbbbf8,0xbce3f473,0xbc987fc0,0xbcbb8f3b,0xbcd84853,0xbcedb293,0xbca73656,0xbcc8d8b3,0xbce33c5a,0xbcf56d97,
0x3d000000,0x3d000000,0x3d000000,0x3d000000,0x3cfec46d,0x3cfe9bc9,0x3cfe70b0,0x3cfe4323,0x3cfb14be,0x3cfa7302,0x3cf9c79d,0x3cf91298,0x3cf4fa0b,0x3cf3913f,0x3cf21352,0x3cf08066,
0x3cec835e,0x3cea09a7,0x3ce76bd8,0x3ce4aa59,0x3ce1c598,0x3cddf6be,0x3cd9f26a,0x3cd5b993,0x3cd4db31,0x3ccf7a1f,0x3cc9d112,0x3cc3e200,0x3cc5e403,0x3cbebc1b,0x3cb73a23,0x3caf61a5,
0x3cb504f3,0x3cabeb4a,0x3ca26799,0x3c987fc0,0x3ca26799,0x3c973c07,0x3c8b9a6b,0x3c7f17b2,0x3c8e39da,0x3c80e7e4,0x3c663375,0x3c49b953,0x3c715aea,0x3c525a09,0x3c3263ef,0x3c119ddd,
0x3c43ef15,0x3c209ae5,0x3bf8cfcc,0x3baf10a2,0x3c14a031,0x3bda3997,0x3b89cf86,0x3ae21469,0x3bc7c5c2,0x3b61bc2e,0x3a490ab0,0xbafb2b74,0x3b48bd36,0x39490f88,0xbb2fb680,0xbbb54098,
0x0,0xbb48bd36,0xbbc7c5c2,0xbc14a031,0xbb48bd36,0xbbcdee60,0xbc1aa086,0xbc4c9b8b,0xbbc7c5c2,0xbc1aa086,0xbc4f7bca,0xbc80e7e4,0xbc14a031,0xbc4c9b8b,0xbc80e7e4,0xbc99c200,
0xbc43ef15,0xbc7c5d27,0xbc987fc0,0xbcb085bb,0xbc715aea,0xbc94b039,0xbcae3bde,0xbcc4e3f5,0xbc8e39da,0xbca99415,0xbcc1d870,0xbcd695e5,0xbca26799,0xbcbca003,0xbcd31849,0xbce55e0b,
0xbcb504f3,0xbccd9f02,0xbce1c598,0xbcf10908,0xbcc5e403,0xbcdc61c7,0xbcedb293,0xbcf96e4e,0xbcd4db31,0xbce8bf3c,0xbcf6ba07,0xbcfe70b0,0xbce1c598,0xbcf294f8,0xbcfcbfc9,0xbcfffec4,
0xbcec835e,0xbcf9c79d,0xbcffb10f,0xbcfe1324,0xbcf4fa0b,0xbcfe4323,0xbcff84ab,0xbcf8b47b,0xbcfb14be,0xbcfffb11,0xbcfc3b28,0xbceff573,0xbcfec46d,0xbcfeea9d,0xbcf5dec6,0xbce3f473,
0x3d000000,0x3d000000,0x3d000000,0x3d000000,0x3cfe1324,0x3cfde0b1,0x3cfdabcc,0x3cfd7474,0x3cf853f8,0x3cf78bc5,0x3cf6ba07,0x3cf5dec6,0x3ceed89e,0x3ced1c1d,0x3ceb4b0c,0x3ce96591,
0x3ce1c598,0x3cdebe05,0x3cdb941a,0x3cd84853,0x3cd14d3d,0x3cccae79,0x3cc7de65,0x3cc2de29,0x3cbdaef9,0x3cb73a23,0x3cb085bb,0x3ca99415,0x3ca73656,0x3c9ebc12,0x3c95f6d9,0x3c8cead0,
0x3c8e39da,0x3c839c3d,0x3c715aea,0x3c5ae880,0x3c663375,0x3c4c9b8b,0x3c3263ef,0x3c17a117,0x3c2c7cd4,0x3c0e9a22,0x3be05c13,0x3ba2abb6,0x3be05c13,0x3b9c76de,0x3b2fb680,0x3a16c9b6,
0x3b48bd36,0x3a490ab0,0xbac8fb30,0xbb7ab273,0xba490ab0,0xbb553db9,0xbbbb6ecf,0xbc0586ce,0xbb964083,0xbbec9a7f,0xbc209ae5,0xbc49b953,0xbc088e93,0xbc3554ec,0xbc60924f,0xbc84f484,
0xbc43ef15,0xbc715aea,0xbc8e39da,0xbca26799,0xbc7c5d27,0xbc94b039,0xbca99415,0xbcbca003,0xbc987fc0,0xbcae3bde,0xbcc1d870,0xbcd31849,0xbcb085bb,0xbcc4e3f5,0xbcd695e5,0xbce55e0b,
0xbcc5e403,0xbcd84853,0xbce76bd8,0xbcf31447,0xbcd84853,0xbce816a8,0xbcf40bdd,0xbcfbf531,0xbce76bd8,0xbcf40bdd,0xbcfc3b28,0xbcffd397,0xbcf31447,0xbcfbf531,0xbcffd397,0xbcfe9bc9,
0xbcfb14be,0xbcffb10f,0xbcfec46d,0xbcf853f8,0xbcff4e6d,0xbcff2f9d,0xbcf91298,0xbced1c1d,0xbcffb10f,0xbcfa7302,0xbceed89e,0xbcdd2d53,0xbcfc3b28,0xbcf18f57,0xbce04621,0xbcc8d8b3,
0xbcf4fa0b,0xbce4aa59,0xbccd9f02,0xbcb085bb,0xbcea09a7,0xbcd3fac3,0xbcb73a23,0xbc94b039,0xbcdb941a,0xbcbfc767,0xbc9d7fd1,0xbc6bcbbb,0xbcc9d112,0xbca86605,0xbc80e7e4,0xbc2986c4,
0x3d000000,0x3d000000,0x3d000000,0x3d000000,0x3cfd3aac,0x3cfcfe73,0x3cfcbfc9,0x3cfc7eb0,0x3cf4fa0b,0x3cf40bdd,0x3cf31447,0x3cf21352,0x3ce76bd8,0x3ce55e0b,0x3ce33c5a,0x3ce106f2,
0x3cd4db31,0x3cd14d3d,0x3ccd9f02,0x3cc9d112,0x3cbdaef9,0x3cb85216,0x3cb2c8c9,0x3cad1469,0x3ca26799,0x3c9b02c6,0x3c93682a,0x3c8b9a6b,0x3c839c3d,0x3c741f07,0x3c60924f,0x3c4c9b8b,
0x3c43ef15,0x3c2c7cd4,0x3c14a031,0x3bf8cfcc,0x3bf8cfcc,0x3bc19b37,0x3b89cf86,0x3b23308c,0x3b48bd36,0x3a96c32c,0xba490ab0,0xbb2fb680,0xbac8fb30,0xbb6e3876,0xbbbb6ecf,0xbbfee6e1,
0xbbc7c5c2,0xbc088e93,0xbc2c7cd4,0xbc4f7bca,0xbc2c7cd4,0xbc525a09,0xbc76e0cb,0xbc8cead0,0xbc715aea,0xbc8b9a6b,0xbc9d7fd1,0xbcae3bde,0xbc987fc0,0xbcaac082,0xbcbb8f3b,0xbccac77f,
0xbcb504f3,0xbcc5e403,0xbcd4db31,0xbce1c598,0xbccd9f02,0xbcdc61c7,0xbce8bf3c,0xbcf294f8,0xbce1c598,0xbcedb293,0xbcf6ba07,0xbcfcbfc9,0xbcf10908,0xbcf96e4e,0xbcfe70b0,0xbcfffec4,
0xbcfb14be,0xbcff4e6d,0xbcffb10f,0xbcfc3b28,0xbcffb10f,0xbcff2f9d,0xbcfa7302,0xbcf18f57,0xbcfec46d,0xbcf91298,0xbceed89e,0xbce04621,0xbcf853f8,0xbced1c1d,0xbcdd2d53,0xbcc8d8b3,
0xbcec835e,0xbcdb941a,0xbcc5e403,0xbcabeb4a,0xbcdb941a,0xbcc4e3f5,0xbca99415,0xbc8a48ad,0xbcc5e403,0xbca99415,0xbc88f59b,0xbc49b953,0xbcabeb4a,0xbc8a48ad,0xbc49b953,0xbbf2b651,
0xbc8e39da,0xbc4f7bca,0xbbf8cfcc,0xbb16a905,0xbc5ae880,0xbc0586ce,0xbb2fb680,0x3b3c3ac3,0xbc14a031,0xbb61bc2e,0x3b16a905,0x3c027dc0,0xbb964083,0x3aafe007,0x3bec9a7f,0x3c525a09,
0x3d000000,0x3d000000,0x3d000000,0x3d000000,0x3cfc3b28,0x3cfbf531,0x3cfbaccd,0x3cfb61fc,0x3cf10908,0x3ceff573,0x3ceed89e,0x3cedb293,0x3cdebe05,0x3cdc61c7,0x3cd9f26a,0x3cd77026,
0x3cc5e403,0x3cc1d870,0x3cbdaef9,0x3cb96842,0x3ca73656,0x3ca12ff9,0x3c9b02c6,0x3c94b039,0x3c839c3d,0x3c76e0cb,0x3c663375,0x3c553641,0x3c38442a,0x3c2395c5,0x3c0e9a22,0x3bf2b651,
0x3bc7c5c2,0x3b964083,0x3b48bd36,0x3ac8fb30,0x3a490ab0,0xba7b49ba,0xbb2fb680,0xbb9008b7,0xbb964083,0xbbd41501,0xbc088e93,0xbc268f12,0xbc209ae5,0xbc41071e,0xbc60924f,0xbc7f17b2,
0xbc715aea,0xbc88f59b,0xbc987fc0,0xbca73656,0xbc9d7fd1,0xbcad1469,0xbcbb8f3b,0xbcc8d8b3,0xbcbdaef9,0xbccbbbf8,0xbcd84853,0xbce33c5a,0xbcd84853,0xbce3f473,0xbcedb293,0xbcf56d97,
0xbcec835e,0xbcf4fa0b,0xbcfb14be,0xbcfec46d,0xbcf9c79d,0xbcfe4323,0xbcfffb11,0xbcfeea9d,0xbcffb10f,0xbcff84ab,0xbcfc3b28,0xbcf5dec6,0xbcfe1324,0xbcf8b47b,0xbceff573,0xbce3f473,
0xbcf4fa0b,0xbcea09a7,0xbcdb941a,0xbcc9d112,0xbce4aa59,0xbcd3fac3,0xbcbfc767,0xbca86605,0xbccd9f02,0xbcb73a23,0xbc9d7fd1,0xbc80e7e4,0xbcb085bb,0xbc94b039,0xbc6bcbbb,0xbc2986c4,
0xbc8e39da,0xbc5ae880,0xbc14a031,0xbb964083,0xbc4f7bca,0xbc0586ce,0xbb61bc2e,0x3aafe007,0xbbf8cfcc,0xbb2fb680,0x3b16a905,0x3bec9a7f,0xbb16a905,0x3b3c3ac3,0x3c027dc0,0x3c525a09,
0x3b48bd36,0x3c088e93,0x3c5ae880,0x3c93682a,0x3c088e93,0x3c5dbe79,0x3c95f6d9,0x3cb85216,0x3c5ae880,0x3c95f6d9,0x3cb96842,0x3cd695e5,0x3c93682a,0x3cb85216,0x3cd695e5,0x3ced1c1d,
0x0,0x0,0x0,0x0,0x0,0x39490f88,0x39c90e90,0x3a16c9b6,0x0,0x39c90e90,0x3a490ab0,0x3a96c32c,0x0,0x3a16c9b6,0x3a96c32c,0x3ae21469,
0x0,0x3a490ab0,0x3ac8fb30,0x3b16a905,0x0,0x3a7b49ba,0x3afb2b74,0x3b3c3ac3,0x0,0x3a96c32c,0x3b16a905,0x3b61bc2e,0x0,0x3aafe007,0x3b2fb680,0x3b839502,
0x0,0x3ac8fb30,0x3b48bd36,0x3b964083,0x0,0x3ae21469,0x3b61bc2e,0x3ba8defc,0x0,0x3afb2b74,0x3b7ab273,0x3bbb6ecf,0x0,0x3b0a200a,0x3b89cf86,0x3bcdee60,
0x0,0x3b16a905,0x3b964083,0x3be05c13,0x0,0x3b23308c,0x3ba2abb6,0x3bf2b651,0x0,0x3b2fb680,0x3baf10a2,0x3c027dc0,0x0,0x3b3c3ac3,0x3bbb6ecf,0x3c0b9507,
0x0,0x3b48bd36,0x3bc7c5c2,0x3c14a031,0x0,0x3b553db9,0x3bd41501,0x3c1d9e78,0x0,0x3b61bc2e,0x3be05c13,0x3c268f12,0x0,0x3b6e3876,0x3bec9a7f,0x3c2f713a,
0x0,0x3b7ab273,0x3bf8cfcc,0x3c38442a,0x0,0x3b839502,0x3c027dc0,0x3c41071e,0x0,0x3b89cf86,0x3c088e93,0x3c49b953,0x0,0x3b9008b7,0x3c0e9a22,0x3c525a09,
0x0,0x3b964083,0x3c14a031,0x3c5ae880,0x0,0x3b9c76de,0x3c1aa086,0x3c6363fa,0x0,0x3ba2abb6,0x3c209ae5,0x3c6bcbbb,0x0,0x3ba8defc,0x3c268f12,0x3c741f07,
0x0,0x3baf10a2,0x3c2c7cd4,0x3c7c5d27,0x0,0x3bb54098,0x3c3263ef,0x3c8242b1,0x0,0x3bbb6ecf,0x3c38442a,0x3c864b82,0x0,0x3bc19b37,0x3c3e1d4a,0x3c8a48ad,
0x0,0x0,0x0,0x0,0x3a490ab0,0x3a7b49ba,0x3a96c32c,0x3aafe007,0x3ac8fb30,0x3afb2b74,0x3b16a905,0x3b2fb680,0x3b16a905,0x3b3c3ac3,0x3b61bc2e,0x3b839502,
0x3b48bd36,0x3b7ab273,0x3b964083,0x3baf10a2,0x3b7ab273,0x3b9c76de,0x3bbb6ecf,0x3bda3997,0x3b964083,0x3bbb6ecf,0x3be05c13,0x3c027dc0,0x3baf10a2,0x3bda3997,0x3c027dc0,0x3c17a117,
0x3bc7c5c2,0x3bf8cfcc,0x3c14a031,0x3c2c7cd4,0x3be05c13,0x3c0b9507,0x3c268f12,0x3c41071e,0x3bf8cfcc,0x3c1aa086,0x3c38442a,0x3c553641,0x3c088e93,0x3c2986c4,0x3c49b953,0x3c6900b7,
0x3c14a031,0x3c38442a,0x3c5ae880,0x3c7c5d27,0x3c209ae5,0x3c46d529,0x3c6bcbbb,0x3c87a136,0x3c2c7cd4,0x3c553641,0x3c7c5d27,0x3c90d3cd,0x3c38442a,0x3c6363fa,0x3c864b82,0x3c99c200,
0x3c43ef15,0x3c715aea,0x3c8e39da,0x3ca26799,0x3c4f7bca,0x3c7f17b2,0x3c95f6d9,0x3caac082,0x3c5ae880,0x3c864b82,0x3c9d7fd1,0x3cb2c8c9,0x3c663375,0x3c8cead0,0x3ca4d225,0x3cba7ca4,
0x3c715aea,0x3c93682a,0x3cabeb4a,0x3cc1d870,0x3c7c5d27,0x3c99c200,0x3cb2c8c9,0x3cc8d8b3,0x3c839c3d,0x3c9ff6cb,0x3cb96842,0x3ccf7a1f,0x3c88f59b,0x3ca6050a,0x3cbfc767,0x3cd5b993,
0x3c8e39da,0x3cabeb4a,0x3cc5e403,0x3cdb941a,0x3c93682a,0x3cb1a81d,0x3ccbbbf8,0x3ce106f2,0x3c987fc0,0x3cb73a23,0x3cd14d3d,0x3ce60f88,0x3c9d7fd1,0x3cbca003,0x3cd695e5,0x3ceaab7b,
0x3ca26799,0x3cc1d870,0x3cdb941a,0x3ceed89e,0x3ca73656,0x3cc6e22a,0x3ce04621,0x3cf294f8,0x3cabeb4a,0x3ccbbbf8,0x3ce4aa59,0x3cf5dec6,0x3cb085bb,0x3cd064af,0x3ce8bf3c,0x3cf8b47b,
0x0,0x0,0x0,0x0,0x3ac8fb30,0x3ae21469,0x3afb2b74,0x3b0a200a,0x3b48bd36,0x3b61bc2e,0x3b7ab273,0x3b89cf86,0x3b964083,0x3ba8defc,0x3bbb6ecf,0x3bcdee60,
0x3bc7c5c2,0x3be05c13,0x3bf8cfcc,0x3c088e93,0x3bf8cfcc,0x3c0b9507,0x3c1aa086,0x3c2986c4,0x3c14a031,0x3c268f12,0x3c38442a,0x3c49b953,0x3c2c7cd4,0x3c41071e,0x3c553641,0x3c6900b7,
0x3c43ef15,0x3c5ae880,0x3c715aea,0x3c839c3d,0x3c5ae880,0x3c741f07,0x3c864b82,0x3c921eb0,0x3c715aea,0x3c864b82,0x3c93682a,0x3c9ff6cb,0x3c839c3d,0x3c921eb0,0x3c9ff6cb,0x3cad1469,
0x3c8e39da,0x3c9d7fd1,0x3cabeb4a,0x3cb96842,0x3c987fc0,0x3ca86605,0x3cb73a23,0x3cc4e3f5,0x3ca26799,0x3cb2c8c9,0x3cc1d870,0x3ccf7a1f,0x3cabeb4a,0x3cbca003,0x3ccbbbf8,0x3cd91e6a,
0x3cb504f3,0x3cc5e403,0x3cd4db31,0x3ce1c598,0x3cbdaef9,0x3cce8d90,0x3cdd2d53,0x3ce96591,0x3cc5e403,0x3cd695e5,0x3ce4aa59,0x3ceff573,0x3ccd9f02,0x3cddf6be,0x3ceb4b0c,0x3cf56d97,
0x3cd4db31,0x3ce4aa59,0x3cf10908,0x3cf9c79d,0x3cdb941a,0x3ceaab7b,0x3cf5dec6,0x3cfcfe73,0x3ce1c598,0x3ceff573,0x3cf9c79d,0x3cff0e58,0x3ce76bd8,0x3cf48422,0x3cfcbfc9,0x3cfff4e6,
0x3cec835e,0x3cf853f8,0x3cfec46d,0x3cffb10f,0x3cf10908,0x3cfb61fc,0x3cffd397,0x3cfe4323,0x3cf4fa0b,0x3cfdabcc,0x3cffec43,0x3cfbaccd,0x3cf853f8,0x3cff2f9d,0x3cff0e58,0x3cf7f110,
0x3cfb14be,0x3cffec43,0x3cfd3aac,0x3cf31447,0x3cfd3aac,0x3cffe129,0x3cfa7302,0x3ced1c1d,0x3cfec46d,0x3cff0e58,0x3cf6ba07,0x3ce60f88,0x3cffb10f,0x3cfd7474,0x3cf21352,0x3cddf6be,
0x0,0x0,0x0,0x0,0x3b16a905,0x3b23308c,0x3b2fb680,0x3b3c3ac3,0x3b964083,0x3ba2abb6,0x3baf10a2,0x3bbb6ecf,0x3be05c13,0x3bf2b651,0x3c027dc0,0x3c0b9507,
0x3c14a031,0x3c209ae5,0x3c2c7cd4,0x3c38442a,0x3c38442a,0x3c46d529,0x3c553641,0x3c6363fa,0x3c5ae880,0x3c6bcbbb,0x3c7c5d27,0x3c864b82,0x3c7c5d27,0x3c87a136,0x3c90d3cd,0x3c99c200,
0x3c8e39da,0x3c987fc0,0x3ca26799,0x3cabeb4a,0x3c9d7fd1,0x3ca86605,0x3cb2c8c9,0x3cbca003,0x3cabeb4a,0x3cb73a23,0x3cc1d870,0x3ccbbbf8,0x3cb96842,0x3cc4e3f5,0x3ccf7a1f,0x3cd91e6a,
0x3cc5e403,0x3cd14d3d,0x3cdb941a,0x3ce4aa59,0x3cd14d3d,0x3cdc61c7,0x3ce60f88,0x3cee46be,0x3cdb941a,0x3ce60f88,0x3ceed89e,0x3cf5dec6,0x3ce4aa59,0x3cee46be,0x3cf5dec6,0x3cfb61fc,
0x3cec835e,0x3cf4fa0b,0x3cfb14be,0x3cfec46d,0x3cf31447,0x3cfa1e84,0x3cfe70b0,0x3cfffec4,0x3cf853f8,0x3cfdabcc,0x3cffec43,0x3cff0e58,0x3cfc3b28,0x3cff9c18,0x3cff84ab,0x3cfbf531,
0x3cfec46d,0x3cffec43,0x3cfd3aac,0x3cf6ba07,0x3cffec43,0x3cfe9bc9,0x3cf91298,0x3cef6830,0x3cffb10f,0x3cfbaccd,0x3cf31447,0x3ce60f88,0x3cfe1324,0x3cf72417,0x3ceb4b0c,0x3cdac450,
0x3cfb14be,0x3cf10908,0x3ce1c598,0x3ccd9f02,0x3cf6ba07,0x3ce96591,0x3cd695e5,0x3cbebc1b,0x3cf10908,0x3ce04621,0x3cc9d112,0x3cae3bde,0x3cea09a7,0x3cd5b993,0x3cbb8f3b,0x3c9c420c,
0x3ce1c598,0x3cc9d112,0x3cabeb4a,0x3c88f59b,0x3cd84853,0x3cbca003,0x3c9b02c6,0x3c6900b7,0x3ccd9f02,0x3cae3bde,0x3c88f59b,0x3c3e1d4a,0x3cc1d870,0x3c9ebc12,0x3c6bcbbb,0x3c119ddd,
0x0,0x0,0x0,0x0,0x3b48bd36,0x3b553db9,0x3b61bc2e,0x3b6e3876,0x3bc7c5c2,0x3bd41501,0x3be05c13,0x3bec9a7f,0x3c14a031,0x3c1d9e78,0x3c268f12,0x3c2f713a,
0x3c43ef15,0x3c4f7bca,0x3c5ae880,0x3c663375,0x3c715aea,0x3c7f17b2,0x3c864b82,0x3c8cead0,0x3c8e39da,0x3c95f6d9,0x3c9d7fd1,0x3ca4d225,0x3ca26799,0x3caac082,0x3cb2c8c9,0x3cba7ca4,
0x3cb504f3,0x3cbdaef9,0x3cc5e403,0x3ccd9f02,0x3cc5e403,0x3cce8d90,0x3cd695e5,0x3cddf6be,0x3cd4db31,0x3cdd2d53,0x3ce4aa59,0x3ceb4b0c,0x3ce1c598,0x3ce96591,0x3ceff573,0x3cf56d97,
0x3cec835e,0x3cf31447,0x3cf853f8,0x3cfc3b28,0x3cf4fa0b,0x3cfa1e84,0x3cfdabcc,0x3cff9c18,0x3cfb14be,0x3cfe70b0,0x3cffec43,0x3cff84ab,0x3cfec46d,0x3cfffec4,0x3cff0e58,0x3cfbf531,
0x3d000000,0x3cfec46d,0x3cfb14be,0x3cf4fa0b,0x3cfec46d,0x3cfac516,0x3cf40bdd,0x3ceaab7b,0x3cfb14be,0x3cf40bdd,0x3cea09a7,0x3cdd2d53,0x3cf4fa0b,0x3ceaab7b,0x3cdd2d53,0x3cccae79,
0x3cec835e,0x3cdebe05,0x3ccd9f02,0x3cb96842,0x3ce1c598,0x3cd064af,0x3cbb8f3b,0x3ca39da9,0x3cd4db31,0x3cbfc767,0x3ca73656,0x3c8b9a6b,0x3cc5e403,0x3cad1469,0x3c90d3cd,0x3c6363fa,
0x3cb504f3,0x3c987fc0,0x3c715aea,0x3c2c7cd4,0x3ca26799,0x3c8242b1,0x3c3e1d4a,0x3be67c66,0x3c8e39da,0x3c553641,0x3c088e93,0x3b61bc2e,0x3c715aea,0x3c2395c5,0x3ba2abb6,0xb9490f88,
0x3c43ef15,0x3be05c13,0x3ac8fb30,0xbb7ab273,0x3c14a031,0x3b6e3876,0xbafb2b74,0xbbf2b651,0x3bc7c5c2,0x39c90e90,0xbbaf10a2,0xbc3263ef,0x3b48bd36,0xbb3c3ac3,0xbc0e9a22,0xbc6900b7,
0x0,0x0,0x0,0x0,0x3b7ab273,0x3b839502,0x3b89cf86,0x3b9008b7,0x3bf8cfcc,0x3c027dc0,0x3c088e93,0x3c0e9a22,0x3c38442a,0x3c41071e,0x3c49b953,0x3c525a09,
0x3c715aea,0x3c7c5d27,0x3c839c3d,0x3c88f59b,0x3c93682a,0x3c99c200,0x3c9ff6cb,0x3ca6050a,0x3cabeb4a,0x3cb2c8c9,0x3cb96842,0x3cbfc767,0x3cc1d870,0x3cc8d8b3,0x3ccf7a1f,0x3cd5b993,
0x3cd4db31,0x3cdb941a,0x3ce1c598,0x3ce76bd8,0x3ce4aa59,0x3ceaab7b,0x3ceff573,0x3cf48422,0x3cf10908,0x3cf5dec6,0x3cf9c79d,0x3cfcbfc9,0x3cf9c79d,0x3cfcfe73,0x3cff0e58,0x3cfff4e6,
0x3cfec46d,0x3cffec43,0x3cffb10f,0x3cfe1324,0x3cffec43,0x3cfe9bc9,0x3cfbaccd,0x3cf72417,0x3cfd3aac,0x3cf91298,0x3cf31447,0x3ceb4b0c,0x3cf6ba07,0x3cef6830,0x3ce60f88,0x3cdac450,
0x3cec835e,0x3ce1c598,0x3cd4db31,0x3cc5e403,0x3cdebe05,0x3cd064af,0x3cbfc767,0x3cad1469,0x3ccd9f02,0x3cbb8f3b,0x3ca73656,0x3c90d3cd,0x3cb96842,0x3ca39da9,0x3c8b9a6b,0x3c6363fa,
0x3ca26799,0x3c88f59b,0x3c5ae880,0x3c209ae5,0x3c88f59b,0x3c58106b,0x3c1aa086,0x3bb54098,0x3c5ae880,0x3c1aa086,0x3baf10a2,0x3a96c32c,0x3c209ae5,0x3bb54098,0x3a96c32c,0xbb553db9,
0x3bc7c5c2,0x3ac8fb30,0xbb48bd36,0xbbf8cfcc,0x3b16a905,0xbb23308c,0xbbec9a7f,0xbc41071e,0xbac8fb30,0xbbd41501,0xbc38442a,0xbc80e7e4,0xbbaf10a2,0xbc2986c4,0xbc76e0cb,0xbc9ebc12,
0xbc14a031,0xbc663375,0xbc987fc0,0xbcb96842,0xbc4f7bca,0xbc8f8784,0xbcb2c8c9,0xbcd064af,0xbc839c3d,0xbca99415,0xbcc9d112,0xbce33c5a,0xbc9d7fd1,0xbcc0d0da,0xbcdd2d53,0xbcf18f57,
0x0,0x0,0x0,0x0,0x3b964083,0x3b9c76de,0x3ba2abb6,0x3ba8defc,0x3c14a031,0x3c1aa086,0x3c209ae5,0x3c268f12,0x3c5ae880,0x3c6363fa,0x3c6bcbbb,0x3c741f07,
0x3c8e39da,0x3c93682a,0x3c987fc0,0x3c9d7fd1,0x3cabeb4a,0x3cb1a81d,0x3cb73a23,0x3cbca003,0x3cc5e403,0x3ccbbbf8,0x3cd14d3d,0x3cd695e5,0x3cdb941a,0x3ce106f2,0x3ce60f88,0x3ceaab7b,
0x3cec835e,0x3cf10908,0x3cf4fa0b,0x3cf853f8,0x3cf853f8,0x3cfb61fc,0x3cfdabcc,0x3cff2f9d,0x3cfec46d,0x3cffd397,0x3cffec43,0x3cff0e58,0x3cffb10f,0x3cfe4323,0x3cfbaccd,0x3cf7f110,
0x3cfb14be,0x3cf6ba07,0x3cf10908,0x3cea09a7,0x3cf10908,0x3ce96591,0x3ce04621,0x3cd5b993,0x3ce1c598,0x3cd695e5,0x3cc9d112,0x3cbb8f3b,0x3ccd9f02,0x3cbebc1b,0x3cae3bde,0x3c9c420c,
0x3cb504f3,0x3ca26799,0x3c8e39da,0x3c715aea,0x3c987fc0,0x3c8242b1,0x3c553641,0x3c2395c5,0x3c715aea,0x3c3e1d4a,0x3c088e93,0x3ba2abb6,0x3c2c7cd4,0x3be67c66,0x3b61bc2e,0xb9490f88,
0x3bc7c5c2,0x3b16a905,0xbac8fb30,0xbbaf10a2,0x3ac8fb30,0xbb23308c,0xbbd41501,0xbc2986c4,0xbb48bd36,0xbbec9a7f,0xbc38442a,0xbc76e0cb,0xbbf8cfcc,0xbc41071e,0xbc80e7e4,0xbc9ebc12,
0xbc43ef15,0xbc839c3d,0xbca26799,0xbcbdaef9,0xbc839c3d,0xbca39da9,0xbcbfc767,0xbcd77026,0xbca26799,0xbcbfc767,0xbcd84853,0xbceb4b0c,0xbcbdaef9,0xbcd77026,0xbceb4b0c,0xbcf8b47b,
0xbcd4db31,0xbcea09a7,0xbcf853f8,0xbcff4e6d,0xbce76bd8,0xbcf72417,0xbcff0e58,0xbcfeea9d,0xbcf4fa0b,0xbcfe70b0,0xbcff4e6d,0xbcf78bc5,0xbcfd3aac,0xbcffc38f,0xbcf91298,0xbce96591,
0x0,0x0,0x0,0x0,0x3baf10a2,0x3bb54098,0x3bbb6ecf,0x3bc19b37,0x3c2c7cd4,0x3c3263ef,0x3c38442a,0x3c3e1d4a,0x3c7c5d27,0x3c8242b1,0x3c864b82,0x3c8a48ad,
0x3ca26799,0x3ca73656,0x3cabeb4a,0x3cb085bb,0x3cc1d870,0x3cc6e22a,0x3ccbbbf8,0x3cd064af,0x3cdb941a,0x3ce04621,0x3ce4aa59,0x3ce8bf3c,0x3ceed89e,0x3cf294f8,0x3cf5dec6,0x3cf8b47b,
0x3cfb14be,0x3cfd3aac,0x3cfec46d,0x3cffb10f,0x3cffec43,0x3cffe129,0x3cff0e58,0x3cfd7474,0x3cfd3aac,0x3cfa7302,0x3cf6ba07,0x3cf21352,0x3cf31447,0x3ced1c1d,0x3ce60f88,0x3cddf6be,
0x3ce1c598,0x3cd84853,0x3ccd9f02,0x3cc1d870,0x3cc9d112,0x3cbca003,0x3cae3bde,0x3c9ebc12,0x3cabeb4a,0x3c9b02c6,0x3c88f59b,0x3c6bcbbb,0x3c88f59b,0x3c6900b7,0x3c3e1d4a,0x3c119ddd,
0x3c43ef15,0x3c14a031,0x3bc7c5c2,0x3b48bd36,0x3be05c13,0x3b6e3876,0x39c90e90,0xbb3c3ac3,0x3ac8fb30,0xbafb2b74,0xbbaf10a2,0xbc0e9a22,0xbb7ab273,0xbbf2b651,0xbc3263ef,0xbc6900b7,
0xbc14a031,0xbc4f7bca,0xbc839c3d,0xbc9d7fd1,0xbc663375,0xbc8f8784,0xbca99415,0xbcc0d0da,0xbc987fc0,0xbcb2c8c9,0xbcc9d112,0xbcdd2d53,0xbcb96842,0xbcd064af,0xbce33c5a,0xbcf18f57,
0xbcd4db31,0xbce76bd8,0xbcf4fa0b,0xbcfd3aac,0xbcea09a7,0xbcf72417,0xbcfe70b0,0xbcffc38f,0xbcf853f8,0xbcff0e58,0xbcff4e6d,0xbcf91298,0xbcff4e6d,0xbcfeea9d,0xbcf78bc5,0xbce96591,
0xbcfec46d,0xbcf6ba07,0xbce76bd8,0xbcd14d3d,0xbcf6ba07,0xbce6becc,0xbccf7a1f,0xbcb1a81d,0xbce76bd8,0xbccf7a1f,0xbcb085bb,0xbc8b9a6b,0xbcd14d3d,0xbcb1a81d,0xbc8b9a6b,0xbc41071e,
0x3f803f80,0x3f803f80,0x3f803f80,0x3f803f80,0x3f7b3f80,0x3f553f6d,0xbe480000,0xbf0ebec4,0x3f6d3f80,0x3ec43f35,0xbf6dbf80,0xbec4bf35,0x3f553f80,0xbe483ec4,0x3f0e0000,0x3f7b3f6d,
0x3f353f80,0xbf350000,0x3f353f80,0xbf350000,0x3f0e3f80,0xbf7bbec4,0xbf550000,0xbe48bf6d,0x3ec43f80,0xbf6dbf35,0xbec4bf80,0x3f6d3f35,0x3e483f80,0xbf0ebf6d,0x3f7b0000,0xbf553ec4,
0x3f80,0xbf80,0x3f80,0xbf80,0xbe483f80,0x3f0ebf6d,0xbf7b0000,0x3f553ec4,0xbec43f80,0x3f6dbf35,0x3ec4bf80,0xbf6d3f35,0xbf0e3f80,0x3f7bbec4,0x3f550000,0x3e48bf6d,
0xbf353f80,0x3f350000,0xbf353f80,0x3f350000,0xbf553f80,0x3e483ec4,0xbf0e0000,0xbf7b3f6d,0xbf6d3f80,0xbec43f35,0x3f6dbf80,0x3ec4bf35,0xbf7b3f80,0xbf553f6d,0x3e480000,0x3f0ebec4,
0xbf803f80,0xbf803f80,0xbf803f80,0xbf803f80,0xbf7b3f80,0xbf553f6d,0x3e480000,0x3f0ebec4,0xbf6d3f80,0xbec43f35,0x3f6dbf80,0x3ec4bf35,0xbf553f80,0x3e483ec4,0xbf0e0000,0xbf7b3f6d,
0xbf353f80,0x3f350000,0xbf353f80,0x3f350000,0xbf0e3f80,0x3f7bbec4,0x3f550000,0x3e48bf6d,0xbec43f80,0x3f6dbf35,0x3ec4bf80,0xbf6d3f35,0xbe483f80,0x3f0ebf6d,0xbf7b0000,0x3f553ec4,
0x3f80,0xbf80,0x3f80,0xbf80,0x3e483f80,0xbf0ebf6d,0x3f7b0000,0xbf553ec4,0x3ec43f80,0xbf6dbf35,0xbec4bf80,0x3f6d3f35,0x3f0e3f80,0xbf7bbec4,0xbf550000,0xbe48bf6d,
0x3f353f80,0xbf350000,0x3f353f80,0xbf350000,0x3f553f80,0xbe483ec4,0x3f0e0000,0x3f7b3f6d,0x3f6d3f80,0x3ec43f35,0xbf6dbf80,0xbec4bf35,0x3f7b3f80,0x3f553f6d,0xbe480000,0xbf0ebec4,
0x3f803f80,0x3f803f80,0x3f803f80,0x3f803f80,0x3f0e3f35,0x3e483ec4,0xbf55bf35,0xbf7bbf6d,0xbec40000,0xbf6dbf35,0x3ec40000,0x3f6d3f35,0xbf7bbf35,0xbf0ebf6d,0x3e483f35,0xbf55bec4,
0xbf35bf80,0x3f350000,0xbf35bf80,0x3f350000,0x3e48bf35,0x3f553f6d,0x3f7b3f35,0xbf0e3ec4,0x3f6d0000,0xbec43f35,0xbf6d0000,0x3ec4bf35,0x3f553f35,0xbf7bbec4,0x3f0ebf35,0xbe483f6d,
0x3f80,0xbf80,0x3f80,0xbf80,0xbf553f35,0x3f7bbec4,0xbf0ebf35,0x3e483f6d,0xbf6d0000,0x3ec43f35,0x3f6d0000,0xbec4bf35,0xbe48bf35,0xbf553f6d,0xbf7b3f35,0x3f0e3ec4,
0x3f35bf80,0xbf350000,0x3f35bf80,0xbf350000,0x3f7bbf35,0x3f0ebf6d,0xbe483f35,0x3f55bec4,0x3ec40000,0x3f6dbf35,0xbec40000,0xbf6d3f35,0xbf0e3f35,0xbe483ec4,0x3f55bf35,0x3f7bbf6d,
0xbf803f80,0xbf803f80,0xbf803f80,0xbf803f80,0xbf0e3f35,0xbe483ec4,0x3f55bf35,0x3f7bbf6d,0x3ec40000,0x3f6dbf35,0xbec40000,0xbf6d3f35,0x3f7bbf35,0x3f0ebf6d,0xbe483f35,0x3f55bec4,
0x3f35bf80,0xbf350000,0x3f35bf80,0xbf350000,0xbe48bf35,0xbf553f6d,0xbf7b3f35,0x3f0e3ec4,0xbf6d0000,0x3ec43f35,0x3f6d0000,0xbec4bf35,0xbf553f35,0x3f7bbec4,0xbf0ebf35,0x3e483f6d,
0x3f80,0xbf80,0x3f80,0xbf80,0x3f553f35,0xbf7bbec4,0x3f0ebf35,0xbe483f6d,0x3f6d0000,0xbec43f35,0xbf6d0000,0x3ec4bf35,0x3e48bf35,0x3f553f6d,0x3f7b3f35,0xbf0e3ec4,
0xbf35bf80,0x3f350000,0xbf35bf80,0x3f350000,0xbf7bbf35,0xbf0ebf6d,0x3e483f35,0xbf55bec4,0xbec40000,0xbf6dbf35,0x3ec40000,0x3f6d3f35,0x3f0e3f35,0x3e483ec4,0xbf55bf35,0xbf7bbf6d,
0x3f803f80,0x3f803f80,0x3f803f80,0x3f803f80,0xbf7bbf80,0xbf55bf6d,0x3e480000,0x3f0e3ec4,0x3f6d3f80,0x3ec43f35,0xbf6dbf80,0xbec4bf35,0xbf55bf80,0x3e48bec4,0xbf0e0000,0xbf7bbf6d,
0x3f353f80,0xbf350000,0x3f353f80,0xbf350000,0xbf0ebf80,0x3f7b3ec4,0x3f550000,0x3e483f6d,0x3ec43f80,0xbf6dbf35,0xbec4bf80,0x3f6d3f35,0xbe48bf80,0x3f0e3f6d,0xbf7b0000,0x3f55bec4,
0x3f80,0xbf80,0x3f80,0xbf80,0x3e48bf80,0xbf0e3f6d,0x3f7b0000,0xbf55bec4,0xbec43f80,0x3f6dbf35,0x3ec4bf80,0xbf6d3f35,0x3f0ebf80,0xbf7b3ec4,0xbf550000,0xbe483f6d,
0xbf353f80,0x3f350000,0xbf353f80,0x3f350000,0x3f55bf80,0xbe48bec4,0x3f0e0000,0x3f7bbf6d,0xbf6d3f80,0xbec43f35,0x3f6dbf80,0x3ec4bf35,0x3f7bbf80,0x3f55bf6d,0xbe480000,0xbf0e3ec4,
0xbf803f80,0xbf803f80,0xbf803f80,0xbf803f80,0x3f7bbf80,0x3f55bf6d,0xbe480000,0xbf0e3ec4,0xbf6d3f80,0xbec43f35,0x3f6dbf80,0x3ec4bf35,0x3f55bf80,0xbe48bec4,0x3f0e0000,0x3f7bbf6d,
0xbf353f80,0x3f350000,0xbf353f80,0x3f350000,0x3f0ebf80,0xbf7b3ec4,0xbf550000,0xbe483f6d,0xbec43f80,0x3f6dbf35,0x3ec4bf80,0xbf6d3f35,0x3e48bf80,0xbf0e3f6d,0x3f7b0000,0xbf55bec4,
0x3f80,0xbf80,0x3f80,0xbf80,0xbe48bf80,0x3f0e3f6d,0xbf7b0000,0x3f55bec4,0x3ec43f80,0xbf6dbf35,0xbec4bf80,0x3f6d3f35,0xbf0ebf80,0x3f7b3ec4,0x3f550000,0x3e483f6d,
0x3f353f80,0xbf350000,0x3f353f80,0xbf350000,0xbf55bf80,0x3e48bec4,0xbf0e0000,0xbf7bbf6d,0x3f6d3f80,0x3ec43f35,0xbf6dbf80,0xbec4bf35,0xbf7bbf80,0xbf55bf6d,0x3e480000,0x3f0e3ec4,
0x3f803f80,0x3f803f80,0x3f803f80,0x3f803f80,0xbf0ebf35,0xbe48bec4,0x3f553f35,0x3f7b3f6d,0xbec40000,0xbf6dbf35,0x3ec40000,0x3f6d3f35,0x3f7b3f35,0x3f0e3f6d,0xbe48bf35,0x3f553ec4,
0xbf35bf80,0x3f350000,0xbf35bf80,0x3f350000,0xbe483f35,0xbf55bf6d,0xbf7bbf35,0x3f0ebec4,0x3f6d0000,0xbec43f35,0xbf6d0000,0x3ec4bf35,0xbf55bf35,0x3f7b3ec4,0xbf0e3f35,0x3e48bf6d,
0x3f80,0xbf80,0x3f80,0xbf80,0x3f55bf35,0xbf7b3ec4,0x3f0e3f35,0xbe48bf6d,0xbf6d0000,0x3ec43f35,0x3f6d0000,0xbec4bf35,0x3e483f35,0x3f55bf6d,0x3f7bbf35,0xbf0ebec4,
0x3f35bf80,0xbf350000,0x3f35bf80,0xbf350000,0xbf7b3f35,0xbf0e3f6d,0x3e48bf35,0xbf553ec4,0x3ec40000,0x3f6dbf35,0xbec40000,0xbf6d3f35,0x3f0ebf35,0x3e48bec4,0xbf553f35,0xbf7b3f6d,
0xbf803f80,0xbf803f80,0xbf803f80,0xbf803f80,0x3f0ebf35,0x3e48bec4,0xbf553f35,0xbf7b3f6d,0x3ec40000,0x3f6dbf35,0xbec40000,0xbf6d3f35,0xbf7b3f35,0xbf0e3f6d,0x3e48bf35,0xbf553ec4,
0x3f35bf80,0xbf350000,0x3f35bf80,0xbf350000,0x3e483f35,0x3f55bf6d,0x3f7bbf35,0xbf0ebec4,0xbf6d0000,0x3ec43f35,0x3f6d0000,0xbec4bf35,0x3f55bf35,0xbf7b3ec4,0x3f0e3f35,0xbe48bf6d,
0x3f80,0xbf80,0x3f80,0xbf80,0xbf55bf35,0x3f7b3ec4,0xbf0e3f35,0x3e48bf6d,0x3f6d0000,0xbec43f35,0xbf6d0000,0x3ec4bf35,0xbe483f35,0xbf55bf6d,0xbf7bbf35,0x3f0ebec4,
0xbf35bf80,0x3f350000,0xbf35bf80,0x3f350000,0x3f7b3f35,0x3f0e3f6d,0xbe48bf35,0x3f553ec4,0xbec40000,0xbf6dbf35,0x3ec40000,0x3f6d3f35,0xbf0ebf35,0xbe48bec4,0x3f553f35,0x3f7b3f6d,
0x0,0x0,0x0,0x0,0xbe480000,0xbf0ebec4,0xbf7bbf80,0xbf55bf6d,0xbec40000,0xbf6dbf35,0x3ec40000,0x3f6d3f35,0xbf0e0000,0xbf7bbf6d,0x3f553f80,0xbe483ec4,
0xbf350000,0xbf35bf80,0xbf350000,0xbf35bf80,0xbf550000,0xbe48bf6d,0xbf0ebf80,0x3f7b3ec4,0xbf6d0000,0x3ec4bf35,0x3f6d0000,0xbec43f35,0xbf7b0000,0x3f55bec4,0x3e483f80,0xbf0ebf6d,
0xbf800000,0x3f800000,0xbf800000,0x3f800000,0xbf7b0000,0x3f553ec4,0x3e48bf80,0xbf0e3f6d,0xbf6d0000,0x3ec43f35,0x3f6d0000,0xbec4bf35,0xbf550000,0xbe483f6d,0xbf0e3f80,0x3f7bbec4,
0xbf350000,0xbf353f80,0xbf350000,0xbf353f80,0xbf0e0000,0xbf7b3f6d,0x3f55bf80,0xbe48bec4,0xbec40000,0xbf6d3f35,0x3ec40000,0x3f6dbf35,0xbe480000,0xbf0e3ec4,0xbf7b3f80,0xbf553f6d,
0x0,0x0,0x0,0x0,0x3e480000,0x3f0ebec4,0x3f7bbf80,0x3f55bf6d,0x3ec40000,0x3f6dbf35,0xbec40000,0xbf6d3f35,0x3f0e0000,0x3f7bbf6d,0xbf553f80,0x3e483ec4,
0x3f350000,0x3f35bf80,0x3f350000,0x3f35bf80,0x3f550000,0x3e48bf6d,0x3f0ebf80,0xbf7b3ec4,0x3f6d0000,0xbec4bf35,0xbf6d0000,0x3ec43f35,0x3f7b0000,0xbf55bec4,0xbe483f80,0x3f0ebf6d,
0x3f800000,0xbf800000,0x3f800000,0xbf800000,0x3f7b0000,0xbf553ec4,0xbe48bf80,0x3f0e3f6d,0x3f6d0000,0xbec43f35,0xbf6d0000,0x3ec4bf35,0x3f550000,0x3e483f6d,0x3f0e3f80,0xbf7bbec4,
0x3f350000,0x3f353f80,0x3f350000,0x3f353f80,0x3f0e0000,0x3f7b3f6d,0xbf55bf80,0x3e48bec4,0x3ec40000,0x3f6d3f35,0xbec40000,0xbf6dbf35,0x3e480000,0x3f0e3ec4,0x3f7b3f80,0x3f553f6d,
0x0,0x0,0x0,0x0,0xbf55bf35,0xbf7bbf6d,0xbf0ebf35,0xbe48bec4,0xbf6dbf80,0xbec4bf35,0x3f6d3f80,0x3ec43f35,0xbe48bf35,0x3f553ec4,0xbf7bbf35,0xbf0ebf6d,
0x3f350000,0x3f353f80,0x3f350000,0x3f353f80,0x3f7b3f35,0xbf0e3ec4,0xbe483f35,0xbf55bf6d,0x3ec43f80,0xbf6dbf35,0xbec4bf80,0x3f6d3f35,0xbf0e3f35,0x3e48bf6d,0x3f553f35,0xbf7bbec4,
0xbf800000,0x3f800000,0xbf800000,0x3f800000,0xbf0ebf35,0x3e483f6d,0x3f55bf35,0xbf7b3ec4,0x3ec4bf80,0xbf6d3f35,0xbec43f80,0x3f6dbf35,0x3f7bbf35,0xbf0ebec4,0xbe48bf35,0xbf553f6d,
0x3f350000,0x3f35bf80,0x3f350000,0x3f35bf80,0xbe483f35,0x3f55bec4,0xbf7b3f35,0xbf0e3f6d,0xbf6d3f80,0xbec43f35,0x3f6dbf80,0x3ec4bf35,0xbf553f35,0xbf7b3f6d,0xbf0e3f35,0xbe483ec4,
0x0,0x0,0x0,0x0,0x3f55bf35,0x3f7bbf6d,0x3f0ebf35,0x3e48bec4,0x3f6dbf80,0x3ec4bf35,0xbf6d3f80,0xbec43f35,0x3e48bf35,0xbf553ec4,0x3f7bbf35,0x3f0ebf6d,
0xbf350000,0xbf353f80,0xbf350000,0xbf353f80,0xbf7b3f35,0x3f0e3ec4,0x3e483f35,0x3f55bf6d,0xbec43f80,0x3f6dbf35,0x3ec4bf80,0xbf6d3f35,0x3f0e3f35,0xbe48bf6d,0xbf553f35,0x3f7bbec4,
0x3f800000,0xbf800000,0x3f800000,0xbf800000,0x3f0ebf35,0xbe483f6d,0xbf55bf35,0x3f7b3ec4,0xbec4bf80,0x3f6d3f35,0x3ec43f80,0xbf6dbf35,0xbf7bbf35,0x3f0ebec4,0x3e48bf35,0x3f553f6d,
0xbf350000,0xbf35bf80,0xbf350000,0xbf35bf80,0x3e483f35,0xbf55bec4,0x3f7b3f35,0x3f0e3f6d,0x3f6d3f80,0x3ec43f35,0xbf6dbf80,0xbec4bf35,0x3f553f35,0x3f7b3f6d,0x3f0e3f35,0x3e483ec4,
0x0,0x0,0x0,0x0,0x3e480000,0x3f0e3ec4,0x3f7b3f80,0x3f553f6d,0xbec40000,0xbf6dbf35,0x3ec40000,0x3f6d3f35,0x3f0e0000,0x3f7b3f6d,0xbf55bf80,0x3e48bec4,
0xbf350000,0xbf35bf80,0xbf350000,0xbf35bf80,0x3f550000,0x3e483f6d,0x3f0e3f80,0xbf7bbec4,0xbf6d0000,0x3ec4bf35,0x3f6d0000,0xbec43f35,0x3f7b0000,0xbf553ec4,0xbe48bf80,0x3f0e3f6d,
0xbf800000,0x3f800000,0xbf800000,0x3f800000,0x3f7b0000,0xbf55bec4,0xbe483f80,0x3f0ebf6d,0xbf6d0000,0x3ec43f35,0x3f6d0000,0xbec4bf35,0x3f550000,0x3e48bf6d,0x3f0ebf80,0xbf7b3ec4,
0xbf350000,0xbf353f80,0xbf350000,0xbf353f80,0x3f0e0000,0x3f7bbf6d,0xbf553f80,0x3e483ec4,0xbec40000,0xbf6d3f35,0x3ec40000,0x3f6dbf35,0x3e480000,0x3f0ebec4,0x3f7bbf80,0x3f55bf6d,
0x0,0x0,0x0,0x0,0xbe480000,0xbf0e3ec4,0xbf7b3f80,0xbf553f6d,0x3ec40000,0x3f6dbf35,0xbec40000,0xbf6d3f35,0xbf0e0000,0xbf7b3f6d,0x3f55bf80,0xbe48bec4,
0x3f350000,0x3f35bf80,0x3f350000,0x3f35bf80,0xbf550000,0xbe483f6d,0xbf0e3f80,0x3f7bbec4,0x3f6d0000,0xbec4bf35,0xbf6d0000,0x3ec43f35,0xbf7b0000,0x3f553ec4,0x3e48bf80,0xbf0e3f6d,
0x3f800000,0xbf800000,0x3f800000,0xbf800000,0xbf7b0000,0x3f55bec4,0x3e483f80,0xbf0ebf6d,0x3f6d0000,0xbec43f35,0xbf6d0000,0x3ec4bf35,0xbf550000,0xbe48bf6d,0xbf0ebf80,0x3f7b3ec4,
0x3f350000,0x3f353f80,0x3f350000,0x3f353f80,0xbf0e0000,0xbf7bbf6d,0x3f553f80,0xbe483ec4,0x3ec40000,0x3f6d3f35,0xbec40000,0xbf6dbf35,0xbe480000,0xbf0ebec4,0xbf7bbf80,0xbf55bf6d,
0x0,0x0,0x0,0x0,0x3f553f35,0x3f7b3f6d,0x3f0e3f35,0x3e483ec4,0xbf6dbf80,0xbec4bf35,0x3f6d3f80,0x3ec43f35,0x3e483f35,0xbf55bec4,0x3f7b3f35,0x3f0e3f6d,
0x3f350000,0x3f353f80,0x3f350000,0x3f353f80,0xbf7bbf35,0x3f0ebec4,0x3e48bf35,0x3f553f6d,0x3ec43f80,0xbf6dbf35,0xbec4bf80,0x3f6d3f35,0x3f0ebf35,0xbe483f6d,0xbf55bf35,0x3f7b3ec4,
0xbf800000,0x3f800000,0xbf800000,0x3f800000,0x3f0e3f35,0xbe48bf6d,0xbf553f35,0x3f7bbec4,0x3ec4bf80,0xbf6d3f35,0xbec43f80,0x3f6dbf35,0xbf7b3f35,0x3f0e3ec4,0x3e483f35,0x3f55bf6d,
0x3f350000,0x3f35bf80,0x3f350000,0x3f35bf80,0x3e48bf35,0xbf553ec4,0x3f7bbf35,0x3f0ebf6d,0xbf6d3f80,0xbec43f35,0x3f6dbf80,0x3ec4bf35,0x3f55bf35,0x3f7bbf6d,0x3f0ebf35,0x3e48bec4,
0x0,0x0,0x0,0x0,0xbf553f35,0xbf7b3f6d,0xbf0e3f35,0xbe483ec4,0x3f6dbf80,0x3ec4bf35,0xbf6d3f80,0xbec43f35,0xbe483f35,0x3f55bec4,0xbf7b3f35,0xbf0e3f6d,
0xbf350000,0xbf353f80,0xbf350000,0xbf353f80,0x3f7bbf35,0xbf0ebec4,0xbe48bf35,0xbf553f6d,0xbec43f80,0x3f6dbf35,0x3ec4bf80,0xbf6d3f35,0xbf0ebf35,0x3e483f6d,0x3f55bf35,0xbf7b3ec4,
0x3f800000,0xbf800000,0x3f800000,0xbf800000,0xbf0e3f35,0x3e48bf6d,0x3f553f35,0xbf7bbec4,0xbec4bf80,0x3f6d3f35,0x3ec43f80,0xbf6dbf35,0x3f7b3f35,0xbf0e3ec4,0xbe483f35,0xbf55bf6d,
0xbf350000,0xbf35bf80,0xbf350000,0xbf35bf80,0xbe48bf35,0x3f553ec4,0xbf7bbf35,0xbf0ebf6d,0x3f6d3f80,0x3ec43f35,0xbf6dbf80,0xbec4bf35,0xbf55bf35,0xbf7bbf6d,0xbf0ebf35,0xbe48bec4,
0x3f80,0x3f80,0x3f80,0x3f80,0x3f80,0xbe483f7b,0xbf353f35,0xbf553f0e,0x3f80,0xbec43f6d,0xbf800000,0xbf6dbec4,0x3f80,0xbf0e3f55,0xbf35bf35,0xbe48bf7b,
0x3f80,0xbf353f35,0xbf80,0x3f35bf35,0x3f80,0xbf553f0e,0x3f35bf35,0x3f7b3e48,0x3f80,0xbf6d3ec4,0x3f800000,0x3ec43f6d,0x3f80,0xbf7b3e48,0x3f353f35,0xbf0e3f55,
0x3f80,0xbf800000,0x3f80,0xbf800000,0x3f80,0xbf7bbe48,0xbf353f35,0xbf0ebf55,0x3f80,0xbf6dbec4,0xbf800000,0x3ec4bf6d,0x3f80,0xbf55bf0e,0xbf35bf35,0x3f7bbe48,
0x3f80,0xbf35bf35,0xbf80,0x3f353f35,0x3f80,0xbf0ebf55,0x3f35bf35,0xbe483f7b,0x3f80,0xbec4bf6d,0x3f800000,0xbf6d3ec4,0x3f80,0xbe48bf7b,0x3f353f35,0xbf55bf0e,
0x3f80,0xbf80,0x3f80,0xbf80,0x3f80,0x3e48bf7b,0xbf353f35,0x3f55bf0e,0x3f80,0x3ec4bf6d,0xbf800000,0x3f6d3ec4,0x3f80,0x3f0ebf55,0xbf35bf35,0x3e483f7b,
0x3f80,0x3f35bf35,0xbf80,0xbf353f35,0x3f80,0x3f55bf0e,0x3f35bf35,0xbf7bbe48,0x3f80,0x3f6dbec4,0x3f800000,0xbec4bf6d,0x3f80,0x3f7bbe48,0x3f353f35,0x3f0ebf55,
0x3f80,0x3f800000,0x3f80,0x3f800000,0x3f80,0x3f7b3e48,0xbf353f35,0x3f0e3f55,0x3f80,0x3f6d3ec4,0xbf800000,0xbec43f6d,0x3f80,0x3f553f0e,0xbf35bf35,0xbf7b3e48,
0x3f80,0x3f353f35,0xbf80,0xbf35bf35,0x3f80,0x3f0e3f55,0x3f35bf35,0x3e48bf7b,0x3f80,0x3ec43f6d,0x3f800000,0x3f6dbec4,0x3f80,0x3e483f7b,0x3f353f35,0x3f553f0e,
0x3f80,0x3f80,0x3f80,0x3f80,0xbec43f6d,0xbf0e3f55,0xbf6d3ec4,0xbf7b3e48,0xbf353f35,0xbf6d3ec4,0xbf35bf35,0xbec4bf6d,0xbf6d3ec4,0xbf7bbe48,0x3ec4bf6d,0x3f55bf0e,
0xbf800000,0xbf35bf35,0x3f800000,0x3f353f35,0xbf6dbec4,0xbe48bf7b,0x3ec43f6d,0xbf0e3f55,0xbf35bf35,0x3ec4bf6d,0xbf353f35,0xbf6dbec4,0xbec4bf6d,0x3f55bf0e,0xbf6dbec4,0x3e48bf7b,
0xbf80,0x3f800000,0xbf80,0x3f800000,0x3ec4bf6d,0x3f553f0e,0x3f6dbec4,0x3e483f7b,0x3f35bf35,0x3ec43f6d,0x3f353f35,0xbf6d3ec4,0x3f6dbec4,0xbe483f7b,0xbec43f6d,0xbf0ebf55,
0x3f800000,0xbf353f35,0xbf800000,0x3f35bf35,0x3f6d3ec4,0xbf7b3e48,0xbec4bf6d,0x3f553f0e,0x3f353f35,0xbf6dbec4,0x3f35bf35,0xbec43f6d,0x3ec43f6d,0xbf0ebf55,0x3f6d3ec4,0xbf7bbe48,
0x3f80,0xbf80,0x3f80,0xbf80,0xbec43f6d,0x3f0ebf55,0xbf6d3ec4,0x3f7bbe48,0xbf353f35,0x3f6dbec4,0xbf35bf35,0x3ec43f6d,0xbf6d3ec4,0x3f7b3e48,0x3ec4bf6d,0xbf553f0e,
0xbf800000,0x3f353f35,0x3f800000,0xbf35bf35,0xbf6dbec4,0x3e483f7b,0x3ec43f6d,0x3f0ebf55,0xbf35bf35,0xbec43f6d,0xbf353f35,0x3f6d3ec4,0xbec4bf6d,0xbf553f0e,0xbf6dbec4,0xbe483f7b,
0xbf80,0xbf800000,0xbf80,0xbf800000,0x3ec4bf6d,0xbf55bf0e,0x3f6dbec4,0xbe48bf7b,0x3f35bf35,0xbec4bf6d,0x3f353f35,0x3f6dbec4,0x3f6dbec4,0x3e48bf7b,0xbec43f6d,0x3f0e3f55,
0x3f800000,0x3f35bf35,0xbf800000,0xbf353f35,0x3f6d3ec4,0x3f7bbe48,0xbec4bf6d,0xbf55bf0e,0x3f353f35,0x3f6d3ec4,0x3f35bf35,0x3ec4bf6d,0x3ec43f6d,0x3f0e3f55,0x3f6d3ec4,0x3f7b3e48,
0x3f80,0x3f80,0x3f80,0x3f80,0xbf800000,0xbf7bbe48,0xbf35bf35,0xbf0ebf55,0xbf80,0x3ec4bf6d,0x3f800000,0x3f6d3ec4,0x3f800000,0x3f553f0e,0xbf353f35,0xbf7b3e48,
0x3f80,0xbf353f35,0xbf80,0x3f35bf35,0xbf800000,0xbf0ebf55,0x3f353f35,0xbe483f7b,0xbf80,0x3f6dbec4,0xbf800000,0xbec4bf6d,0x3f800000,0x3e483f7b,0x3f35bf35,0x3f553f0e,
0x3f80,0xbf800000,0x3f80,0xbf800000,0xbf800000,0x3e48bf7b,0xbf35bf35,0x3f55bf0e,0xbf80,0x3f6d3ec4,0x3f800000,0xbec43f6d,0x3f800000,0xbf0e3f55,0xbf353f35,0xbe48bf7b,
0x3f80,0xbf35bf35,0xbf80,0x3f353f35,0xbf800000,0x3f55bf0e,0x3f353f35,0xbf7bbe48,0xbf80,0x3ec43f6d,0xbf800000,0x3f6dbec4,0x3f800000,0xbf7b3e48,0x3f35bf35,0xbf0e3f55,
0x3f80,0xbf80,0x3f80,0xbf80,0xbf800000,0x3f7b3e48,0xbf35bf35,0x3f0e3f55,0xbf80,0xbec43f6d,0x3f800000,0xbf6dbec4,0x3f800000,0xbf55bf0e,0xbf353f35,0x3f7bbe48,
0x3f80,0x3f35bf35,0xbf80,0xbf353f35,0xbf800000,0x3f0e3f55,0x3f353f35,0x3e48bf7b,0xbf80,0xbf6d3ec4,0xbf800000,0x3ec43f6d,0x3f800000,0xbe48bf7b,0x3f35bf35,0xbf55bf0e,
0x3f80,0x3f800000,0x3f80,0x3f800000,0xbf800000,0xbe483f7b,0xbf35bf35,0xbf553f0e,0xbf80,0xbf6dbec4,0x3f800000,0x3ec4bf6d,0x3f800000,0x3f0ebf55,0xbf353f35,0x3e483f7b,
0x3f80,0x3f353f35,0xbf80,0xbf35bf35,0xbf800000,0xbf553f0e,0x3f353f35,0x3f7b3e48,0xbf80,0xbec4bf6d,0xbf800000,0xbf6d3ec4,0x3f800000,0x3f7bbe48,0x3f35bf35,0x3f0ebf55,
0x3f80,0x3f80,0x3f80,0x3f80,0xbf6dbec4,0xbf55bf0e,0xbec4bf6d,0xbe48bf7b,0x3f35bf35,0x3f6dbec4,0x3f353f35,0x3ec43f6d,0x3ec43f6d,0xbe483f7b,0xbf6dbec4,0xbf0ebf55,
0xbf800000,0xbf35bf35,0x3f800000,0x3f353f35,0x3ec4bf6d,0x3f7bbe48,0xbf6d3ec4,0xbf55bf0e,0x3f353f35,0xbec43f6d,0x3f35bf35,0x3f6d3ec4,0xbf6d3ec4,0xbf0ebf55,0xbec43f6d,0xbf7bbe48,
0xbf80,0x3f800000,0xbf80,0x3f800000,0x3f6d3ec4,0xbf0e3f55,0x3ec43f6d,0xbf7b3e48,0xbf353f35,0xbec4bf6d,0xbf35bf35,0x3f6dbec4,0xbec4bf6d,0x3f7b3e48,0x3f6d3ec4,0xbf553f0e,
0x3f800000,0xbf353f35,0xbf800000,0x3f35bf35,0xbec43f6d,0xbe48bf7b,0x3f6dbec4,0xbf0e3f55,0xbf35bf35,0x3f6d3ec4,0xbf353f35,0x3ec4bf6d,0x3f6dbec4,0xbf553f0e,0x3ec4bf6d,0xbe483f7b,
0x3f80,0xbf80,0x3f80,0xbf80,0xbf6dbec4,0x3f553f0e,0xbec4bf6d,0x3e483f7b,0x3f35bf35,0xbf6d3ec4,0x3f353f35,0xbec4bf6d,0x3ec43f6d,0x3e48bf7b,0xbf6dbec4,0x3f0e3f55,
0xbf800000,0x3f353f35,0x3f800000,0xbf35bf35,0x3ec4bf6d,0xbf7b3e48,0xbf6d3ec4,0x3f553f0e,0x3f353f35,0x3ec4bf6d,0x3f35bf35,0xbf6dbec4,0xbf6d3ec4,0x3f0e3f55,0xbec43f6d,0x3f7b3e48,
0xbf80,0xbf800000,0xbf80,0xbf800000,0x3f6d3ec4,0x3f0ebf55,0x3ec43f6d,0x3f7bbe48,0xbf353f35,0x3ec43f6d,0xbf35bf35,0xbf6d3ec4,0xbec4bf6d,0xbf7bbe48,0x3f6d3ec4,0x3f55bf0e,
0x3f800000,0x3f35bf35,0xbf800000,0xbf353f35,0xbec43f6d,0x3e483f7b,0x3f6dbec4,0x3f0ebf55,0xbf35bf35,0xbf6dbec4,0xbf353f35,0xbec43f6d,0x3f6dbec4,0x3f55bf0e,0x3ec4bf6d,0x3e48bf7b,
0x3f800000,0x3f800000,0x3f800000,0x3f800000,0x3f800000,0x3f7b3e48,0x3f353f35,0x3f0e3f55,0x3f800000,0x3f6d3ec4,0x3f80,0xbec43f6d,0x3f800000,0x3f553f0e,0xbf353f35,0xbf7b3e48,
0x3f800000,0x3f353f35,0xbf800000,0xbf35bf35,0x3f800000,0x3f0e3f55,0xbf35bf35,0x3e48bf7b,0x3f800000,0x3ec43f6d,0xbf80,0x3f6dbec4,0x3f800000,0x3e483f7b,0x3f35bf35,0x3f553f0e,
0x3f800000,0x3f80,0x3f800000,0x3f80,0x3f800000,0xbe483f7b,0x3f353f35,0xbf553f0e,0x3f800000,0xbec43f6d,0x3f80,0xbf6dbec4,0x3f800000,0xbf0e3f55,0xbf353f35,0xbe48bf7b,
0x3f800000,0xbf353f35,0xbf800000,0x3f35bf35,0x3f800000,0xbf553f0e,0xbf35bf35,0x3f7b3e48,0x3f800000,0xbf6d3ec4,0xbf80,0x3ec43f6d,0x3f800000,0xbf7b3e48,0x3f35bf35,0xbf0e3f55,
0x3f800000,0xbf800000,0x3f800000,0xbf800000,0x3f800000,0xbf7bbe48,0x3f353f35,0xbf0ebf55,0x3f800000,0xbf6dbec4,0x3f80,0x3ec4bf6d,0x3f800000,0xbf55bf0e,0xbf353f35,0x3f7bbe48,
0x3f800000,0xbf35bf35,0xbf800000,0x3f353f35,0x3f800000,0xbf0ebf55,0xbf35bf35,0xbe483f7b,0x3f800000,0xbec4bf6d,0xbf80,0xbf6d3ec4,0x3f800000,0xbe48bf7b,0x3f35bf35,0xbf55bf0e,
0x3f800000,0xbf80,0x3f800000,0xbf80,0x3f800000,0x3e48bf7b,0x3f353f35,0x3f55bf0e,0x3f800000,0x3ec4bf6d,0x3f80,0x3f6d3ec4,0x3f800000,0x3f0ebf55,0xbf353f35,0x3e483f7b,
0x3f800000,0x3f35bf35,0xbf800000,0xbf353f35,0x3f800000,0x3f55bf0e,0xbf35bf35,0xbf7bbe48,0x3f800000,0x3f6dbec4,0xbf80,0xbec4bf6d,0x3f800000,0x3f7bbe48,0x3f35bf35,0x3f0ebf55,
0x3f800000,0x3f800000,0x3f800000,0x3f800000,0x3f6d3ec4,0x3f553f0e,0x3ec43f6d,0x3e483f7b,0x3f353f35,0x3ec43f6d,0xbf353f35,0xbf6d3ec4,0x3ec43f6d,0xbe483f7b,0xbf6dbec4,0xbf0ebf55,
0x3f80,0xbf353f35,0xbf80,0x3f35bf35,0xbec43f6d,0xbf7b3e48,0x3f6dbec4,0x3f553f0e,0xbf353f35,0xbf6dbec4,0x3f353f35,0xbec43f6d,0xbf6d3ec4,0xbf0ebf55,0xbec43f6d,0xbf7bbe48,
0xbf800000,0xbf80,0xbf800000,0xbf80,0xbf6dbec4,0x3f0ebf55,0xbec4bf6d,0x3f7bbe48,0xbf35bf35,0x3f6dbec4,0x3f35bf35,0x3ec43f6d,0xbec4bf6d,0x3f7b3e48,0x3f6d3ec4,0xbf553f0e,
0xbf80,0x3f353f35,0x3f80,0xbf35bf35,0x3ec4bf6d,0x3e483f7b,0xbf6d3ec4,0x3f0ebf55,0x3f35bf35,0xbec43f6d,0xbf35bf35,0x3f6d3ec4,0x3f6dbec4,0xbf553f0e,0x3ec4bf6d,0xbe483f7b,
0x3f800000,0xbf800000,0x3f800000,0xbf800000,0x3f6d3ec4,0xbf55bf0e,0x3ec43f6d,0xbe48bf7b,0x3f353f35,0xbec4bf6d,0xbf353f35,0x3f6dbec4,0x3ec43f6d,0x3e48bf7b,0xbf6dbec4,0x3f0e3f55,
0x3f80,0x3f35bf35,0xbf80,0xbf353f35,0xbec43f6d,0x3f7bbe48,0x3f6dbec4,0xbf55bf0e,0xbf353f35,0x3f6d3ec4,0x3f353f35,0x3ec4bf6d,0xbf6d3ec4,0x3f0e3f55,0xbec43f6d,0x3f7b3e48,
0xbf800000,0x3f80,0xbf800000,0x3f80,0xbf6dbec4,0xbf0e3f55,0xbec4bf6d,0xbf7b3e48,0xbf35bf35,0xbf6d3ec4,0x3f35bf35,0xbec4bf6d,0xbec4bf6d,0xbf7bbe48,0x3f6d3ec4,0x3f55bf0e,
0xbf80,0xbf35bf35,0x3f80,0x3f353f35,0x3ec4bf6d,0xbe48bf7b,0xbf6d3ec4,0xbf0e3f55,0x3f35bf35,0x3ec4bf6d,0xbf35bf35,0xbf6dbec4,0x3f6dbec4,0x3f55bf0e,0x3ec4bf6d,0x3e48bf7b,
0x3f800000,0x3f800000,0x3f800000,0x3f800000,0x3f80,0xbe483f7b,0xbf353f35,0xbf553f0e,0xbf800000,0xbf6dbec4,0xbf80,0x3ec4bf6d,0xbf80,0x3f0ebf55,0x3f353f35,0x3e483f7b,
0x3f800000,0x3f353f35,0xbf800000,0xbf35bf35,0x3f80,0xbf553f0e,0x3f35bf35,0x3f7b3e48,0xbf800000,0xbec4bf6d,0x3f80,0xbf6d3ec4,0xbf80,0x3f7bbe48,0xbf35bf35,0x3f0ebf55,
0x3f800000,0x3f80,0x3f800000,0x3f80,0x3f80,0xbf7bbe48,0xbf353f35,0xbf0ebf55,0xbf800000,0x3ec4bf6d,0xbf80,0x3f6d3ec4,0xbf80,0x3f553f0e,0x3f353f35,0xbf7b3e48,
0x3f800000,0xbf353f35,0xbf800000,0x3f35bf35,0x3f80,0xbf0ebf55,0x3f35bf35,0xbe483f7b,0xbf800000,0x3f6dbec4,0x3f80,0xbec4bf6d,0xbf80,0x3e483f7b,0xbf35bf35,0x3f553f0e,
0x3f800000,0xbf800000,0x3f800000,0xbf800000,0x3f80,0x3e48bf7b,0xbf353f35,0x3f55bf0e,0xbf800000,0x3f6d3ec4,0xbf80,0xbec43f6d,0xbf80,0xbf0e3f55,0x3f353f35,0xbe48bf7b,
0x3f800000,0xbf35bf35,0xbf800000,0x3f353f35,0x3f80,0x3f55bf0e,0x3f35bf35,0xbf7bbe48,0xbf800000,0x3ec43f6d,0x3f80,0x3f6dbec4,0xbf80,0xbf7b3e48,0xbf35bf35,0xbf0e3f55,
0x3f800000,0xbf80,0x3f800000,0xbf80,0x3f80,0x3f7b3e48,0xbf353f35,0x3f0e3f55,0xbf800000,0xbec43f6d,0xbf80,0xbf6dbec4,0xbf80,0xbf55bf0e,0x3f353f35,0x3f7bbe48,
0x3f800000,0x3f35bf35,0xbf800000,0xbf353f35,0x3f80,0x3f0e3f55,0x3f35bf35,0x3e48bf7b,0xbf800000,0xbf6d3ec4,0x3f80,0x3ec43f6d,0xbf80,0xbe48bf7b,0xbf35bf35,0xbf55bf0e,
0x3f800000,0x3f800000,0x3f800000,0x3f800000,0xbec43f6d,0xbf0e3f55,0xbf6d3ec4,0xbf7b3e48,0xbf35bf35,0xbec4bf6d,0x3f35bf35,0x3f6dbec4,0x3f6dbec4,0x3f7b3e48,0xbec43f6d,0xbf553f0e,
0x3f80,0xbf353f35,0xbf80,0x3f35bf35,0xbf6dbec4,0xbe48bf7b,0x3ec43f6d,0xbf0e3f55,0x3f35bf35,0x3f6d3ec4,0xbf35bf35,0x3ec4bf6d,0x3ec43f6d,0xbf553f0e,0x3f6d3ec4,0xbe483f7b,
0xbf800000,0xbf80,0xbf800000,0xbf80,0x3ec4bf6d,0x3f553f0e,0x3f6dbec4,0x3e483f7b,0x3f353f35,0xbf6d3ec4,0xbf353f35,0xbec4bf6d,0xbf6d3ec4,0x3e48bf7b,0x3ec4bf6d,0x3f0e3f55,
0xbf80,0x3f353f35,0x3f80,0xbf35bf35,0x3f6d3ec4,0xbf7b3e48,0xbec4bf6d,0x3f553f0e,0xbf353f35,0x3ec4bf6d,0x3f353f35,0xbf6dbec4,0xbec4bf6d,0x3f0e3f55,0xbf6dbec4,0x3f7b3e48,
0x3f800000,0xbf800000,0x3f800000,0xbf800000,0xbec43f6d,0x3f0ebf55,0xbf6d3ec4,0x3f7bbe48,0xbf35bf35,0x3ec43f6d,0x3f35bf35,0xbf6d3ec4,0x3f6dbec4,0xbf7bbe48,0xbec43f6d,0x3f55bf0e,
0x3f80,0x3f35bf35,0xbf80,0xbf353f35,0xbf6dbec4,0x3e483f7b,0x3ec43f6d,0x3f0ebf55,0x3f35bf35,0xbf6dbec4,0xbf35bf35,0xbec43f6d,0x3ec43f6d,0x3f55bf0e,0x3f6d3ec4,0x3e48bf7b,
0xbf800000,0x3f80,0xbf800000,0x3f80,0x3ec4bf6d,0xbf55bf0e,0x3f6dbec4,0xbe48bf7b,0x3f353f35,0x3f6dbec4,0xbf353f35,0x3ec43f6d,0xbf6d3ec4,0xbe483f7b,0x3ec4bf6d,0xbf0ebf55,
0xbf80,0xbf35bf35,0x3f80,0x3f353f35,0x3f6d3ec4,0x3f7bbe48,0xbec4bf6d,0xbf55bf0e,0xbf353f35,0xbec43f6d,0x3f353f35,0x3f6d3ec4,0xbec4bf6d,0xbf0ebf55,0xbf6dbec4,0xbf7bbe48,
0x3c003c00,0x3c003c00,0x3c003c00,0x3c003c00,0x3bd93c00,0x3aa73b64,0xb23e0000,0xb872b61f,0x3b643c00,0x361f39a8,0xbb64bc00,0xb61fb9a8,0x3aa73c00,0xb23e361f,0x38720000,0x3bd93b64,
0x39a83c00,0xb9a80000,0x39a83c00,0xb9a80000,0x38723c00,0xbbd9b61f,0xbaa70000,0xb23ebb64,0x361f3c00,0xbb64b9a8,0xb61fbc00,0x3b6439a8,0x323e3c00,0xb872bb64,0x3bd90000,0xbaa7361f,
0x3c00,0xbc00,0x3c00,0xbc00,0xb23e3c00,0x3872bb64,0xbbd90000,0x3aa7361f,0xb61f3c00,0x3b64b9a8,0x361fbc00,0xbb6439a8,0xb8723c00,0x3bd9b61f,0x3aa70000,0x323ebb64,
0xb9a83c00,0x39a80000,0xb9a83c00,0x39a80000,0xbaa73c00,0x323e361f,0xb8720000,0xbbd93b64,0xbb643c00,0xb61f39a8,0x3b64bc00,0x361fb9a8,0xbbd93c00,0xbaa73b64,0x323e0000,0x3872b61f,
0xbc003c00,0xbc003c00,0xbc003c00,0xbc003c00,0xbbd93c00,0xbaa73b64,0x323e0000,0x3872b61f,0xbb643c00,0xb61f39a8,0x3b64bc00,0x361fb9a8,0xbaa73c00,0x323e361f,0xb8720000,0xbbd93b64,
0xb9a83c00,0x39a80000,0xb9a83c00,0x39a80000,0xb8723c00,0x3bd9b61f,0x3aa70000,0x323ebb64,0xb61f3c00,0x3b64b9a8,0x361fbc00,0xbb6439a8,0xb23e3c00,0x3872bb64,0xbbd90000,0x3aa7361f,
0x3c00,0xbc00,0x3c00,0xbc00,0x323e3c00,0xb872bb64,0x3bd90000,0xbaa7361f,0x361f3c00,0xbb64b9a8,0xb61fbc00,0x3b6439a8,0x38723c00,0xbbd9b61f,0xbaa70000,0xb23ebb64,
0x39a83c00,0xb9a80000,0x39a83c00,0xb9a80000,0x3aa73c00,0xb23e361f,0x38720000,0x3bd93b64,0x3b643c00,0x361f39a8,0xbb64bc00,0xb61fb9a8,0x3bd93c00,0x3aa73b64,0xb23e0000,0xb872b61f,
0x3c003c00,0x3c003c00,0x3c003c00,0x3c003c00,0x387239a8,0x323e361f,0xbaa7b9a8,0xbbd9bb64,0xb61f0000,0xbb64b9a8,0x361f0000,0x3b6439a8,0xbbd9b9a8,0xb872bb64,0x323e39a8,0xbaa7b61f,
0xb9a8bc00,0x39a80000,0xb9a8bc00,0x39a80000,0x323eb9a8,0x3aa73b64,0x3bd939a8,0xb872361f,0x3b640000,0xb61f39a8,0xbb640000,0x361fb9a8,0x3aa739a8,0xbbd9b61f,0x3872b9a8,0xb23e3b64,
0x3c00,0xbc00,0x3c00,0xbc00,0xbaa739a8,0x3bd9b61f,0xb872b9a8,0x323e3b64,0xbb640000,0x361f39a8,0x3b640000,0xb61fb9a8,0xb23eb9a8,0xbaa73b64,0xbbd939a8,0x3872361f,
0x39a8bc00,0xb9a80000,0x39a8bc00,0xb9a80000,0x3bd9b9a8,0x3872bb64,0xb23e39a8,0x3aa7b61f,0x361f0000,0x3b64b9a8,0xb61f0000,0xbb6439a8,0xb87239a8,0xb23e361f,0x3aa7b9a8,0x3bd9bb64,
0xbc003c00,0xbc003c00,0xbc003c00,0xbc003c00,0xb87239a8,0xb23e361f,0x3aa7b9a8,0x3bd9bb64,0x361f0000,0x3b64b9a8,0xb61f0000,0xbb6439a8,0x3bd9b9a8,0x3872bb64,0xb23e39a8,0x3aa7b61f,
0x39a8bc00,0xb9a80000,0x39a8bc00,0xb9a80000,0xb23eb9a8,0xbaa73b64,0xbbd939a8,0x3872361f,0xbb640000,0x361f39a8,0x3b640000,0xb61fb9a8,0xbaa739a8,0x3bd9b61f,0xb872b9a8,0x323e3b64,
0x3c00,0xbc00,0x3c00,0xbc00,0x3aa739a8,0xbbd9b61f,0x3872b9a8,0xb23e3b64,0x3b640000,0xb61f39a8,0xbb640000,0x361fb9a8,0x323eb9a8,0x3aa73b64,0x3bd939a8,0xb872361f,
0xb9a8bc00,0x39a80000,0xb9a8bc00,0x39a80000,0xbbd9b9a8,0xb872bb64,0x323e39a8,0xbaa7b61f,0xb61f0000,0xbb64b9a8,0x361f0000,0x3b6439a8,0x387239a8,0x323e361f,0xbaa7b9a8,0xbbd9bb64,
0x3c003c00,0x3c003c00,0x3c003c00,0x3c003c00,0xbbd9bc00,0xbaa7bb64,0x323e0000,0x3872361f,0x3b643c00,0x361f39a8,0xbb64bc00,0xb61fb9a8,0xbaa7bc00,0x323eb61f,0xb8720000,0xbbd9bb64,
0x39a83c00,0xb9a80000,0x39a83c00,0xb9a80000,0xb872bc00,0x3bd9361f,0x3aa70000,0x323e3b64,0x361f3c00,0xbb64b9a8,0xb61fbc00,0x3b6439a8,0xb23ebc00,0x38723b64,0xbbd90000,0x3aa7b61f,
0x3c00,0xbc00,0x3c00,0xbc00,0x323ebc00,0xb8723b64,0x3bd90000,0xbaa7b61f,0xb61f3c00,0x3b64b9a8,0x361fbc00,0xbb6439a8,0x3872bc00,0xbbd9361f,0xbaa70000,0xb23e3b64,
0xb9a83c00,0x39a80000,0xb9a83c00,0x39a80000,0x3aa7bc00,0xb23eb61f,0x38720000,0x3bd9bb64,0xbb643c00,0xb61f39a8,0x3b64bc00,0x361fb9a8,0x3bd9bc00,0x3aa7bb64,0xb23e0000,0xb872361f,
0xbc003c00,0xbc003c00,0xbc003c00,0xbc003c00,0x3bd9bc00,0x3aa7bb64,0xb23e0000,0xb872361f,0xbb643c00,0xb61f39a8,0x3b64bc00,0x361fb9a8,0x3aa7bc00,0xb23eb61f,0x38720000,0x3bd9bb64,
0xb9a83c00,0x39a80000,0xb9a83c00,0x39a80000,0x3872bc00,0xbbd9361f,0xbaa70000,0xb23e3b64,0xb61f3c00,0x3b64b9a8,0x361fbc00,0xbb6439a8,0x323ebc00,0xb8723b64,0x3bd90000,0xbaa7b61f,
0x3c00,0xbc00,0x3c00,0xbc00,0xb23ebc00,0x38723b64,0xbbd90000,0x3aa7b61f,0x361f3c00,0xbb64b9a8,0xb61fbc00,0x3b6439a8,0xb872bc00,0x3bd9361f,0x3aa70000,0x323e3b64,
0x39a83c00,0xb9a80000,0x39a83c00,0xb9a80000,0xbaa7bc00,0x323eb61f,0xb8720000,0xbbd9bb64,0x3b643c00,0x361f39a8,0xbb64bc00,0xb61fb9a8,0xbbd9bc00,0xbaa7bb64,0x323e0000,0x3872361f,
0x3c003c00,0x3c003c00,0x3c003c00,0x3c003c00,0xb872b9a8,0xb23eb61f,0x3aa739a8,0x3bd93b64,0xb61f0000,0xbb64b9a8,0x361f0000,0x3b6439a8,0x3bd939a8,0x38723b64,0xb23eb9a8,0x3aa7361f,
0xb9a8bc00,0x39a80000,0xb9a8bc00,0x39a80000,0xb23e39a8,0xbaa7bb64,0xbbd9b9a8,0x3872b61f,0x3b640000,0xb61f39a8,0xbb640000,0x361fb9a8,0xbaa7b9a8,0x3bd9361f,0xb87239a8,0x323ebb64,
0x3c00,0xbc00,0x3c00,0xbc00,0x3aa7b9a8,0xbbd9361f,0x387239a8,0xb23ebb64,0xbb640000,0x361f39a8,0x3b640000,0xb61fb9a8,0x323e39a8,0x3aa7bb64,0x3bd9b9a8,0xb872b61f,
0x39a8bc00,0xb9a80000,0x39a8bc00,0xb9a80000,0xbbd939a8,0xb8723b64,0x323eb9a8,0xbaa7361f,0x361f0000,0x3b64b9a8,0xb61f0000,0xbb6439a8,0x3872b9a8,0x323eb61f,0xbaa739a8,0xbbd93b64,
0xbc003c00,0xbc003c00,0xbc003c00,0xbc003c00,0x3872b9a8,0x323eb61f,0xbaa739a8,0xbbd93b64,0x361f0000,0x3b64b9a8,0xb61f0000,0xbb6439a8,0xbbd939a8,0xb8723b64,0x323eb9a8,0xbaa7361f,
0x39a8bc00,0xb9a80000,0x39a8bc00,0xb9a80000,0x323e39a8,0x3aa7bb64,0x3bd9b9a8,0xb872b61f,0xbb640000,0x361f39a8,0x3b640000,0xb61fb9a8,0x3aa7b9a8,0xbbd9361f,0x387239a8,0xb23ebb64,
0x3c00,0xbc00,0x3c00,0xbc00,0xbaa7b9a8,0x3bd9361f,0xb87239a8,0x323ebb64,0x3b640000,0xb61f39a8,0xbb640000,0x361fb9a8,0xb23e39a8,0xbaa7bb64,0xbbd9b9a8,0x3872b61f,
0xb9a8bc00,0x39a80000,0xb9a8bc00,0x39a80000,0x3bd939a8,0x38723b64,0xb23eb9a8,0x3aa7361f,0xb61f0000,0xbb64b9a8,0x361f0000,0x3b6439a8,0xb872b9a8,0xb23eb61f,0x3aa739a8,0x3bd93b64,
0x0,0x0,0x0,0x0,0xb23e0000,0xb872b61f,0xbbd9bc00,0xbaa7bb64,0xb61f0000,0xbb64b9a8,0x361f0000,0x3b6439a8,0xb8720000,0xbbd9bb64,0x3aa73c00,0xb23e361f,
0xb9a80000,0xb9a8bc00,0xb9a80000,0xb9a8bc00,0xbaa70000,0xb23ebb64,0xb872bc00,0x3bd9361f,0xbb640000,0x361fb9a8,0x3b640000,0xb61f39a8,0xbbd90000,0x3aa7b61f,0x323e3c00,0xb872bb64,
0xbc000000,0x3c000000,0xbc000000,0x3c000000,0xbbd90000,0x3aa7361f,0x323ebc00,0xb8723b64,0xbb640000,0x361f39a8,0x3b640000,0xb61fb9a8,0xbaa70000,0xb23e3b64,0xb8723c00,0x3bd9b61f,
0xb9a80000,0xb9a83c00,0xb9a80000,0xb9a83c00,0xb8720000,0xbbd93b64,0x3aa7bc00,0xb23eb61f,0xb61f0000,0xbb6439a8,0x361f0000,0x3b64b9a8,0xb23e0000,0xb872361f,0xbbd93c00,0xbaa73b64,
0x0,0x0,0x0,0x0,0x323e0000,0x3872b61f,0x3bd9bc00,0x3aa7bb64,0x361f0000,0x3b64b9a8,0xb61f0000,0xbb6439a8,0x38720000,0x3bd9bb64,0xbaa73c00,0x323e361f,
0x39a80000,0x39a8bc00,0x39a80000,0x39a8bc00,0x3aa70000,0x323ebb64,0x3872bc00,0xbbd9361f,0x3b640000,0xb61fb9a8,0xbb640000,0x361f39a8,0x3bd90000,0xbaa7b61f,0xb23e3c00,0x3872bb64,
0x3c000000,0xbc000000,0x3c000000,0xbc000000,0x3bd90000,0xbaa7361f,0xb23ebc00,0x38723b64,0x3b640000,0xb61f39a8,0xbb640000,0x361fb9a8,0x3aa70000,0x323e3b64,0x38723c00,0xbbd9b61f,
0x39a80000,0x39a83c00,0x39a80000,0x39a83c00,0x38720000,0x3bd93b64,0xbaa7bc00,0x323eb61f,0x361f0000,0x3b6439a8,0xb61f0000,0xbb64b9a8,0x323e0000,0x3872361f,0x3bd93c00,0x3aa73b64,
0x0,0x0,0x0,0x0,0xbaa7b9a8,0xbbd9bb64,0xb872b9a8,0xb23eb61f,0xbb64bc00,0xb61fb9a8,0x3b643c00,0x361f39a8,0xb23eb9a8,0x3aa7361f,0xbbd9b9a8,0xb872bb64,
0x39a80000,0x39a83c00,0x39a80000,0x39a83c00,0x3bd939a8,0xb872361f,0xb23e39a8,0xbaa7bb64,0x361f3c00,0xbb64b9a8,0xb61fbc00,0x3b6439a8,0xb87239a8,0x323ebb64,0x3aa739a8,0xbbd9b61f,
0xbc000000,0x3c000000,0xbc000000,0x3c000000,0xb872b9a8,0x323e3b64,0x3aa7b9a8,0xbbd9361f,0x361fbc00,0xbb6439a8,0xb61f3c00,0x3b64b9a8,0x3bd9b9a8,0xb872b61f,0xb23eb9a8,0xbaa73b64,
0x39a80000,0x39a8bc00,0x39a80000,0x39a8bc00,0xb23e39a8,0x3aa7b61f,0xbbd939a8,0xb8723b64,0xbb643c00,0xb61f39a8,0x3b64bc00,0x361fb9a8,0xbaa739a8,0xbbd93b64,0xb87239a8,0xb23e361f,
0x0,0x0,0x0,0x0,0x3aa7b9a8,0x3bd9bb64,0x3872b9a8,0x323eb61f,0x3b64bc00,0x361fb9a8,0xbb643c00,0xb61f39a8,0x323eb9a8,0xbaa7361f,0x3bd9b9a8,0x3872bb64,
0xb9a80000,0xb9a83c00,0xb9a80000,0xb9a83c00,0xbbd939a8,0x3872361f,0x323e39a8,0x3aa7bb64,0xb61f3c00,0x3b64b9a8,0x361fbc00,0xbb6439a8,0x387239a8,0xb23ebb64,0xbaa739a8,0x3bd9b61f,
0x3c000000,0xbc000000,0x3c000000,0xbc000000,0x3872b9a8,0xb23e3b64,0xbaa7b9a8,0x3bd9361f,0xb61fbc00,0x3b6439a8,0x361f3c00,0xbb64b9a8,0xbbd9b9a8,0x3872b61f,0x323eb9a8,0x3aa73b64,
0xb9a80000,0xb9a8bc00,0xb9a80000,0xb9a8bc00,0x323e39a8,0xbaa7b61f,0x3bd939a8,0x38723b64,0x3b643c00,0x361f39a8,0xbb64bc00,0xb61fb9a8,0x3aa739a8,0x3bd93b64,0x387239a8,0x323e361f,
0x0,0x0,0x0,0x0,0x323e0000,0x3872361f,0x3bd93c00,0x3aa73b64,0xb61f0000,0xbb64b9a8,0x361f0000,0x3b6439a8,0x38720000,0x3bd93b64,0xbaa7bc00,0x323eb61f,
0xb9a80000,0xb9a8bc00,0xb9a80000,0xb9a8bc00,0x3aa70000,0x323e3b64,0x38723c00,0xbbd9b61f,0xbb640000,0x361fb9a8,0x3b640000,0xb61f39a8,0x3bd90000,0xbaa7361f,0xb23ebc00,0x38723b64,
0xbc000000,0x3c000000,0xbc000000,0x3c000000,0x3bd90000,0xbaa7b61f,0xb23e3c00,0x3872bb64,0xbb640000,0x361f39a8,0x3b640000,0xb61fb9a8,0x3aa70000,0x323ebb64,0x3872bc00,0xbbd9361f,
0xb9a80000,0xb9a83c00,0xb9a80000,0xb9a83c00,0x38720000,0x3bd9bb64,0xbaa73c00,0x323e361f,0xb61f0000,0xbb6439a8,0x361f0000,0x3b64b9a8,0x323e0000,0x3872b61f,0x3bd9bc00,0x3aa7bb64,
0x0,0x0,0x0,0x0,0xb23e0000,0xb872361f,0xbbd93c00,0xbaa73b64,0x361f0000,0x3b64b9a8,0xb61f0000,0xbb6439a8,0xb8720000,0xbbd93b64,0x3aa7bc00,0xb23eb61f,
0x39a80000,0x39a8bc00,0x39a80000,0x39a8bc00,0xbaa70000,0xb23e3b64,0xb8723c00,0x3bd9b61f,0x3b640000,0xb61fb9a8,0xbb640000,0x361f39a8,0xbbd90000,0x3aa7361f,0x323ebc00,0xb8723b64,
0x3c000000,0xbc000000,0x3c000000,0xbc000000,0xbbd90000,0x3aa7b61f,0x323e3c00,0xb872bb64,0x3b640000,0xb61f39a8,0xbb640000,0x361fb9a8,0xbaa70000,0xb23ebb64,0xb872bc00,0x3bd9361f,
0x39a80000,0x39a83c00,0x39a80000,0x39a83c00,0xb8720000,0xbbd9bb64,0x3aa73c00,0xb23e361f,0x361f0000,0x3b6439a8,0xb61f0000,0xbb64b9a8,0xb23e0000,0xb872b61f,0xbbd9bc00,0xbaa7bb64,
0x0,0x0,0x0,0x0,0x3aa739a8,0x3bd93b64,0x387239a8,0x323e361f,0xbb64bc00,0xb61fb9a8,0x3b643c00,0x361f39a8,0x323e39a8,0xbaa7b61f,0x3bd939a8,0x38723b64,
0x39a80000,0x39a83c00,0x39a80000,0x39a83c00,0xbbd9b9a8,0x3872b61f,0x323eb9a8,0x3aa73b64,0x361f3c00,0xbb64b9a8,0xb61fbc00,0x3b6439a8,0x3872b9a8,0xb23e3b64,0xbaa7b9a8,0x3bd9361f,
0xbc000000,0x3c000000,0xbc000000,0x3c000000,0x387239a8,0xb23ebb64,0xbaa739a8,0x3bd9b61f,0x361fbc00,0xbb6439a8,0xb61f3c00,0x3b64b9a8,0xbbd939a8,0x3872361f,0x323e39a8,0x3aa7bb64,
0x39a80000,0x39a8bc00,0x39a80000,0x39a8bc00,0x323eb9a8,0xbaa7361f,0x3bd9b9a8,0x3872bb64,0xbb643c00,0xb61f39a8,0x3b64bc00,0x361fb9a8,0x3aa7b9a8,0x3bd9bb64,0x3872b9a8,0x323eb61f,
0x0,0x0,0x0,0x0,0xbaa739a8,0xbbd93b64,0xb87239a8,0xb23e361f,0x3b64bc00,0x361fb9a8,0xbb643c00,0xb61f39a8,0xb23e39a8,0x3aa7b61f,0xbbd939a8,0xb8723b64,
0xb9a80000,0xb9a83c00,0xb9a80000,0xb9a83c00,0x3bd9b9a8,0xb872b61f,0xb23eb9a8,0xbaa73b64,0xb61f3c00,0x3b64b9a8,0x361fbc00,0xbb6439a8,0xb872b9a8,0x323e3b64,0x3aa7b9a8,0xbbd9361f,
0x3c000000,0xbc000000,0x3c000000,0xbc000000,0xb87239a8,0x323ebb64,0x3aa739a8,0xbbd9b61f,0xb61fbc00,0x3b6439a8,0x361f3c00,0xbb64b9a8,0x3bd939a8,0xb872361f,0xb23e39a8,0xbaa7bb64,
0xb9a80000,0xb9a8bc00,0xb9a80000,0xb9a8bc00,0xb23eb9a8,0x3aa7361f,0xbbd9b9a8,0xb872bb64,0x3b643c00,0x361f39a8,0xbb64bc00,0xb61fb9a8,0xbaa7b9a8,0xbbd9bb64,0xb872b9a8,0xb23eb61f,
0x3c003c00,0x3c003c00,0x3c003c00,0x3c003c00,0x0,0x0,0x0,0x0,0x3bd93c00,0x3aa73b64,0xb23e0000,0xb872b61f,0xb23e0000,0xb872b61f,0xbbd9bc00,0xbaa7bb64,
0x3b643c00,0x361f39a8,0xbb64bc00,0xb61fb9a8,0xb61f0000,0xbb64b9a8,0x361f0000,0x3b6439a8,0x3aa73c00,0xb23e361f,0x38720000,0x3bd93b64,0xb8720000,0xbbd9bb64,0x3aa73c00,0xb23e361f,
0x39a83c00,0xb9a80000,0x39a83c00,0xb9a80000,0xb9a80000,0xb9a8bc00,0xb9a80000,0xb9a8bc00,0x38723c00,0xbbd9b61f,0xbaa70000,0xb23ebb64,0xbaa70000,0xb23ebb64,0xb872bc00,0x3bd9361f,
0x361f3c00,0xbb64b9a8,0xb61fbc00,0x3b6439a8,0xbb640000,0x361fb9a8,0x3b640000,0xb61f39a8,0x323e3c00,0xb872bb64,0x3bd90000,0xbaa7361f,0xbbd90000,0x3aa7b61f,0x323e3c00,0xb872bb64,
0x3c00,0xbc00,0x3c00,0xbc00,0xbc000000,0x3c000000,0xbc000000,0x3c000000,0xb23e3c00,0x3872bb64,0xbbd90000,0x3aa7361f,0xbbd90000,0x3aa7361f,0x323ebc00,0xb8723b64,
0xb61f3c00,0x3b64b9a8,0x361fbc00,0xbb6439a8,0xbb640000,0x361f39a8,0x3b640000,0xb61fb9a8,0xb8723c00,0x3bd9b61f,0x3aa70000,0x323ebb64,0xbaa70000,0xb23e3b64,0xb8723c00,0x3bd9b61f,
0xb9a83c00,0x39a80000,0xb9a83c00,0x39a80000,0xb9a80000,0xb9a83c00,0xb9a80000,0xb9a83c00,0xbaa73c00,0x323e361f,0xb8720000,0xbbd93b64,0xb8720000,0xbbd93b64,0x3aa7bc00,0xb23eb61f,
0xbb643c00,0xb61f39a8,0x3b64bc00,0x361fb9a8,0xb61f0000,0xbb6439a8,0x361f0000,0x3b64b9a8,0xbbd93c00,0xbaa73b64,0x323e0000,0x3872b61f,0xb23e0000,0xb872361f,0xbbd93c00,0xbaa73b64,
0x3c003c00,0x3c003c00,0x3c003c00,0x3c003c00,0x0,0x0,0x0,0x0,0x387239a8,0x323e361f,0xbaa7b9a8,0xbbd9bb64,0xbaa7b9a8,0xbbd9bb64,0xb872b9a8,0xb23eb61f,
0xb61f0000,0xbb64b9a8,0x361f0000,0x3b6439a8,0xbb64bc00,0xb61fb9a8,0x3b643c00,0x361f39a8,0xbbd9b9a8,0xb872bb64,0x323e39a8,0xbaa7b61f,0xb23eb9a8,0x3aa7361f,0xbbd9b9a8,0xb872bb64,
0xb9a8bc00,0x39a80000,0xb9a8bc00,0x39a80000,0x39a80000,0x39a83c00,0x39a80000,0x39a83c00,0x323eb9a8,0x3aa73b64,0x3bd939a8,0xb872361f,0x3bd939a8,0xb872361f,0xb23e39a8,0xbaa7bb64,
0x3b640000,0xb61f39a8,0xbb640000,0x361fb9a8,0x361f3c00,0xbb64b9a8,0xb61fbc00,0x3b6439a8,0x3aa739a8,0xbbd9b61f,0x3872b9a8,0xb23e3b64,0xb87239a8,0x323ebb64,0x3aa739a8,0xbbd9b61f,
0x3c00,0xbc00,0x3c00,0xbc00,0xbc000000,0x3c000000,0xbc000000,0x3c000000,0xbaa739a8,0x3bd9b61f,0xb872b9a8,0x323e3b64,0xb872b9a8,0x323e3b64,0x3aa7b9a8,0xbbd9361f,
0xbb640000,0x361f39a8,0x3b640000,0xb61fb9a8,0x361fbc00,0xbb6439a8,0xb61f3c00,0x3b64b9a8,0xb23eb9a8,0xbaa73b64,0xbbd939a8,0x3872361f,0x3bd9b9a8,0xb872b61f,0xb23eb9a8,0xbaa73b64,
0x39a8bc00,0xb9a80000,0x39a8bc00,0xb9a80000,0x39a80000,0x39a8bc00,0x39a80000,0x39a8bc00,0x3bd9b9a8,0x3872bb64,0xb23e39a8,0x3aa7b61f,0xb23e39a8,0x3aa7b61f,0xbbd939a8,0xb8723b64,
0x361f0000,0x3b64b9a8,0xb61f0000,0xbb6439a8,0xbb643c00,0xb61f39a8,0x3b64bc00,0x361fb9a8,0xb87239a8,0xb23e361f,0x3aa7b9a8,0x3bd9bb64,0xbaa739a8,0xbbd93b64,0xb87239a8,0xb23e361f,
0x3c003c00,0x3c003c00,0x3c003c00,0x3c003c00,0x0,0x0,0x0,0x0,0xbbd9bc00,0xbaa7bb64,0x323e0000,0x3872361f,0x323e0000,0x3872361f,0x3bd93c00,0x3aa73b64,
0x3b643c00,0x361f39a8,0xbb64bc00,0xb61fb9a8,0xb61f0000,0xbb64b9a8,0x361f0000,0x3b6439a8,0xbaa7bc00,0x323eb61f,0xb8720000,0xbbd9bb64,0x38720000,0x3bd93b64,0xbaa7bc00,0x323eb61f,
0x39a83c00,0xb9a80000,0x39a83c00,0xb9a80000,0xb9a80000,0xb9a8bc00,0xb9a80000,0xb9a8bc00,0xb872bc00,0x3bd9361f,0x3aa70000,0x323e3b64,0x3aa70000,0x323e3b64,0x38723c00,0xbbd9b61f,
0x361f3c00,0xbb64b9a8,0xb61fbc00,0x3b6439a8,0xbb640000,0x361fb9a8,0x3b640000,0xb61f39a8,0xb23ebc00,0x38723b64,0xbbd90000,0x3aa7b61f,0x3bd90000,0xbaa7361f,0xb23ebc00,0x38723b64,
0x3c00,0xbc00,0x3c00,0xbc00,0xbc000000,0x3c000000,0xbc000000,0x3c000000,0x323ebc00,0xb8723b64,0x3bd90000,0xbaa7b61f,0x3bd90000,0xbaa7b61f,0xb23e3c00,0x3872bb64,
0xb61f3c00,0x3b64b9a8,0x361fbc00,0xbb6439a8,0xbb640000,0x361f39a8,0x3b640000,0xb61fb9a8,0x3872bc00,0xbbd9361f,0xbaa70000,0xb23e3b64,0x3aa70000,0x323ebb64,0x3872bc00,0xbbd9361f,
0xb9a83c00,0x39a80000,0xb9a83c00,0x39a80000,0xb9a80000,0xb9a83c00,0xb9a80000,0xb9a83c00,0x3aa7bc00,0xb23eb61f,0x38720000,0x3bd9bb64,0x38720000,0x3bd9bb64,0xbaa73c00,0x323e361f,
0xbb643c00,0xb61f39a8,0x3b64bc00,0x361fb9a8,0xb61f0000,0xbb6439a8,0x361f0000,0x3b64b9a8,0x3bd9bc00,0x3aa7bb64,0xb23e0000,0xb872361f,0x323e0000,0x3872b61f,0x3bd9bc00,0x3aa7bb64,
0x3c003c00,0x3c003c00,0x3c003c00,0x3c003c00,0x0,0x0,0x0,0x0,0xb872b9a8,0xb23eb61f,0x3aa739a8,0x3bd93b64,0x3aa739a8,0x3bd93b64,0x387239a8,0x323e361f,
0xb61f0000,0xbb64b9a8,0x361f0000,0x3b6439a8,0xbb64bc00,0xb61fb9a8,0x3b643c00,0x361f39a8,0x3bd939a8,0x38723b64,0xb23eb9a8,0x3aa7361f,0x323e39a8,0xbaa7b61f,0x3bd939a8,0x38723b64,
0xb9a8bc00,0x39a80000,0xb9a8bc00,0x39a80000,0x39a80000,0x39a83c00,0x39a80000,0x39a83c00,0xb23e39a8,0xbaa7bb64,0xbbd9b9a8,0x3872b61f,0xbbd9b9a8,0x3872b61f,0x323eb9a8,0x3aa73b64,
0x3b640000,0xb61f39a8,0xbb640000,0x361fb9a8,0x361f3c00,0xbb64b9a8,0xb61fbc00,0x3b6439a8,0xbaa7b9a8,0x3bd9361f,0xb87239a8,0x323ebb64,0x3872b9a8,0xb23e3b64,0xbaa7b9a8,0x3bd9361f,
0x3c00,0xbc00,0x3c00,0xbc00,0xbc000000,0x3c000000,0xbc000000,0x3c000000,0x3aa7b9a8,0xbbd9361f,0x387239a8,0xb23ebb64,0x387239a8,0xb23ebb64,0xbaa739a8,0x3bd9b61f,
0xbb640000,0x361f39a8,0x3b640000,0xb61fb9a8,0x361fbc00,0xbb6439a8,0xb61f3c00,0x3b64b9a8,0x323e39a8,0x3aa7bb64,0x3bd9b9a8,0xb872b61f,0xbbd939a8,0x3872361f,0x323e39a8,0x3aa7bb64,
0x39a8bc00,0xb9a80000,0x39a8bc00,0xb9a80000,0x39a80000,0x39a8bc00,0x39a80000,0x39a8bc00,0xbbd939a8,0xb8723b64,0x323eb9a8,0xbaa7361f,0x323eb9a8,0xbaa7361f,0x3bd9b9a8,0x3872bb64,
0x361f0000,0x3b64b9a8,0xb61f0000,0xbb6439a8,0xbb643c00,0xb61f39a8,0x3b64bc00,0x361fb9a8,0x3872b9a8,0x323eb61f,0xbaa739a8,0xbbd93b64,0x3aa7b9a8,0x3bd9bb64,0x3872b9a8,0x323eb61f,
0x0,0x0,0x0,0x0,0x3c003c00,0x3c003c00,0x3c003c00,0x3c003c00,0x323e0000,0x3872361f,0x3bd93c00,0x3aa73b64,0x3bd93c00,0x3aa73b64,0xb23e0000,0xb872b61f,
0x361f0000,0x3b6439a8,0xb61f0000,0xbb64b9a8,0x3b643c00,0x361f39a8,0xbb64bc00,0xb61fb9a8,0x38720000,0x3bd93b64,0xbaa7bc00,0x323eb61f,0x3aa73c00,0xb23e361f,0x38720000,0x3bd93b64,
0x39a80000,0x39a83c00,0x39a80000,0x39a83c00,0x39a83c00,0xb9a80000,0x39a83c00,0xb9a80000,0x3aa70000,0x323e3b64,0x38723c00,0xbbd9b61f,0x38723c00,0xbbd9b61f,0xbaa70000,0xb23ebb64,
0x3b640000,0xb61f39a8,0xbb640000,0x361fb9a8,0x361f3c00,0xbb64b9a8,0xb61fbc00,0x3b6439a8,0x3bd90000,0xbaa7361f,0xb23ebc00,0x38723b64,0x323e3c00,0xb872bb64,0x3bd90000,0xbaa7361f,
0x3c000000,0xbc000000,0x3c000000,0xbc000000,0x3c00,0xbc00,0x3c00,0xbc00,0x3bd90000,0xbaa7b61f,0xb23e3c00,0x3872bb64,0xb23e3c00,0x3872bb64,0xbbd90000,0x3aa7361f,
0x3b640000,0xb61fb9a8,0xbb640000,0x361f39a8,0xb61f3c00,0x3b64b9a8,0x361fbc00,0xbb6439a8,0x3aa70000,0x323ebb64,0x3872bc00,0xbbd9361f,0xb8723c00,0x3bd9b61f,0x3aa70000,0x323ebb64,
0x39a80000,0x39a8bc00,0x39a80000,0x39a8bc00,0xb9a83c00,0x39a80000,0xb9a83c00,0x39a80000,0x38720000,0x3bd9bb64,0xbaa73c00,0x323e361f,0xbaa73c00,0x323e361f,0xb8720000,0xbbd93b64,
0x361f0000,0x3b64b9a8,0xb61f0000,0xbb6439a8,0xbb643c00,0xb61f39a8,0x3b64bc00,0x361fb9a8,0x323e0000,0x3872b61f,0x3bd9bc00,0x3aa7bb64,0xbbd93c00,0xbaa73b64,0x323e0000,0x3872b61f,
0x0,0x0,0x0,0x0,0x3c003c00,0x3c003c00,0x3c003c00,0x3c003c00,0x3aa739a8,0x3bd93b64,0x387239a8,0x323e361f,0x387239a8,0x323e361f,0xbaa7b9a8,0xbbd9bb64,
0x3b643c00,0x361f39a8,0xbb64bc00,0xb61fb9a8,0xb61f0000,0xbb64b9a8,0x361f0000,0x3b6439a8,0x323e39a8,0xbaa7b61f,0x3bd939a8,0x38723b64,0xbbd9b9a8,0xb872bb64,0x323e39a8,0xbaa7b61f,
0xb9a80000,0xb9a8bc00,0xb9a80000,0xb9a8bc00,0xb9a8bc00,0x39a80000,0xb9a8bc00,0x39a80000,0xbbd9b9a8,0x3872b61f,0x323eb9a8,0x3aa73b64,0x323eb9a8,0x3aa73b64,0x3bd939a8,0xb872361f,
0xb61fbc00,0x3b6439a8,0x361f3c00,0xbb64b9a8,0x3b640000,0xb61f39a8,0xbb640000,0x361fb9a8,0x3872b9a8,0xb23e3b64,0xbaa7b9a8,0x3bd9361f,0x3aa739a8,0xbbd9b61f,0x3872b9a8,0xb23e3b64,
0x3c000000,0xbc000000,0x3c000000,0xbc000000,0x3c00,0xbc00,0x3c00,0xbc00,0x387239a8,0xb23ebb64,0xbaa739a8,0x3bd9b61f,0xbaa739a8,0x3bd9b61f,0xb872b9a8,0x323e3b64,
0xb61f3c00,0x3b64b9a8,0x361fbc00,0xbb6439a8,0xbb640000,0x361f39a8,0x3b640000,0xb61fb9a8,0xbbd939a8,0x3872361f,0x323e39a8,0x3aa7bb64,0xb23eb9a8,0xbaa73b64,0xbbd939a8,0x3872361f,
0xb9a80000,0xb9a83c00,0xb9a80000,0xb9a83c00,0x39a8bc00,0xb9a80000,0x39a8bc00,0xb9a80000,0x323eb9a8,0xbaa7361f,0x3bd9b9a8,0x3872bb64,0x3bd9b9a8,0x3872bb64,0xb23e39a8,0x3aa7b61f,
0x3b64bc00,0x361fb9a8,0xbb643c00,0xb61f39a8,0x361f0000,0x3b64b9a8,0xb61f0000,0xbb6439a8,0x3aa7b9a8,0x3bd9bb64,0x3872b9a8,0x323eb61f,0xb87239a8,0xb23e361f,0x3aa7b9a8,0x3bd9bb64,
0x0,0x0,0x0,0x0,0x3c003c00,0x3c003c00,0x3c003c00,0x3c003c00,0xb23e0000,0xb872b61f,0xbbd9bc00,0xbaa7bb64,0xbbd9bc00,0xbaa7bb64,0x323e0000,0x3872361f,
0x361f0000,0x3b6439a8,0xb61f0000,0xbb64b9a8,0x3b643c00,0x361f39a8,0xbb64bc00,0xb61fb9a8,0xb8720000,0xbbd9bb64,0x3aa73c00,0xb23e361f,0xbaa7bc00,0x323eb61f,0xb8720000,0xbbd9bb64,
0x39a80000,0x39a83c00,0x39a80000,0x39a83c00,0x39a83c00,0xb9a80000,0x39a83c00,0xb9a80000,0xbaa70000,0xb23ebb64,0xb872bc00,0x3bd9361f,0xb872bc00,0x3bd9361f,0x3aa70000,0x323e3b64,
0x3b640000,0xb61f39a8,0xbb640000,0x361fb9a8,0x361f3c00,0xbb64b9a8,0xb61fbc00,0x3b6439a8,0xbbd90000,0x3aa7b61f,0x323e3c00,0xb872bb64,0xb23ebc00,0x38723b64,0xbbd90000,0x3aa7b61f,
0x3c000000,0xbc000000,0x3c000000,0xbc000000,0x3c00,0xbc00,0x3c00,0xbc00,0xbbd90000,0x3aa7361f,0x323ebc00,0xb8723b64,0x323ebc00,0xb8723b64,0x3bd90000,0xbaa7b61f,
0x3b640000,0xb61fb9a8,0xbb640000,0x361f39a8,0xb61f3c00,0x3b64b9a8,0x361fbc00,0xbb6439a8,0xbaa70000,0xb23e3b64,0xb8723c00,0x3bd9b61f,0x3872bc00,0xbbd9361f,0xbaa70000,0xb23e3b64,
0x39a80000,0x39a8bc00,0x39a80000,0x39a8bc00,0xb9a83c00,0x39a80000,0xb9a83c00,0x39a80000,0xb8720000,0xbbd93b64,0x3aa7bc00,0xb23eb61f,0x3aa7bc00,0xb23eb61f,0x38720000,0x3bd9bb64,
0x361f0000,0x3b64b9a8,0xb61f0000,0xbb6439a8,0xbb643c00,0xb61f39a8,0x3b64bc00,0x361fb9a8,0xb23e0000,0xb872361f,0xbbd93c00,0xbaa73b64,0x3bd9bc00,0x3aa7bb64,0xb23e0000,0xb872361f,
0x0,0x0,0x0,0x0,0x3c003c00,0x3c003c00,0x3c003c00,0x3c003c00,0xbaa7b9a8,0xbbd9bb64,0xb872b9a8,0xb23eb61f,0xb872b9a8,0xb23eb61f,0x3aa739a8,0x3bd93b64,
0x3b643c00,0x361f39a8,0xbb64bc00,0xb61fb9a8,0xb61f0000,0xbb64b9a8,0x361f0000,0x3b6439a8,0xb23eb9a8,0x3aa7361f,0xbbd9b9a8,0xb872bb64,0x3bd939a8,0x38723b64,0xb23eb9a8,0x3aa7361f,
0xb9a80000,0xb9a8bc00,0xb9a80000,0xb9a8bc00,0xb9a8bc00,0x39a80000,0xb9a8bc00,0x39a80000,0x3bd939a8,0xb872361f,0xb23e39a8,0xbaa7bb64,0xb23e39a8,0xbaa7bb64,0xbbd9b9a8,0x3872b61f,
0xb61fbc00,0x3b6439a8,0x361f3c00,0xbb64b9a8,0x3b640000,0xb61f39a8,0xbb640000,0x361fb9a8,0xb87239a8,0x323ebb64,0x3aa739a8,0xbbd9b61f,0xbaa7b9a8,0x3bd9361f,0xb87239a8,0x323ebb64,
0x3c000000,0xbc000000,0x3c000000,0xbc000000,0x3c00,0xbc00,0x3c00,0xbc00,0xb872b9a8,0x323e3b64,0x3aa7b9a8,0xbbd9361f,0x3aa7b9a8,0xbbd9361f,0x387239a8,0xb23ebb64,
0xb61f3c00,0x3b64b9a8,0x361fbc00,0xbb6439a8,0xbb640000,0x361f39a8,0x3b640000,0xb61fb9a8,0x3bd9b9a8,0xb872b61f,0xb23eb9a8,0xbaa73b64,0x323e39a8,0x3aa7bb64,0x3bd9b9a8,0xb872b61f,
0xb9a80000,0xb9a83c00,0xb9a80000,0xb9a83c00,0x39a8bc00,0xb9a80000,0x39a8bc00,0xb9a80000,0xb23e39a8,0x3aa7b61f,0xbbd939a8,0xb8723b64,0xbbd939a8,0xb8723b64,0x323eb9a8,0xbaa7361f,
0x3b64bc00,0x361fb9a8,0xbb643c00,0xb61f39a8,0x361f0000,0x3b64b9a8,0xb61f0000,0xbb6439a8,0xbaa739a8,0xbbd93b64,0xb87239a8,0xb23e361f,0x3872b9a8,0x323eb61f,0xbaa739a8,0xbbd93b64};

__global__ __launch_bounds__(256, 1) __attribute__((aligned(4096))) void k_fused(const void* x, const void* wr, const void* wi, const void* w1, const void* b1, const void* w2, const void* b2, void* ws, void* out) {
  const unsigned long long ka = (unsigned long long)__builtin_amdgcn_kernarg_segment_ptr(), tb = (unsigned long long)k_tab;
  asm volatile(
    ".p2align 5\n"
    "s_load_dwordx2 s[20:21], s[98:99], 0\n"
    "s_load_dwordx2 s[24:25], s[98:99], 64\n"
    "s_load_dwordx2 s[52:53], s[98:99], 8\n"
    "s_load_dwordx2 s[56:57], s[98:99], 16\n"
    "s_load_dwordx2 s[8:9], s[98:99], 24\n"
    "s_load_dwordx2 s[16:17], s[98:99], 56\n"
    "s_load_dwordx2 s[28:29], s[98:99], 32\n"
    "s_getpc_b64 s[72:73]\n"
    "LBEG_%=:\n"
    "s_mov_b32 s74, LEND_%=-LBEG_%=\n"
    "s_mov_b32 s75, 0x20000\n"
    "v_lshlrev_b32 v36, 3, v0\n"
    "s_lshr_b32 s84, s100, 3\n"
    "s_lshl_b32 s84, s84, 11\n"
    "buffer_load_dword v35, v36, s[72:75], s84 offen\n"
    "v_readfirstlane_b32 s48, v0\n"
    "v_and_b32 v30, 63, v0\n"
    "s_mov_b32 s22, 4194304\n"
    "s_mov_b32 s23, 0x20000\n"
    "s_lshr_b32 s48, s48, 6\n"
    "s_mov_b32 s70, 24576\n"
    "s_mov_b32 s71, 0x20000\n"
    "s_lshl_b32 s36, s48, 10\n"
    "v_lshlrev_b32 v41, 4, v30\n"
    "v_add_u32 v31, s36, v41\n"
    "s_add_u32 m0, s36, 87040\n"
    "s_mov_b32 s84, 16384\n"
    "buffer_load_dwordx4 v31, s[68:71], s84 offen lds\n"
    "s_add_u32 m0, s36, 91136\n"
    "s_mov_b32 s84, 20480\n"
    "buffer_load_dwordx4 v31, s[68:71], s84 offen lds\n"
    "s_add_u32 m0, s36, 131072\n"
    "s_mov_b32 s84, 0\n"
    "buffer_load_dwordx4 v31, s[68:71], s84 offen lds\n"
    "s_add_u32 m0, s36, 135168\n"
    "s_mov_b32 s84, 4096\n"
    "buffer_load_dwordx4 v31, s[68:71], s84 offen lds\n"
    "s_add_u32 m0, s36, 139264\n"
    "s_mov_b32 s84, 8192\n"
    "buffer_load_dwordx4 v31, s[68:71], s84 offen lds\n"
    "s_add_u32 m0, s36, 143360\n"
    "s_mov_b32 s84, 12288\n"
    "buffer_load_dwordx4 v31, s[68:71], s84 offen lds\n"
    "s_and_b32 s60, s100, 7\n"
    "s_lshl_b32 s60, s60, 7\n"
    "s_lshr_b32 s86, s100, 3\n"
    "s_add_u32 s60, s60, s86\n"
    "s_lshl_b32 s60, s60, 11\n"
    "v_bfe_u32 v32, v30, 3, 2\n"
    "v_lshlrev_b32 v32, 5, v32\n"
    "v_lshrrev_b32 v59, 5, v30\n"
    "v_lshl_add_u32 v32, v59, 10, v32\n"
    "s_lshl_b32 s86, s48, 9\n"
    "v_lshl_add_u32 v32, v32, 11, s86\n"
    "v_lshrrev_b32 v59, 3, v30\n"
    "v_and_b32 v59, 6, v59\n"
    "v_and_b32 v58, 7, v30\n"
    "v_xor_b32_e64 v59, v58, v59\n"
    "v_lshl_add_u32 v32, v59, 4, v32\n"
    "s_lshl_b32 s83, s48, 12\n"
    "s_add_u32 s83, s83, 0x24000\n"
    "v_and_b32 v34, 3, v30\n"
    "v_lshlrev_b32 v34, 5, v34\n"
    "v_and_b32 v59, 4, v30\n"
    "v_lshl_add_u32 v34, v59, 8, v34\n"
    "v_lshlrev_b32 v34, 11, v34\n"
    "s_add_u32 m0, s83, 0\n"
    "s_waitcnt lgkmcnt(0)\n"
    "buffer_load_dwordx4 v32, s[20:23], s60 offen offset:0 lds\n"
    "s_add_u32 m0, s83, 896\n"
    "s_nop 0\n"
    "buffer_load_dwordx4 v32, s[20:23], s60 offen offset:128 lds\n"
    "s_add_u32 m0, s83, 1792\n"
    "s_nop 0\n"
    "buffer_load_dwordx4 v32, s[20:23], s60 offen offset:256 lds\n"
    "s_add_u32 m0, s83, 2688\n"
    "s_nop 0\n"
    "buffer_load_dwordx4 v32, s[20:23], s60 offen offset:384 lds\n"
    "buffer_load_ushort v80, v34, s[20:23], s60 offen\n"
    "s_load_dwordx2 s[12:13], s[98:99], 40\n"
    "s_load_dwordx2 s[32:33], s[98:99], 48\n"
    "s_mov_b32 s54, 2101248\n"
    "s_mov_b32 s55, 0x20000\n"
    "s_mov_b32 s58, 2101248\n"
    "s_mov_b32 s59, 0x20000\n"
    "s_bfe_u32 s79, s100, 0x30003\n"
    "s_lshl_b32 s79, s79, 3\n"
    "s_bfe_u32 s86, s100, 0x10006\n"
    "s_add_u32 s79, s79, s86\n"
    "s_lshl_b32 s86, s48, 1\n"
    "s_add_u32 s79, s79, s86\n"
    "s_mul_i32 s79, s79, 0x402\n"
    "s_and_b32 s79, s79, 0xffffffc0\n"
    "s_lshr_b32 s86, s100, 7\n"
    "s_lshl_b32 s86, s86, 9\n"
    "s_add_u32 s79, s79, s86\n"
    "s_and_b32 s86, s100, 7\n"
    "s_mul_i32 s86, s86, 0x40200\n"
    "s_add_u32 s79, s79, s86\n"
    "s_mul_i32 s63, s48, 0x240\n"
    "s_add_u32 s63, s63, 0x1b800\n"
    "s_add_u32 m0, s63, 0\n"
    "s_add_u32 s84, s79, 0\n"
    "s_mov_b32 exec_hi, 15\n"
    "buffer_load_dwordx4 v41, s[52:55], s84 offen lds\n"
    "s_mov_b32 exec_hi, -1\n"
    "s_add_u32 m0, s63, 2304\n"
    "s_add_u32 s84, s79, 65664\n"
    "s_mov_b32 exec_hi, 15\n"
    "buffer_load_dwordx4 v41, s[52:55], s84 offen lds\n"
    "s_mov_b32 exec_hi, -1\n"
    "s_add_u32 m0, s63, 4608\n"
    "s_add_u32 s84, s79, 131328\n"
    "s_mov_b32 exec_hi, 15\n"
    "buffer_load_dwordx4 v41, s[52:55], s84 offen lds\n"
    "s_mov_b32 exec_hi, -1\n"
    "s_add_u32 m0, s63, 6912\n"
    "s_add_u32 s84, s79, 196992\n"
    "s_mov_b32 exec_hi, 15\n"
    "buffer_load_dwordx4 v41, s[52:55], s84 offen lds\n"
    "s_mov_b32 exec_hi, -1\n"
    "s_add_u32 m0, s63, 9216\n"
    "s_add_u32 s84, s79, 0\n"
    "s_mov_b32 exec_hi, 15\n"
    "buffer_load_dwordx4 v41, s[56:59], s84 offen lds\n"
    "s_mov_b32 exec_hi, -1\n"
    "s_add_u32 m0, s63, 11520\n"
    "s_add_u32 s84, s79, 65664\n"
    "s_mov_b32 exec_hi, 15\n"
    "buffer_load_dwordx4 v41, s[56:59], s84 offen lds\n"
    "s_mov_b32 exec_hi, -1\n"
    "s_add_u32 m0, s63, 13824\n"
    "s_add_u32 s84, s79, 131328\n"
    "s_mov_b32 exec_hi, 15\n"
    "buffer_load_dwordx4 v41, s[56:59], s84 offen lds\n"
    "s_mov_b32 exec_hi, -1\n"
    "s_add_u32 m0, s63, 16128\n"
    "s_add_u32 s84, s79, 196992\n"
    "s_mov_b32 exec_hi, 15\n"
    "buffer_load_dwordx4 v41, s[56:59], s84 offen lds\n"
    "s_mov_b32 exec_hi, -1\n"
    "s_mov_b32 s18, 51380224\n"
    "s_mov_b32 s19, 0x20000\n"
    "s_mov_b32 s10, 7340032\n"
    "s_mov_b32 s11, 0x20000\n"
    "s_mov_b32 s84, 12648448\n"
    "buffer_load_dwordx2 v[60:61], off, s[16:19], s84 sc1\n"
    "s_bfe_u32 s76, s100, 0x20001\n"
    "s_and_b32 s77, s100, 1\n"
    "s_lshl_b32 s77, s77, 3\n"
    "s_bfe_u32 s86, s100, 0x30003\n"
    "s_add_u32 s77, s77, s86\n"
    "s_lshr_b32 s78, s100, 6\n"
    "v_lshrrev_b32_e64 v54, 4, v30\n"
    "v_bfe_u32 v55, v30, 2, 2\n"
    "v_and_b32 v56, 3, v30\n"
    "v_and_b32 v36, 7, v30\n"
    "v_lshl_add_u32 v36, v36, 7, s83\n"
    "v_and_b32 v59, 7, v30\n"
    "v_and_b32 v59, 6, v59\n"
    "v_lshrrev_b32 v58, 3, v30\n"
    "v_xor_b32 v59, v58, v59\n"
    "v_lshl_add_u32 v36, v59, 4, v36\n"
    "v_and_b32 v38, 7, v30\n"
    "v_lshlrev_b32 v38, 5, v38\n"
    "v_add_u32 v38, 0x15000, v38\n"
    "s_lshl_b32 s86, s48, 3\n"
    "v_add_u32 v39, s86, v38\n"
    "v_and_b32_e64 v59, 1, v54\n"
    "v_lshl_add_u32 v39, v59, 2, v39\n"
    "s_lshl_b32 s61, s77, 18\n"
    "s_add_u32 s61, s61, 0\n"
    "v_lshrrev_b32 v40, 5, v30\n"
    "s_lshl_b32 s86, s48, 16\n"
    "s_lshl_b32 s85, s76, 2\n"
    "s_and_b32 s84, s78, 2\n"
    "s_add_u32 s85, s85, s84\n"
    "s_lshl_b32 s85, s85, 9\n"
    "s_add_u32 s86, s86, s85\n"
    "s_and_b32 s85, s78, 1\n"
    "s_lshl_b32 s85, s85, 7\n"
    "s_add_u32 s86, s86, s85\n"
    "s_add_u32 s86, s86, s61\n"
    "v_lshl_add_u32 v40, v40, 13, s86\n"
    "v_bfe_u32 v59, v30, 3, 2\n"
    "v_lshl_add_u32 v40, v59, 8, v40\n"
    "v_and_b32 v59, 7, v30\n"
    "v_lshl_add_u32 v40, v59, 4, v40\n"
    "s_lshl_b32 s43, s76, 2\n"
    "s_add_u32 s43, s43, s78\n"
    "s_mul_i32 s43, s43, 7\n"
    "s_and_b32 s86, s78, 1\n"
    "s_mul_i32 s86, s86, 3\n"
    "s_add_u32 s43, s43, s86\n"
    "s_lshl_b32 s43, s43, 6\n"
    "s_and_b32 s62, s77, 7\n"
    "s_add_u32 s0, s62, 0\n"
    "s_and_b32 s0, s0, 7\n"
    "s_mul_i32 s0, s0, 0xe0000\n"
    "s_add_u32 s1, s62, 1\n"
    "s_and_b32 s1, s1, 7\n"
    "s_mul_i32 s1, s1, 0xe0000\n"
    "s_add_u32 s2, s62, 2\n"
    "s_and_b32 s2, s2, 7\n"
    "s_mul_i32 s2, s2, 0xe0000\n"
    "v_mul_u32_u24 v14, 0x1c00, v55\n"
    "v_lshl_add_u32 v14, v54, 6, v14\n"
    "s_and_b32 s86, s48, 1\n"
    "s_lshl_b32 s86, s86, 1\n"
    "v_xor_b32_e64 v59, s86, v56\n"
    "v_lshl_add_u32 v14, v59, 4, v14\n"
    "s_mul_i32 s86, s48, 0xe000\n"
    "s_add_u32 s86, s86, s43\n"
    "v_add_u32 v14, s86, v14\n"
    "s_lshl_b32 s89, s48, 9\n"
    "s_add_u32 s89, s89, 0x24000\n"
    "v_lshl_add_u32 v47, v55, 11, s89\n"
    "v_lshlrev_b32 v59, 6, v55\n"
    "v_lshlrev_b32_e64 v58, 5, v54\n"
    "v_lshl_add_u32 v58, v56, 3, v58\n"
    "v_xor_b32 v59, v58, v59\n"
    "v_add_u32 v47, v47, v59\n"
    "v_xor_b32 v48, 0x80, v47\n"
    "v_mul_u32_u24 v49, 0x240, v56\n"
    "s_bfe_u32 s89, s100, 0x30003\n"
    "s_lshl_b32 s89, s89, 4\n"
    "s_bfe_u32 s88, s100, 0x10006\n"
    "s_lshl_b32 s88, s88, 1\n"
    "s_add_u32 s89, s89, s88\n"
    "v_lshl_add_u32 v59, v56, 2, s89\n"
    "v_and_b32 v59, 63, v59\n"
    "v_add_u32 v49, v49, v59\n"
    "v_lshrrev_b32 v59, 2, v30\n"
    "v_lshl_add_u32 v49, v59, 1, v49\n"
    "s_lshl_b32 s89, s48, 7\n"
    "s_add_u32 s89, s89, 0x1b800\n"
    "v_add_u32 v49, s89, v49\n"
    "s_mov_b64 exec, 0\n"
    "s_cmp_eq_u32 s48, 0\n"
    "s_cbranch_scc1 LPAD0_%=\n"
    "s_cmp_eq_u32 s48, 1\n"
    "s_cbranch_scc1 LPAD1_%=\n"
    "s_cmp_eq_u32 s48, 2\n"
    "s_cbranch_scc1 LPAD2_%=\n"
    "s_branch LPAD3_%=\n"
    "LPADR_%=:\n"
    "s_mov_b64 exec, -1\n"
    "s_waitcnt lgkmcnt(0)\n"
    "s_waitcnt vmcnt(9)\n"
    "ds_read_b128 v[64:67], v36 offset:0\n"
    "ds_read_b128 v[68:71], v36 offset:1024\n"
    "ds_read_b128 v[72:75], v36 offset:2048\n"
    "ds_read_b128 v[76:79], v36 offset:3072\n"
    "v_lshlrev_b32 v80, 16, v80\n"
    "s_waitcnt lgkmcnt(3)\n"
    "v_lshlrev_b32 v96, 16, v64\n"
    "v_and_b32 v97, 0xffff0000, v64\n"
    "v_pk_add_f32 v[96:97], v[96:97], v[80:81] op_sel_hi:[1,0] neg_lo:[0,1] neg_hi:[0,1]\n"
    "v_pk_mul_f32 v[84:85], v[96:97], v[96:97]\n"
    "v_lshlrev_b32_e64 v98, 16, v65\n"
    "v_and_b32 v99, 0xffff0000, v65\n"
    "v_pk_add_f32 v[98:99], v[98:99], v[80:81] op_sel_hi:[1,0] neg_lo:[0,1] neg_hi:[0,1]\n"
    "v_pk_add_f32 v[82:83], v[98:99], v[96:97]\n"
    "v_pk_fma_f32 v[84:85], v[98:99], v[98:99], v[84:85]\n"
    "v_lshlrev_b32_e64 v100, 16, v66\n"
    "v_and_b32 v101, 0xffff0000, v66\n"
    "v_pk_add_f32 v[100:101], v[100:101], v[80:81] op_sel_hi:[1,0] neg_lo:[0,1] neg_hi:[0,1]\n"
    "v_pk_add_f32 v[82:83], v[100:101], v[82:83]\n"
    "v_pk_fma_f32 v[84:85], v[100:101], v[100:101], v[84:85]\n"
    "v_lshlrev_b32_e64 v102, 16, v67\n"
    "v_and_b32 v103, 0xffff0000, v67\n"
    "v_pk_add_f32 v[102:103], v[102:103], v[80:81] op_sel_hi:[1,0] neg_lo:[0,1] neg_hi:[0,1]\n"
    "v_pk_add_f32 v[82:83], v[102:103], v[82:83]\n"
    "v_pk_fma_f32 v[84:85], v[102:103], v[102:103], v[84:85]\n"
    "s_waitcnt lgkmcnt(2)\n"
    "v_lshlrev_b32 v104, 16, v68\n"
    "v_and_b32 v105, 0xffff0000, v68\n"
    "v_pk_add_f32 v[104:105], v[104:105], v[80:81] op_sel_hi:[1,0] neg_lo:[0,1] neg_hi:[0,1]\n"
    "v_pk_add_f32 v[82:83], v[104:105], v[82:83]\n"
    "v_pk_fma_f32 v[84:85], v[104:105], v[104:105], v[84:85]\n"
    "v_lshlrev_b32_e64 v106, 16, v69\n"
    "v_and_b32 v107, 0xffff0000, v69\n"
    "v_pk_add_f32 v[106:107], v[106:107], v[80:81] op_sel_hi:[1,0] neg_lo:[0,1] neg_hi:[0,1]\n"
    "v_pk_add_f32 v[82:83], v[106:107], v[82:83]\n"
    "v_pk_fma_f32 v[84:85], v[106:107], v[106:107], v[84:85]\n"
    "v_lshlrev_b32_e64 v108, 16, v70\n"
    "v_and_b32 v109, 0xffff0000, v70\n"
    "v_pk_add_f32 v[108:109], v[108:109], v[80:81] op_sel_hi:[1,0] neg_lo:[0,1] neg_hi:[0,1]\n"
    "v_pk_add_f32 v[82:83], v[108:109], v[82:83]\n"
    "v_pk_fma_f32 v[84:85], v[108:109], v[108:109], v[84:85]\n"
    "v_lshlrev_b32_e64 v110, 16, v71\n"
    "v_and_b32 v111, 0xffff0000, v71\n"
    "v_pk_add_f32 v[110:111], v[110:111], v[80:81] op_sel_hi:[1,0] neg_lo:[0,1] neg_hi:[0,1]\n"
    "v_pk_add_f32 v[82:83], v[110:111], v[82:83]\n"
    "v_pk_fma_f32 v[84:85], v[110:111], v[110:111], v[84:85]\n"
    "s_waitcnt lgkmcnt(1)\n"
    "v_lshlrev_b32 v112, 16, v72\n"
    "v_and_b32 v113, 0xffff0000, v72\n"
    "v_pk_add_f32 v[112:113], v[112:113], v[80:81] op_sel_hi:[1,0] neg_lo:[0,1] neg_hi:[0,1]\n"
    "v_pk_add_f32 v[82:83], v[112:113], v[82:83]\n"
    "v_pk_fma_f32 v[84:85], v[112:113], v[112:113], v[84:85]\n"
    "v_lshlrev_b32_e64 v114, 16, v73\n"
    "v_and_b32 v115, 0xffff0000, v73\n"
    "v_pk_add_f32 v[114:115], v[114:115], v[80:81] op_sel_hi:[1,0] neg_lo:[0,1] neg_hi:[0,1]\n"
    "v_pk_add_f32 v[82:83], v[114:115], v[82:83]\n"
    "v_pk_fma_f32 v[84:85], v[114:115], v[114:115], v[84:85]\n"
    "v_lshlrev_b32_e64 v116, 16, v74\n"
    "v_and_b32 v117, 0xffff0000, v74\n"
    "v_pk_add_f32 v[116:117], v[116:117], v[80:81] op_sel_hi:[1,0] neg_lo:[0,1] neg_hi:[0,1]\n"
    "v_pk_add_f32 v[82:83], v[116:117], v[82:83]\n"
    "v_pk_fma_f32 v[84:85], v[116:117], v[116:117], v[84:85]\n"
    "v_lshlrev_b32_e64 v118, 16, v75\n"
    "v_and_b32 v119, 0xffff0000, v75\n"
    "v_pk_add_f32 v[118:119], v[118:119], v[80:81] op_sel_hi:[1,0] neg_lo:[0,1] neg_hi:[0,1]\n"
    "v_pk_add_f32 v[82:83], v[118:119], v[82:83]\n"
    "v_pk_fma_f32 v[84:85], v[118:119], v[118:119], v[84:85]\n"
    "s_waitcnt lgkmcnt(0)\n"
    "v_lshlrev_b32 v120, 16, v76\n"
    "v_and_b32 v121, 0xffff0000, v76\n"
    "v_pk_add_f32 v[120:121], v[120:121], v[80:81] op_sel_hi:[1,0] neg_lo:[0,1] neg_hi:[0,1]\n"
    "v_pk_add_f32 v[82:83], v[120:121], v[82:83]\n"
    "v_pk_fma_f32 v[84:85], v[120:121], v[120:121], v[84:85]\n"
    "v_lshlrev_b32_e64 v122, 16, v77\n"
    "v_and_b32 v123, 0xffff0000, v77\n"
    "v_pk_add_f32 v[122:123], v[122:123], v[80:81] op_sel_hi:[1,0] neg_lo:[0,1] neg_hi:[0,1]\n"
    "v_pk_add_f32 v[82:83], v[122:123], v[82:83]\n"
    "v_pk_fma_f32 v[84:85], v[122:123], v[122:123], v[84:85]\n"
    "v_lshlrev_b32_e64 v124, 16, v78\n"
    "v_and_b32 v125, 0xffff0000, v78\n"
    "v_pk_add_f32 v[124:125], v[124:125], v[80:81] op_sel_hi:[1,0] neg_lo:[0,1] neg_hi:[0,1]\n"
    "v_pk_add_f32 v[82:83], v[124:125], v[82:83]\n"
    "v_pk_fma_f32 v[84:85], v[124:125], v[124:125], v[84:85]\n"
    "v_lshlrev_b32_e64 v126, 16, v79\n"
    "v_and_b32 v127, 0xffff0000, v79\n"
    "v_pk_add_f32 v[126:127], v[126:127], v[80:81] op_sel_hi:[1,0] neg_lo:[0,1] neg_hi:[0,1]\n"
    "v_pk_add_f32 v[82:83], v[126:127], v[82:83]\n"
    "v_pk_fma_f32 v[84:85], v[126:127], v[126:127], v[84:85]\n"
    "v_add_f32 v82, v82, v83\n"
    "v_add_f32 v83, v84, v85\n"
    "s_nop 1\n"
    "v_permlane16_swap_b32 v82, v83\n"
    "v_add_f32 v82, v82, v83\n"
    "s_nop 1\n"
    "v_add_f32_dpp v82, v82, v82 row_ror:8 row_mask:0xf bank_mask:0xf\n"
    "v_mov_b32 v83, v82\n"
    "s_nop 1\n"
    "v_permlane32_swap_b32 v82, v83\n"
    "v_add_f32 v82, v82, v83\n"
    "ds_write_b32 v39, v82\n"
    "s_waitcnt lgkmcnt(0)\n"
    "s_barrier\n"
    "ds_read_b128 v[64:67], v38\n"
    "ds_read_b128 v[68:71], v38 offset:16\n"
    "s_waitcnt lgkmcnt(1)\n"
    "v_pk_add_f32 v[64:65], v[64:65], v[66:67]\n"
    "s_waitcnt lgkmcnt(0)\n"
    "v_pk_add_f32 v[68:69], v[68:69], v[70:71]\n"
    "v_pk_add_f32 v[64:65], v[64:65], v[68:69]\n"
    "v_mul_f32 v64, 0x3a800000, v64\n"
    "v_mul_f32 v65, 0x3a800000, v65\n"
    "v_add_f32_e64 v82, v64, v80\n"
    "v_fma_f32 v83, -v64, v64, v65\n"
    "v_max_f32_e64 v83, 0, v83\n"
    "v_cvt_pk_bf16_f32 v82, v82, v83\n"
    "v_and_b32 v83, 0xffff0000, v82\n"
    "v_add_f32 v83, 0x3727c5ac, v83\n"
    "v_cvt_pk_bf16_f32 v83, v83, v83\n"
    "v_lshlrev_b32 v83, 16, v83\n"
    "v_sqrt_f32 v83, v83\n"
    "v_lshlrev_b32_e64 v82, 16, v82\n"
    "v_cvt_pk_bf16_f32 v83, v83, v83\n"
    "v_lshlrev_b32 v83, 16, v83\n"
    "v_rcp_f32 v86, v83\n"
    "v_sub_f32 v87, v80, v82\n"
    "v_mul_f32 v87, v87, v86\n"
    "v_pk_fma_f32 v[96:97], v[96:97], v[86:87], v[86:87] op_sel:[0,0,1] op_sel_hi:[1,0,1]\n"
    "v_pk_fma_f32 v[98:99], v[98:99], v[86:87], v[86:87] op_sel:[0,0,1] op_sel_hi:[1,0,1]\n"
    "v_pk_fma_f32 v[100:101], v[100:101], v[86:87], v[86:87] op_sel:[0,0,1] op_sel_hi:[1,0,1]\n"
    "v_pk_fma_f32 v[102:103], v[102:103], v[86:87], v[86:87] op_sel:[0,0,1] op_sel_hi:[1,0,1]\n"
    "v_cvt_pk_bf16_f32 v128, v96, v97\n"
    "v_cvt_pk_bf16_f32 v129, v98, v99\n"
    "v_cvt_pk_bf16_f32 v130, v100, v101\n"
    "v_cvt_pk_bf16_f32 v131, v102, v103\n"
    "s_mov_b32 s84, 0\n"
    "s_nop 0\n"
    "buffer_store_dwordx4 v[128:131], v40, s[16:19], s84 offen sc1\n"
    "v_pk_fma_f32 v[104:105], v[104:105], v[86:87], v[86:87] op_sel:[0,0,1] op_sel_hi:[1,0,1]\n"
    "v_pk_fma_f32 v[106:107], v[106:107], v[86:87], v[86:87] op_sel:[0,0,1] op_sel_hi:[1,0,1]\n"
    "v_pk_fma_f32 v[108:109], v[108:109], v[86:87], v[86:87] op_sel:[0,0,1] op_sel_hi:[1,0,1]\n"
    "v_pk_fma_f32 v[110:111], v[110:111], v[86:87], v[86:87] op_sel:[0,0,1] op_sel_hi:[1,0,1]\n"
    "v_cvt_pk_bf16_f32 v132, v104, v105\n"
    "v_cvt_pk_bf16_f32 v133, v106, v107\n"
    "v_cvt_pk_bf16_f32 v134, v108, v109\n"
    "v_cvt_pk_bf16_f32 v135, v110, v111\n"
    "s_mov_b32 s84, 16384\n"
    "buffer_store_dwordx4 v[132:135], v40, s[16:19], s84 offen sc1\n"
    "v_pk_fma_f32 v[112:113], v[112:113], v[86:87], v[86:87] op_sel:[0,0,1] op_sel_hi:[1,0,1]\n"
    "v_pk_fma_f32 v[114:115], v[114:115], v[86:87], v[86:87] op_sel:[0,0,1] op_sel_hi:[1,0,1]\n"
    "v_pk_fma_f32 v[116:117], v[116:117], v[86:87], v[86:87] op_sel:[0,0,1] op_sel_hi:[1,0,1]\n"
    "v_pk_fma_f32 v[118:119], v[118:119], v[86:87], v[86:87] op_sel:[0,0,1] op_sel_hi:[1,0,1]\n"
    "v_cvt_pk_bf16_f32 v136, v112, v113\n"
    "v_cvt_pk_bf16_f32 v137, v114, v115\n"
    "v_cvt_pk_bf16_f32 v138, v116, v117\n"
    "v_cvt_pk_bf16_f32 v139, v118, v119\n"
    "s_mov_b32 s84, 32768\n"
    "buffer_store_dwordx4 v[136:139], v40, s[16:19], s84 offen sc1\n"
    "v_pk_fma_f32 v[120:121], v[120:121], v[86:87], v[86:87] op_sel:[0,0,1] op_sel_hi:[1,0,1]\n"
    "v_pk_fma_f32 v[122:123], v[122:123], v[86:87], v[86:87] op_sel:[0,0,1] op_sel_hi:[1,0,1]\n"
    "v_pk_fma_f32 v[124:125], v[124:125], v[86:87], v[86:87] op_sel:[0,0,1] op_sel_hi:[1,0,1]\n"
    "v_pk_fma_f32 v[126:127], v[126:127], v[86:87], v[86:87] op_sel:[0,0,1] op_sel_hi:[1,0,1]\n"
    "v_cvt_pk_bf16_f32 v140, v120, v121\n"
    "v_cvt_pk_bf16_f32 v141, v122, v123\n"
    "v_cvt_pk_bf16_f32 v142, v124, v125\n"
    "v_cvt_pk_bf16_f32 v143, v126, v127\n"
    "s_mov_b32 s84, 49152\n"
    "buffer_store_dwordx4 v[140:143], v40, s[16:19], s84 offen sc1\n"
    "v_and_b32_e64 v42, 7, v30\n"
    "v_mul_u32_u24 v42, 0x810, v42\n"
    "v_lshrrev_b32_e64 v59, 3, v30\n"
    "v_lshl_add_u32 v42, v59, 4, v42\n"
    "s_lshl_b32 s89, s48, 9\n"
    "s_add_u32 s89, s89, 0x17700\n"
    "v_add_u32 v42, s89, v42\n"
    "v_lshrrev_b32_e64 v43, 5, v30\n"
    "s_mul_i32 s89, s48, 0x810\n"
    "s_add_u32 s89, s89, 0x17700\n"
    "v_lshl_add_u32 v43, v43, 8, s89\n"
    "v_lshl_add_u32 v43, v55, 6, v43\n"
    "v_lshl_add_u32 v43, v56, 3, v43\n"
    "v_and_b32_e64 v59, 1, v54\n"
    "v_lshl_add_u32 v43, v59, 5, v43\n"
    "v_add_u32 v44, 0x20000, v41\n"
    "v_add_u32 v45, 0x15400, v41\n"
    "v_and_b32 v23, 15, v30\n"
    "s_lshl_b32 s89, s77, 4\n"
    "s_lshl_b32 s88, s76, 2\n"
    "s_add_u32 s88, s88, s78\n"
    "s_add_u32 s89, s89, s88\n"
    "s_lshl_b32 s89, s89, 2\n"
    "s_add_u32 s89, s89, s48\n"
    "s_lshl_b32 s89, s89, 7\n"
    "v_lshl_add_u32 v23, v23, 3, s89\n"
    "v_lshrrev_b32 v27, 2, v30\n"
    "s_lshl_b32 s89, s77, 4\n"
    "s_lshl_b32 s88, s48, 2\n"
    "s_add_u32 s89, s89, s88\n"
    "s_lshl_b32 s89, s89, 9\n"
    "v_lshl_add_u32 v27, v27, 7, s89\n"
    "s_lshr_b32 s60, s60, 11\n"
    "s_lshl_b32 s89, s48, 5\n"
    "s_add_u32 s60, s60, s89\n"
    "v_lshrrev_b32 v46, 5, v30\n"
    "s_lshl_b32 s89, s36, 1\n"
    "s_add_u32 s89, s89, 0x24000\n"
    "v_lshl_add_u32 v46, v46, 8, s89\n"
    "v_and_b32 v59, 31, v30\n"
    "v_lshlrev_b32 v59, 3, v59\n"
    "s_lshl_b32 s89, s48, 6\n"
    "v_xor_b32 v59, s89, v59\n"
    "v_add_u32 v46, v46, v59\n"
    "v_cvt_f32_u32 v50, s60\n"
    "v_mul_f32 v50, 0x3a000000, v50\n"
    "v_cos_f32 v51, v50\n"
    "v_sin_f32 v50, v50\n"
    "v_readfirstlane_b32 s50, v51\n"
    "v_readfirstlane_b32 s51, v50\n"
    "s_mov_b32 s30, 7168\n"
    "s_mov_b32 s31, 0x20000\n"
    "s_mov_b32 s34, 2048\n"
    "s_mov_b32 s35, 0x20000\n"
    "v_lshlrev_b32 v252, 5, v30\n"
    "v_and_b32 v28, 15, v30\n"
    "v_add_u32 v59, 1, v55\n"
    "v_lshrrev_b32 v59, 2, v59\n"
    "v_mul_u32_u24 v59, 6, v59\n"
    "v_lshrrev_b32 v58, 1, v55\n"
    "v_mul_u32_u24 v58, 12, v58\n"
    "v_sub_u32 v58, 6, v58\n"
    "v_sub_u32 v59, v58, v59\n"
    "s_and_b32 s88, s78, 1\n"
    "s_sub_u32 s89, 0, s88\n"
    "v_and_b32 v59, s89, v59\n"
    "v_add_u32 v28, v28, v59\n"
    "s_lshl_b32 s89, s76, 2\n"
    "s_add_u32 s89, s89, s78\n"
    "s_mul_i32 s89, s89, 0x1c0\n"
    "v_lshl_add_u32 v28, v28, 5, s89\n"
    "v_lshl_add_u32 v28, v54, 3, v28\n"
    "v_lshrrev_b32 v246, 3, v30\n"
    "v_mul_u32_u24 v246, 0x1c00, v246\n"
    "v_and_b32_e64 v59, 7, v30\n"
    "v_lshl_add_u32 v246, v59, 4, v246\n"
    "s_lshr_b32 s89, s100, 3\n"
    "s_mul_i32 s89, s89, 0x38000\n"
    "s_mul_i32 s88, s48, 0xe000\n"
    "s_add_u32 s89, s89, s88\n"
    "s_and_b32 s88, s100, 7\n"
    "s_mul_i32 s88, s88, 0x380\n"
    "s_add_u32 s89, s89, s88\n"
    "s_add_u32 s89, s89, 0x180\n"
    "v_add_u32 v246, s89, v246\n"
    "v_and_b32 v33, 1, v54\n"
    "s_and_b32 s89, s100, 31\n"
    "s_lshl_b32 s89, s89, 1\n"
    "s_lshr_b32 s88, s100, 5\n"
    "s_lshl_b32 s88, s88, 8\n"
    "s_add_u32 s89, s89, s88\n"
    "v_add_u32 v33, s89, v33\n"
    "v_lshl_add_u32 v33, v55, 6, v33\n"
    "v_and_b32 v37, 1, v30\n"
    "v_sub_u32 v37, 0, v37\n"
    "v_xor_b32 v33, v33, v37\n"
    "v_and_b32 v59, 1, v30\n"
    "v_add_u32_e64 v33, v33, v59\n"
    "v_and_b32 v33, 0x7ff, v33\n"
    "v_mul_u32_u24 v33, 0x402, v33\n"
    "v_and_b32_e64 v59, 2, v30\n"
    "v_lshl_add_u32 v33, v59, 9, v33\n"
    "buffer_load_dwordx4 v[248:251], v246, s[8:11], 0 offen\n"
    "v_cvt_pk_f16_f32 v64, v96, v97\n"
    "v_cvt_pk_f16_f32 v65, v98, v99\n"
    "v_cvt_pk_f16_f32 v66, v100, v101\n"
    "buffer_load_dwordx2 v[28:29], v28, s[28:31], 0 offen\n"
    "v_cvt_pk_f16_f32 v67, v102, v103\n"
    "ds_write_b128 v42, v[64:67] offset:0\n"
    "v_cvt_pk_f16_f32 v68, v104, v105\n"
    "s_cmp_eq_u32 s48, 3\n"
    "s_cbranch_scc0 LWA1r_%=\n"
    "buffer_load_ushort v52, v33, s[52:55], 0 offen\n"
    "buffer_load_ushort v53, v33, s[56:59], 0 offen\n"
    "buffer_load_dword v252, v252, s[32:35], 0 offen\n"
    "s_nop 1\n"
    "LWA1r_%=:\n"
    "v_cvt_pk_f16_f32 v69, v106, v107\n"
    "v_cvt_pk_f16_f32 v70, v108, v109\n"
    "v_cvt_pk_f16_f32 v71, v110, v111\n"
    "s_add_u32 m0, s36, 0\n"
    "s_add_u32 s84, s0, 0\n"
    "s_nop 0\n"
    "buffer_load_dwordx4 v14, s[8:11], s84 offen lds\n"
    "ds_write_b128 v42, v[68:71] offset:128\n"
    "v_cvt_pk_f16_f32 v72, v112, v113\n"
    "v_cvt_pk_f16_f32 v73, v114, v115\n"
    "s_add_u32 m0, s36, 4096\n"
    "s_add_u32 s84, s0, 28672\n"
    "buffer_load_dwordx4 v14, s[8:11], s84 offen lds\n"
    "v_cvt_pk_f16_f32 v74, v116, v117\n"
    "v_cvt_pk_f16_f32 v75, v118, v119\n"
    "ds_write_b128 v42, v[72:75] offset:256\n"
    "s_add_u32 m0, s36, 8192\n"
    "s_add_u32 s84, s0, 229376\n"
    "buffer_load_dwordx4 v14, s[8:11], s84 offen lds\n"
    "v_cvt_pk_f16_f32 v76, v120, v121\n"
    "v_cvt_pk_f16_f32 v77, v122, v123\n"
    "v_cvt_pk_f16_f32 v78, v124, v125\n"
    "s_add_u32 m0, s36, 12288\n"
    "s_add_u32 s84, s0, 258048\n"
    "buffer_load_dwordx4 v14, s[8:11], s84 offen lds\n"
    "v_cvt_pk_f16_f32 v79, v126, v127\n"
    "ds_write_b128 v42, v[76:79] offset:384\n"
    "s_waitcnt vmcnt(10)\n"
    "v_readfirstlane_b32 s98, v60\n"
    "v_readfirstlane_b32 s99, v61\n"
    "s_nop 0\n"
    "s_add_u32 s98, s98, 0x7f4a7c15\n"
    "s_addc_u32 s99, s99, 0x9e3779b9\n"
    "v_mov_b32 v60, s98\n"
    "v_mov_b32 v61, s99\n"
    "s_waitcnt lgkmcnt(0)\n"
    "s_barrier\n"
    "ds_read_b64_tr_b16 v[64:65], v43 offset:0\n"
    "ds_read_b64_tr_b16 v[66:67], v43 offset:512\n"
    "ds_read_b64_tr_b16 v[68:69], v43 offset:1024\n"
    "ds_read_b64_tr_b16 v[70:71], v43 offset:1536\n"
    "ds_read_b64_tr_b16 v[72:73], v43 offset:8256\n"
    "ds_read_b64_tr_b16 v[74:75], v43 offset:8768\n"
    "ds_read_b64_tr_b16 v[76:77], v43 offset:9280\n"
    "ds_read_b64_tr_b16 v[78:79], v43 offset:9792\n"
    "ds_read_b128 v[144:147], v45 offset:0\n"
    "ds_read_b128 v[148:151], v45 offset:1024\n"
    "ds_read_b128 v[152:155], v45 offset:2048\n"
    "ds_read_b128 v[156:159], v45 offset:3072\n"
    "ds_read_b128 v[160:163], v45 offset:4096\n"
    "ds_read_b128 v[164:167], v45 offset:5120\n"
    "ds_read_b128 v[168:171], v45 offset:6144\n"
    "ds_read_b128 v[172:175], v45 offset:7168\n"
    "ds_read_b128 v[176:179], v44 offset:0\n"
    "ds_read_b128 v[180:183], v44 offset:1024\n"
    "ds_read_b128 v[184:187], v44 offset:2048\n"
    "ds_read_b128 v[188:191], v44 offset:3072\n"
    "ds_read_b128 v[192:195], v44 offset:4096\n"
    "ds_read_b128 v[196:199], v44 offset:5120\n"
    "ds_read_b128 v[200:203], v44 offset:6144\n"
    "ds_read_b128 v[204:207], v44 offset:7168\n"
    "v_mov_b32 v240, 0xbc003c00\n"
    "v_mov_b32 v241, 0xbc003c00\n"
    "v_mov_b32 v242, 0xbc003c00\n"
    "v_mov_b32 v243, 0xbc003c00\n"
    "s_waitcnt lgkmcnt(15)\n"
    "v_pk_add_f16 v80, v64, v72\n"
    "v_pk_add_f16 v81, v65, v73\n"
    "v_pk_add_f16 v82, v66, v74\n"
    "v_pk_add_f16 v83, v67, v75\n"
    "v_pk_add_f16 v84, v68, v76\n"
    "v_pk_add_f16 v85, v69, v77\n"
    "v_pk_add_f16 v86, v70, v78\n"
    "v_pk_add_f16 v87, v71, v79\n"
    "v_mfma_f32_32x32x16_f16 v[96:111], v[80:83], v[144:147], 0\n"
    "v_pk_add_f16 v88, v64, v72 neg_lo:[0,1] neg_hi:[0,1]\n"
    "v_pk_add_f16 v89, v65, v73 neg_lo:[0,1] neg_hi:[0,1]\n"
    "s_waitcnt lgkmcnt(13)\n"
    "v_mfma_f32_32x32x16_f16 v[112:127], v[80:83], v[152:155], 0\n"
    "v_pk_add_f16 v90, v66, v74 neg_lo:[0,1] neg_hi:[0,1]\n"
    "v_pk_add_f16 v91, v67, v75 neg_lo:[0,1] neg_hi:[0,1]\n"
    "v_mfma_f32_32x32x16_f16 v[96:111], v[84:87], v[148:151], v[96:111]\n"
    "v_pk_add_f16 v92, v68, v76 neg_lo:[0,1] neg_hi:[0,1]\n"
    "v_pk_add_f16 v93, v69, v77 neg_lo:[0,1] neg_hi:[0,1]\n"
    "s_waitcnt lgkmcnt(12)\n"
    "v_mfma_f32_32x32x16_f16 v[112:127], v[84:87], v[156:159], v[112:127]\n"
    "v_pk_add_f16 v94, v70, v78 neg_lo:[0,1] neg_hi:[0,1]\n"
    "v_pk_add_f16 v95, v71, v79 neg_lo:[0,1] neg_hi:[0,1]\n"
    "v_mfma_f32_32x32x16_f16 v[208:223], v[88:91], v[144:147], 0\n"
    "v_mfma_f32_32x32x16_f16 v[224:239], v[88:91], v[152:155], 0\n"
    "v_mfma_f32_32x32x16_f16 v[208:223], v[92:95], v[148:151], v[208:223]\n"
    "v_mfma_f32_32x32x16_f16 v[224:239], v[92:95], v[156:159], v[224:239]\n"
    "s_waitcnt lgkmcnt(3)\n"
    "s_nop 0\n"
    "v_pk_mul_f32 v[128:129], v[96:97], v[192:193]\n"
    "v_pk_mul_f32 v[96:97], v[96:97], v[176:177]\n"
    "s_nop 1\n"
    "v_pk_fma_f32 v[96:97], v[112:113], v[192:193], v[96:97]\n"
    "v_pk_fma_f32 v[112:113], v[112:113], v[176:177], v[128:129] neg_lo:[0,0,1] neg_hi:[0,0,1]\n"
    "v_pk_mul_f32 v[130:131], v[98:99], v[194:195]\n"
    "v_pk_mul_f32 v[98:99], v[98:99], v[178:179]\n"
    "v_pk_fma_f32 v[98:99], v[114:115], v[194:195], v[98:99]\n"
    "v_pk_fma_f32 v[114:115], v[114:115], v[178:179], v[130:131] neg_lo:[0,0,1] neg_hi:[0,0,1]\n"
    "s_waitcnt lgkmcnt(2)\n"
    "v_pk_mul_f32 v[128:129], v[100:101], v[196:197]\n"
    "LPAD0_%=:\n"
    "s_cbranch_execz LPAD4_%=\n"
    "v_pk_mul_f32 v[100:101], v[100:101], v[180:181]\n"
    "v_pk_fma_f32 v[100:101], v[116:117], v[196:197], v[100:101]\n"
    "v_pk_fma_f32 v[116:117], v[116:117], v[180:181], v[128:129] neg_lo:[0,0,1] neg_hi:[0,0,1]\n"
    "v_pk_mul_f32 v[130:131], v[102:103], v[198:199]\n"
    "v_pk_mul_f32 v[102:103], v[102:103], v[182:183]\n"
    "v_pk_fma_f32 v[102:103], v[118:119], v[198:199], v[102:103]\n"
    "v_pk_fma_f32 v[118:119], v[118:119], v[182:183], v[130:131] neg_lo:[0,0,1] neg_hi:[0,0,1]\n"
    "s_waitcnt lgkmcnt(1)\n"
    "v_pk_mul_f32 v[128:129], v[104:105], v[200:201]\n"
    "v_pk_mul_f32 v[104:105], v[104:105], v[184:185]\n"
    "v_pk_fma_f32 v[104:105], v[120:121], v[200:201], v[104:105]\n"
    "v_pk_fma_f32 v[120:121], v[120:121], v[184:185], v[128:129] neg_lo:[0,0,1] neg_hi:[0,0,1]\n"
    "v_pk_mul_f32 v[130:131], v[106:107], v[202:203]\n"
    "v_pk_mul_f32 v[106:107], v[106:107], v[186:187]\n"
    "v_pk_fma_f32 v[106:107], v[122:123], v[202:203], v[106:107]\n"
    "v_pk_fma_f32 v[122:123], v[122:123], v[186:187], v[130:131] neg_lo:[0,0,1] neg_hi:[0,0,1]\n"
    "s_waitcnt lgkmcnt(0)\n"
    "v_pk_mul_f32 v[128:129], v[108:109], v[204:205]\n"
    "v_pk_mul_f32 v[108:109], v[108:109], v[188:189]\n"
    "v_pk_fma_f32 v[108:109], v[124:125], v[204:205], v[108:109]\n"
    "v_pk_fma_f32 v[124:125], v[124:125], v[188:189], v[128:129] neg_lo:[0,0,1] neg_hi:[0,0,1]\n"
    "v_pk_mul_f32 v[130:131], v[110:111], v[206:207]\n"
    "v_pk_mul_f32 v[110:111], v[110:111], v[190:191]\n"
    "v_pk_fma_f32 v[110:111], v[126:127], v[206:207], v[110:111]\n"
    "v_pk_fma_f32 v[126:127], v[126:127], v[190:191], v[130:131] neg_lo:[0,0,1] neg_hi:[0,0,1]\n"
    "v_pk_mul_f32 v[128:129], v[208:209], v[192:193]\n"
    "v_pk_mul_f32 v[208:209], v[208:209], v[176:177]\n"
    "v_pk_fma_f32 v[208:209], v[224:225], v[192:193], v[208:209]\n"
    "v_pk_fma_f32 v[224:225], v[224:225], v[176:177], v[128:129] neg_lo:[0,0,1] neg_hi:[0,0,1]\n"
    "v_pk_mul_f32 v[130:131], v[210:211], v[194:195]\n"
    "v_pk_mul_f32 v[210:211], v[210:211], v[178:179]\n"
    "v_pk_fma_f32 v[210:211], v[226:227], v[194:195], v[210:211]\n"
    "v_pk_fma_f32 v[226:227], v[226:227], v[178:179], v[130:131] neg_lo:[0,0,1] neg_hi:[0,0,1]\n"
    "v_pk_mul_f32 v[128:129], v[212:213], v[196:197]\n"
    "v_pk_mul_f32 v[212:213], v[212:213], v[180:181]\n"
    "v_pk_fma_f32 v[212:213], v[228:229], v[196:197], v[212:213]\n"
    "v_pk_fma_f32 v[228:229], v[228:229], v[180:181], v[128:129] neg_lo:[0,0,1] neg_hi:[0,0,1]\n"
    "v_pk_mul_f32 v[130:131], v[214:215], v[198:199]\n"
    "v_pk_mul_f32 v[214:215], v[214:215], v[182:183]\n"
    "v_pk_fma_f32 v[214:215], v[230:231], v[198:199], v[214:215]\n"
    "v_pk_fma_f32 v[230:231], v[230:231], v[182:183], v[130:131] neg_lo:[0,0,1] neg_hi:[0,0,1]\n"
    "v_pk_mul_f32 v[128:129], v[216:217], v[200:201]\n"
    "v_pk_mul_f32 v[216:217], v[216:217], v[184:185]\n"
    "v_pk_fma_f32 v[216:217], v[232:233], v[200:201], v[216:217]\n"
    "v_pk_fma_f32 v[232:233], v[232:233], v[184:185], v[128:129] neg_lo:[0,0,1] neg_hi:[0,0,1]\n"
    "v_pk_mul_f32 v[130:131], v[218:219], v[202:203]\n"
    "v_pk_mul_f32 v[218:219], v[218:219], v[186:187]\n"
    "v_pk_fma_f32 v[218:219], v[234:235], v[202:203], v[218:219]\n"
    "v_pk_fma_f32 v[234:235], v[234:235], v[186:187], v[130:131] neg_lo:[0,0,1] neg_hi:[0,0,1]\n"
    "v_pk_mul_f32 v[128:129], v[220:221], v[204:205]\n"
    "v_pk_mul_f32 v[220:221], v[220:221], v[188:189]\n"
    "v_pk_fma_f32 v[220:221], v[236:237], v[204:205], v[220:221]\n"
    "v_pk_fma_f32 v[236:237], v[236:237], v[188:189], v[128:129] neg_lo:[0,0,1] neg_hi:[0,0,1]\n"
    "v_pk_mul_f32 v[130:131], v[222:223], v[206:207]\n"
    "v_pk_mul_f32 v[222:223], v[222:223], v[190:191]\n"
    "v_pk_fma_f32 v[222:223], v[238:239], v[206:207], v[222:223]\n"
    "s_waitcnt vmcnt(6)\n"
    "s_nop 0\n"
    "s_mov_b32 s83, 13762560\n"
    "buffer_store_dwordx2 v[60:61], v23, s[16:19], s83 offen sc1\n"
    "v_pk_fma_f32 v[238:239], v[238:239], v[190:191], v[130:131] neg_lo:[0,0,1] neg_hi:[0,0,1]\n"
    "v_cvt_pk_f16_f32 v64, v96, v97\n"
    "v_cvt_pk_f16_f32 v65, v98, v99\n"
    "v_cvt_pk_f16_f32 v66, v100, v101\n"
    "v_cvt_pk_f16_f32 v67, v102, v103\n"
    "v_cvt_pk_f16_f32 v68, v104, v105\n"
    "v_cvt_pk_f16_f32 v69, v106, v107\n"
    "v_cvt_pk_f16_f32 v70, v108, v109\n"
    "v_cvt_pk_f16_f32 v71, v110, v111\n"
    "v_cvt_pk_f16_f32 v72, v112, v113\n"
    "v_cvt_pk_f16_f32 v73, v114, v115\n"
    "v_cvt_pk_f16_f32 v74, v116, v117\n"
    "v_cvt_pk_f16_f32 v75, v118, v119\n"
    "v_cvt_pk_f16_f32 v76, v120, v121\n"
    "v_cvt_pk_f16_f32 v77, v122, v123\n"
    "v_cvt_pk_f16_f32 v78, v124, v125\n"
    "v_cvt_pk_f16_f32 v79, v126, v127\n"
    "v_mfma_f32_32x32x16_f16 v[96:111], v[160:163], v[64:67], 0\n"
    "v_cvt_pk_f16_f32 v80, v208, v209\n"
    "v_cvt_pk_f16_f32 v81, v210, v211\n"
    "v_cvt_pk_f16_f32 v82, v212, v213\n"
    "v_cvt_pk_f16_f32 v83, v214, v215\n"
    "v_mfma_f32_32x32x16_f16 v[96:111], v[168:171], v[72:75], v[96:111]\n"
    "v_cvt_pk_f16_f32 v84, v216, v217\n"
    "v_cvt_pk_f16_f32 v85, v218, v219\n"
    "v_cvt_pk_f16_f32 v86, v220, v221\n"
    "v_cvt_pk_f16_f32 v87, v222, v223\n"
    "v_mfma_f32_32x32x16_f16 v[96:111], v[164:167], v[68:71], v[96:111]\n"
    "v_cvt_pk_f16_f32 v88, v224, v225\n"
    "v_cvt_pk_f16_f32 v89, v226, v227\n"
    "s_add_u32 m0, s36, 16384\n"
    "s_add_u32 s84, s0, 458752\n"
    "buffer_load_dwordx4 v14, s[8:11], s84 offen lds\n"
    "v_cvt_pk_f16_f32 v90, v228, v229\n"
    "v_cvt_pk_f16_f32 v91, v230, v231\n"
    "v_mfma_f32_32x32x16_f16 v[96:111], v[172:175], v[76:79], v[96:111]\n"
    "v_cvt_pk_f16_f32 v92, v232, v233\n"
    "v_cvt_pk_f16_f32 v93, v234, v235\n"
    "v_cvt_pk_f16_f32 v94, v236, v237\n"
    "v_cvt_pk_f16_f32 v95, v238, v239\n"
    "v_mfma_f32_32x32x16_f16 v[208:223], v[240:243], v[64:67], 0\n"
    "v_mfma_f32_32x32x16_f16 v[208:223], v[240:243], v[68:71], v[208:223]\n"
    "v_mfma_f32_32x32x16_f16 v[224:239], v[240:243], v[80:83], 0\n"
    "v_mfma_f32_32x32x16_f16 v[224:239], v[240:243], v[84:87], v[224:239]\n"
    "v_mfma_f32_32x32x16_f16 v[112:127], v[160:163], v[80:83], 0\n"
    "s_add_u32 m0, s36, 20480\n"
    "s_add_u32 s84, s0, 487424\n"
    "buffer_load_dwordx4 v14, s[8:11], s84 offen lds\n"
    "v_mfma_f32_32x32x16_f16 v[112:127], v[168:171], v[88:91], v[112:127]\n"
    "v_mfma_f32_32x32x16_f16 v[112:127], v[164:167], v[84:87], v[112:127]\n"
    "v_mfma_f32_32x32x16_f16 v[112:127], v[172:175], v[92:95], v[112:127]\n"
    "s_mov_b64 exec, 1\n"
    "s_nop 1\n"
    "v_mov_b32 v97, v208\n"
    "s_mov_b64 exec, -1\n"
    "v_cvt_pk_bf16_f32 v64, v96, v97\n"
    "v_cvt_pk_bf16_f32 v65, v98, v99\n"
    "v_cvt_pk_bf16_f32 v66, v100, v101\n"
    "v_cvt_pk_bf16_f32 v67, v102, v103\n"
    "v_cvt_pk_bf16_f32 v68, v104, v105\n"
    "v_cvt_pk_bf16_f32 v69, v106, v107\n"
    "v_cvt_pk_bf16_f32 v70, v108, v109\n"
    "v_cvt_pk_bf16_f32 v71, v110, v111\n"
    "s_add_u32 m0, s36, 24576\n"
    "s_add_u32 s84, s0, 688128\n"
    "buffer_load_dwordx4 v14, s[8:11], s84 offen lds\n"
    "ds_write_b64 v46, v[64:65] offset:0\n"
    "ds_write_b64 v46, v[66:67] offset:512\n"
    "ds_write_b64 v46, v[68:69] offset:1024\n"
    "ds_write_b64 v46, v[70:71] offset:1536\n"
    "s_mov_b64 exec, 1\n"
    "v_mov_b32 v113, v224\n"
    "s_mov_b64 exec, -1\n"
    "s_nop 0\n"
    "v_pk_mul_f32 v[128:129], v[112:113], s[50:51] op_sel_hi:[1,0]\n"
    "v_pk_mul_f32 v[130:131], v[114:115], s[50:51] op_sel_hi:[1,0]\n"
    "v_pk_mul_f32 v[132:133], v[116:117], s[50:51] op_sel_hi:[1,0]\n"
    "v_pk_mul_f32 v[134:135], v[118:119], s[50:51] op_sel_hi:[1,0]\n"
    "v_pk_mul_f32 v[136:137], v[120:121], s[50:51] op_sel_hi:[1,0]\n"
    "v_pk_mul_f32 v[138:139], v[122:123], s[50:51] op_sel_hi:[1,0]\n"
    "v_pk_mul_f32 v[140:141], v[124:125], s[50:51] op_sel_hi:[1,0]\n"
    "s_add_u32 m0, s36, 28672\n"
    "s_add_u32 s84, s0, 716800\n"
    "buffer_load_dwordx4 v14, s[8:11], s84 offen lds\n"
    "v_pk_mul_f32 v[142:143], v[126:127], s[50:51] op_sel_hi:[1,0]\n"
    "v_pk_fma_f32 v[128:129], v[112:113], s[50:51], v[128:129] op_sel:[1,1,0] op_sel_hi:[0,1,1] neg_hi:[0,1,0]\n"
    "v_pk_fma_f32 v[130:131], v[114:115], s[50:51], v[130:131] op_sel:[1,1,0] op_sel_hi:[0,1,1] neg_hi:[0,1,0]\n"
    "v_pk_fma_f32 v[132:133], v[116:117], s[50:51], v[132:133] op_sel:[1,1,0] op_sel_hi:[0,1,1] neg_hi:[0,1,0]\n"
    "v_pk_fma_f32 v[134:135], v[118:119], s[50:51], v[134:135] op_sel:[1,1,0] op_sel_hi:[0,1,1] neg_hi:[0,1,0]\n"
    "v_pk_fma_f32 v[136:137], v[120:121], s[50:51], v[136:137] op_sel:[1,1,0] op_sel_hi:[0,1,1] neg_hi:[0,1,0]\n"
    "v_pk_fma_f32 v[138:139], v[122:123], s[50:51], v[138:139] op_sel:[1,1,0] op_sel_hi:[0,1,1] neg_hi:[0,1,0]\n"
    "v_pk_fma_f32 v[140:141], v[124:125], s[50:51], v[140:141] op_sel:[1,1,0] op_sel_hi:[0,1,1] neg_hi:[0,1,0]\n"
    "v_pk_fma_f32 v[142:143], v[126:127], s[50:51], v[142:143] op_sel:[1,1,0] op_sel_hi:[0,1,1] neg_hi:[0,1,0]\n"
    "v_cvt_pk_bf16_f32 v72, v128, v129\n"
    "v_cvt_pk_bf16_f32 v73, v130, v131\n"
    "v_cvt_pk_bf16_f32 v74, v132, v133\n"
    "s_add_u32 m0, s36, 32768\n"
    "s_add_u32 s84, s1, 0\n"
    "s_nop 0\n"
    "buffer_load_dwordx4 v14, s[8:11], s84 offen lds\n"
    "v_cvt_pk_bf16_f32 v75, v134, v135\n"
    "v_cvt_pk_bf16_f32 v76, v136, v137\n"
    "v_cvt_pk_bf16_f32 v77, v138, v139\n"
    "v_cvt_pk_bf16_f32 v78, v140, v141\n"
    "v_cvt_pk_bf16_f32 v79, v142, v143\n"
    "ds_write_b64 v46, v[72:73] offset:8192\n"
    "ds_write_b64 v46, v[74:75] offset:8704\n"
    "ds_write_b64 v46, v[76:77] offset:9216\n"
    "ds_write_b64 v46, v[78:79] offset:9728\n"
    "s_add_u32 m0, s36, 36864\n"
    "s_add_u32 s84, s1, 28672\n"
    "buffer_load_dwordx4 v14, s[8:11], s84 offen lds\n"
    "s_add_u32 m0, s36, 40960\n"
    "s_add_u32 s84, s1, 229376\n"
    "buffer_load_dwordx4 v14, s[8:11], s84 offen lds\n"
    "s_add_u32 m0, s36, 45056\n"
    "s_add_u32 s84, s1, 258048\n"
    "buffer_load_dwordx4 v14, s[8:11], s84 offen lds\n"
    "s_mov_b32 s87, 13762560\n"
    "buffer_load_dwordx2 v[62:63], v27, s[16:19], s87 offen sc1\n"
    "s_waitcnt lgkmcnt(0)\n"
    "s_barrier\n"
    "ds_read_b64_tr_b16 v[176:177], v47 offset:0\n"
    "ds_read_b64_tr_b16 v[178:179], v47 offset:8192\n"
    "ds_read_b64_tr_b16 v[180:181], v48 offset:0\n"
    "ds_read_b64_tr_b16 v[182:183], v48 offset:8192\n"
    "ds_read_b64_tr_b16 v[184:185], v47 offset:256\n"
    "ds_read_b64_tr_b16 v[186:187], v47 offset:8448\n"
    "ds_read_b64_tr_b16 v[188:189], v48 offset:256\n"
    "ds_read_b64_tr_b16 v[190:191], v48 offset:8448\n"
    "v_add_u32 v20, s36, v31\n"
    "s_lshl_b32 s64, s62, 15\n"
    "s_add_u32 s64, s64, s61\n"
    "s_add_u32 s65, s62, 1\n"
    "s_and_b32 s65, s65, 7\n"
    "s_lshl_b32 s65, s65, 15\n"
    "s_add_u32 s65, s65, s61\n"
    "v_lshlrev_b32 v2, 6, v55\n"
    "v_lshl_add_u32 v2, v56, 3, v2\n"
    "v_and_b32_e64 v59, 1, v54\n"
    "v_lshl_add_u32 v2, v59, 5, v2\n"
    "v_lshl_add_u32 v2, v54, 10, v2\n"
    "v_xor_b32_e64 v3, 32, v2\n"
    "v_add_u32 v4, 0x10000, v2\n"
    "v_add_u32 v5, 0x10000, v3\n"
    "ds_read_u16 v224, v49 offset:0\n"
    "ds_read_u16 v208, v49 offset:2304\n"
    "ds_read_u16 v225, v49 offset:4608\n"
    "ds_read_u16 v209, v49 offset:6912\n"
    "ds_read_u16 v226, v49 offset:9216\n"
    "ds_read_u16 v210, v49 offset:11520\n"
    "ds_read_u16 v227, v49 offset:13824\n"
    "ds_read_u16 v211, v49 offset:16128\n"
    "ds_read_u16 v228, v49 offset:32\n"
    "ds_read_u16 v212, v49 offset:2336\n"
    "ds_read_u16 v229, v49 offset:4640\n"
    "ds_read_u16 v213, v49 offset:6944\n"
    "ds_read_u16 v230, v49 offset:9248\n"
    "ds_read_u16 v214, v49 offset:11552\n"
    "ds_read_u16 v231, v49 offset:13856\n"
    "ds_read_u16 v215, v49 offset:16160\n"
    "ds_read_u16 v232, v49 offset:64\n"
    "ds_read_u16 v216, v49 offset:2368\n"
    "ds_read_u16 v233, v49 offset:4672\n"
    "ds_read_u16 v217, v49 offset:6976\n"
    "ds_read_u16 v234, v49 offset:9280\n"
    "ds_read_u16 v218, v49 offset:11584\n"
    "ds_read_u16 v235, v49 offset:13888\n"
    "ds_read_u16 v219, v49 offset:16192\n"
    "ds_read_u16 v236, v49 offset:96\n"
    "ds_read_u16 v220, v49 offset:2400\n"
    "ds_read_u16 v237, v49 offset:4704\n"
    "ds_read_u16 v221, v49 offset:7008\n"
    "ds_read_u16 v238, v49 offset:9312\n"
    "ds_read_u16 v222, v49 offset:11616\n"
    "ds_read_u16 v239, v49 offset:13920\n"
    "ds_read_u16 v223, v49 offset:16224\n"
    "s_waitcnt lgkmcnt(15)\n"
    "v_lshl_or_b32 v224, v208, 16, v224\n"
    "v_lshl_or_b32 v225, v209, 16, v225\n"
    "v_lshl_or_b32 v226, v210, 16, v226\n"
    "v_lshl_or_b32 v227, v211, 16, v227\n"
    "v_lshl_or_b32 v228, v212, 16, v228\n"
    "v_lshl_or_b32 v229, v213, 16, v229\n"
    "v_lshl_or_b32 v230, v214, 16, v230\n"
    "v_lshl_or_b32 v231, v215, 16, v231\n"
    "s_waitcnt lgkmcnt(14)\n"
    "v_lshl_or_b32 v232, v216, 16, v232\n"
    "s_waitcnt lgkmcnt(12)\n"
    "v_lshl_or_b32 v233, v217, 16, v233\n"
    "s_waitcnt lgkmcnt(10)\n"
    "v_lshl_or_b32 v234, v218, 16, v234\n"
    "s_waitcnt lgkmcnt(8)\n"
    "v_lshl_or_b32 v235, v219, 16, v235\n"
    "s_waitcnt lgkmcnt(6)\n"
    "v_lshl_or_b32 v236, v220, 16, v236\n"
    "s_waitcnt lgkmcnt(4)\n"
    "v_lshl_or_b32 v237, v221, 16, v237\n"
    "s_waitcnt lgkmcnt(2)\n"
    "v_lshl_or_b32 v238, v222, 16, v238\n"
    "s_waitcnt lgkmcnt(0)\n"
    "v_lshl_or_b32 v239, v223, 16, v239\n"
    "v_lshrrev_b32 v244, 2, v30\n"
    "s_lshr_b32 s89, s100, 7\n"
    "s_lshl_b32 s89, s89, 21\n"
    "s_lshl_b32 s88, s48, 19\n"
    "s_add_u32 s89, s89, s88\n"
    "s_bfe_u32 s88, s100, 0x10006\n"
    "s_lshl_b32 s88, s88, 12\n"
    "s_add_u32 s89, s89, s88\n"
    "s_and_b32 s88, s100, 7\n"
    "s_lshl_b32 s88, s88, 9\n"
    "s_add_u32 s89, s89, s88\n"
    "s_bfe_u32 s88, s100, 0x30003\n"
    "s_lshl_b32 s88, s88, 6\n"
    "s_add_u32 s89, s89, s88\n"
    "v_lshl_add_u32 v244, v244, 13, s89\n"
    "v_lshl_add_u32 v244, v56, 4, v244\n"
    "v_and_b32 v245, 1, v55\n"
    "s_and_b32 s89, s100, 7\n"
    "s_lshl_b32 s89, s89, 17\n"
    "s_lshr_b32 s88, s100, 3\n"
    "s_lshl_b32 s88, s88, 11\n"
    "s_add_u32 s89, s89, s88\n"
    "s_lshl_b32 s88, s48, 9\n"
    "s_add_u32 s89, s89, s88\n"
    "s_add_u32 s89, s89, 0xe00000\n"
    "v_lshl_add_u32 v245, v245, 16, s89\n"
    "v_lshrrev_b32 v59, 3, v30\n"
    "v_lshl_add_u32 v245, v59, 6, v245\n"
    "v_lshl_add_u32 v245, v56, 4, v245\n"
    "s_and_b32 s92, s100, 7\n"
    "s_lshl_b32 s92, s92, 5\n"
    "s_lshr_b32 s89, s100, 3\n"
    "s_add_u32 s92, s92, s89\n"
    "s_lshl_b32 s92, s92, 14\n"
    "v_lshrrev_b32 v26, 2, v30\n"
    "s_lshl_b32 s89, s48, 20\n"
    "s_lshr_b32 s88, s92, 8\n"
    "s_add_u32 s89, s89, s88\n"
    "v_lshl_add_u32 v26, v26, 14, s89\n"
    "s_lshr_b32 s88, s100, 3\n"
    "s_bfe_u32 s89, s88, 0x20002\n"
    "v_xor_b32_e64 v59, s89, v56\n"
    "v_lshl_add_u32 v26, v59, 4, v26\n"
    "s_lshr_b32 s96, s92, 9\n"
    "s_mul_i32 s89, s48, 0x1fc00000\n"
    "s_add_u32 s96, s96, s89\n"
    "v_and_b32 v51, 2, v30\n"
    "s_lshr_b32 s89, s100, 5\n"
    "s_lshl_b32 s89, s89, 9\n"
    "s_and_b32 s88, s100, 31\n"
    "s_lshl_b32 s88, s88, 4\n"
    "s_add_u32 s89, s89, s88\n"
    "s_add_u32 s89, s89, 0xc00000\n"
    "v_lshl_add_u32 v51, v51, 12, s89\n"
    "v_and_b32_e64 v59, 1, v54\n"
    "v_lshl_add_u32 v51, v59, 12, v51\n"
    "v_lshl_add_u32 v51, v55, 1, v51\n"
    "v_and_b32_e64 v59, 1, v30\n"
    "v_lshl_add_u32 v51, v59, 3, v51\n"
    "v_mov_b32 v57, 0xffff0000\n"
    "v_mov_b32 v58, 0x10000\n"
    "s_waitcnt vmcnt(14)\n"
    "s_nop 0\n"
    "v_mul_u32_u24_dpp v64, v28, v58 row_newbcast:0 row_mask:0xf bank_mask:0xf\n"
    "v_and_b32_dpp v65, v28, v57 row_newbcast:0 row_mask:0xf bank_mask:0xf\n"
    "v_mul_u32_u24_dpp v66, v29, v58 row_newbcast:0 row_mask:0xf bank_mask:0xf\n"
    "v_and_b32_dpp v67, v29, v57 row_newbcast:0 row_mask:0xf bank_mask:0xf\n"
    "v_mul_u32_u24_dpp v120, v28, v58 row_newbcast:1 row_mask:0xf bank_mask:0xf\n"
    "v_and_b32_dpp v121, v28, v57 row_newbcast:1 row_mask:0xf bank_mask:0xf\n"
    "v_mul_u32_u24_dpp v122, v29, v58 row_newbcast:1 row_mask:0xf bank_mask:0xf\n"
    "v_and_b32_dpp v123, v29, v57 row_newbcast:1 row_mask:0xf bank_mask:0xf\n"
    "v_mul_u32_u24_dpp v72, v28, v58 row_newbcast:2 row_mask:0xf bank_mask:0xf\n"
    "v_and_b32_dpp v73, v28, v57 row_newbcast:2 row_mask:0xf bank_mask:0xf\n"
    "v_mul_u32_u24_dpp v74, v29, v58 row_newbcast:2 row_mask:0xf bank_mask:0xf\n"
    "v_and_b32_dpp v75, v29, v57 row_newbcast:2 row_mask:0xf bank_mask:0xf\n"
    "v_mul_u32_u24_dpp v128, v28, v58 row_newbcast:3 row_mask:0xf bank_mask:0xf\n"
    "v_and_b32_dpp v129, v28, v57 row_newbcast:3 row_mask:0xf bank_mask:0xf\n"
    "v_mul_u32_u24_dpp v130, v29, v58 row_newbcast:3 row_mask:0xf bank_mask:0xf\n"
    "v_and_b32_dpp v131, v29, v57 row_newbcast:3 row_mask:0xf bank_mask:0xf\n"
    "v_mul_u32_u24_dpp v80, v28, v58 row_newbcast:4 row_mask:0xf bank_mask:0xf\n"
    "v_and_b32_dpp v81, v28, v57 row_newbcast:4 row_mask:0xf bank_mask:0xf\n"
    "v_mul_u32_u24_dpp v82, v29, v58 row_newbcast:4 row_mask:0xf bank_mask:0xf\n"
    "v_and_b32_dpp v83, v29, v57 row_newbcast:4 row_mask:0xf bank_mask:0xf\n"
    "v_mul_u32_u24_dpp v136, v28, v58 row_newbcast:5 row_mask:0xf bank_mask:0xf\n"
    "v_and_b32_dpp v137, v28, v57 row_newbcast:5 row_mask:0xf bank_mask:0xf\n"
    "v_mul_u32_u24_dpp v138, v29, v58 row_newbcast:5 row_mask:0xf bank_mask:0xf\n"
    "v_and_b32_dpp v139, v29, v57 row_newbcast:5 row_mask:0xf bank_mask:0xf\n"
    "v_mul_u32_u24_dpp v88, v28, v58 row_newbcast:6 row_mask:0xf bank_mask:0xf\n"
    "v_and_b32_dpp v89, v28, v57 row_newbcast:6 row_mask:0xf bank_mask:0xf\n"
    "v_mul_u32_u24_dpp v90, v29, v58 row_newbcast:6 row_mask:0xf bank_mask:0xf\n"
    "v_and_b32_dpp v91, v29, v57 row_newbcast:6 row_mask:0xf bank_mask:0xf\n"
    "v_mul_u32_u24_dpp v144, v28, v58 row_newbcast:7 row_mask:0xf bank_mask:0xf\n"
    "v_and_b32_dpp v145, v28, v57 row_newbcast:7 row_mask:0xf bank_mask:0xf\n"
    "v_mul_u32_u24_dpp v146, v29, v58 row_newbcast:7 row_mask:0xf bank_mask:0xf\n"
    "v_and_b32_dpp v147, v29, v57 row_newbcast:7 row_mask:0xf bank_mask:0xf\n"
    "s_lshr_b32 s28, s100, 3\n"
    "s_and_b32 s89, s100, 1\n"
    "s_lshl_b32 s89, s89, 2\n"
    "s_add_u32 s28, s28, s89\n"
    "s_and_b32 s28, s28, 7\n"
    "v_mov_b32 v192, v54\n"
    "v_mov_b32 v193, v55\n"
    "v_mov_b32 v194, v56\n"
    "s_waitcnt vmcnt(0)\n"
    "v_cmp_ne_u64 vcc, s[98:99], v[62:63]\n"
    "s_nop 1\n"
    "s_cbranch_vccnz LPLXN_%=s\n"
    "LPLXN_%=:\n"
    "s_add_u32 s85, s64, 0\n"
    "s_nop 0\n"
    "buffer_load_dwordx4 a[0:3], v20, s[16:19], s85 offen offset:0 sc1\n"
    "buffer_load_dwordx4 a[4:7], v20, s[16:19], s85 offen offset:1024 sc1\n"
    "s_add_u32 s85, s64, 8192\n"
    "buffer_load_dwordx4 a[8:11], v20, s[16:19], s85 offen offset:0 sc1\n"
    "buffer_load_dwordx4 a[12:15], v20, s[16:19], s85 offen offset:1024 sc1\n"
    "s_add_u32 s85, s64, 16384\n"
    "buffer_load_dwordx4 a[16:19], v20, s[16:19], s85 offen offset:0 sc1\n"
    "buffer_load_dwordx4 a[20:23], v20, s[16:19], s85 offen offset:1024 sc1\n"
    "s_add_u32 s85, s64, 24576\n"
    "buffer_load_dwordx4 a[24:27], v20, s[16:19], s85 offen offset:0 sc1\n"
    "buffer_load_dwordx4 a[28:31], v20, s[16:19], s85 offen offset:1024 sc1\n"
    "s_add_u32 m0, s36, 49152\n"
    "s_add_u32 s84, s1, 458752\n"
    "buffer_load_dwordx4 v14, s[8:11], s84 offen lds\n"
    "s_mov_b64 exec, 0\n"
    "s_cmp_eq_u32 s48, 0\n"
    "s_cbranch_scc1 LPAD8_%=\n"
    "s_cmp_eq_u32 s48, 1\n"
    "s_cbranch_scc1 LPAD9_%=\n"
    "s_cmp_eq_u32 s48, 2\n"
    "s_cbranch_scc1 LPAD10_%=\n"
    "s_branch LPAD11_%=\n"
    "LPADS_%=:\n"
    "s_mov_b64 exec, -1\n"
    "s_add_u32 s3, s62, 3\n"
    "s_and_b32 s3, s3, 7\n"
    "s_mul_i32 s3, s3, 0xe0000\n"
    "s_add_u32 s4, s62, 4\n"
    "s_and_b32 s4, s4, 7\n"
    "s_add_u32 m0, s36, 53248\n"
    "s_add_u32 s84, s1, 487424\n"
    "buffer_load_dwordx4 v14, s[8:11], s84 offen lds\n"
    "s_mul_i32 s4, s4, 0xe0000\n"
    "s_add_u32 s5, s62, 5\n"
    "s_and_b32 s5, s5, 7\n"
    "s_mul_i32 s5, s5, 0xe0000\n"
    "s_add_u32 s6, s62, 6\n"
    "s_and_b32 s6, s6, 7\n"
    "s_add_u32 m0, s36, 57344\n"
    "s_add_u32 s84, s1, 688128\n"
    "buffer_load_dwordx4 v14, s[8:11], s84 offen lds\n"
    "s_mul_i32 s6, s6, 0xe0000\n"
    "s_add_u32 s7, s62, 7\n"
    "s_and_b32 s7, s7, 7\n"
    "s_mul_i32 s7, s7, 0xe0000\n"
    "s_add_u32 s66, s62, 2\n"
    "s_and_b32 s66, s66, 7\n"
    "s_add_u32 m0, s36, 61440\n"
    "s_add_u32 s84, s1, 716800\n"
    "buffer_load_dwordx4 v14, s[8:11], s84 offen lds\n"
    "s_lshl_b32 s66, s66, 15\n"
    "s_add_u32 s66, s66, s61\n"
    "s_add_u32 s67, s62, 3\n"
    "s_and_b32 s67, s67, 7\n"
    "s_lshl_b32 s67, s67, 15\n"
    "s_add_u32 s67, s67, s61\n"
    "s_add_u32 m0, s36, 65536\n"
    "s_add_u32 s84, s2, 0\n"
    "buffer_load_dwordx4 v14, s[8:11], s84 offen lds\n"
    "s_add_u32 s80, s62, 4\n"
    "s_and_b32 s80, s80, 7\n"
    "s_lshl_b32 s80, s80, 15\n"
    "s_add_u32 s80, s80, s61\n"
    "s_add_u32 s81, s62, 5\n"
    "s_and_b32 s81, s81, 7\n"
    "s_add_u32 m0, s36, 69632\n"
    "s_add_u32 s84, s2, 28672\n"
    "buffer_load_dwordx4 v14, s[8:11], s84 offen lds\n"
    "s_lshl_b32 s81, s81, 15\n"
    "s_add_u32 s81, s81, s61\n"
    "s_add_u32 s82, s62, 6\n"
    "s_and_b32 s82, s82, 7\n"
    "s_lshl_b32 s82, s82, 15\n"
    "s_add_u32 s82, s82, s61\n"
    "s_add_u32 m0, s36, 73728\n"
    "s_add_u32 s84, s2, 229376\n"
    "buffer_load_dwordx4 v14, s[8:11], s84 offen lds\n"
    "s_add_u32 s37, s62, 7\n"
    "s_and_b32 s37, s37, 7\n"
    "s_lshl_b32 s37, s37, 15\n"
    "s_add_u32 s37, s37, s61\n"
    "s_mov_b32 s14, 7340032\n"
    "s_mov_b32 s15, 0x20000\n"
    "s_add_u32 m0, s36, 77824\n"
    "s_add_u32 s84, s2, 258048\n"
    "buffer_load_dwordx4 v14, s[8:11], s84 offen lds\n"
    "s_cmp_eq_u32 s48, 3\n"
    "s_cbranch_scc0 LWA2r_%=\n"
    "v_lshlrev_b32 v52, 16, v52\n"
    "v_lshlrev_b32 v50, 31, v30\n"
    "v_lshlrev_b32 v53, 16, v53\n"
    "v_xor_b32 v53, v53, v50\n"
    "v_lshlrev_b32_e64 v50, 30, v30\n"
    "v_and_b32 v50, 0x80000000, v50\n"
    "v_add_f32_dpp v52, v52, v52 quad_perm:[1,0,3,2] row_mask:0xf bank_mask:0xf\n"
    "v_add_f32_dpp v53, v53, v53 quad_perm:[1,0,3,2] row_mask:0xf bank_mask:0xf\n"
    "v_xor_b32 v32, v52, v50\n"
    "v_xor_b32 v37, v53, v50\n"
    "s_nop 0\n"
    "v_add_f32_dpp v32, v52, v32 quad_perm:[2,3,0,1] row_mask:0xf bank_mask:0xf\n"
    "v_add_f32_dpp v37, v53, v37 quad_perm:[2,3,0,1] row_mask:0xf bank_mask:0xf\n"
    "v_mul_f32 v32, 0x3e000000, v32\n"
    "v_mul_f32 v37, 0x3e000000, v37\n"
    "v_cvt_pk_bf16_f32 v32, v32, v37\n"
    "v_lshlrev_b32 v50, 4, v30\n"
    "v_lshrrev_b32_e64 v32, v50, v32\n"
    "buffer_store_short v32, v51, s[16:19], 0 offen sc1\n"
    "s_nop 1\n"
    "LWA2r_%=:\n"
    "buffer_store_dwordx4 v[248:251], v245, s[16:19], 0 offen sc1\n"
    "s_mov_b32 s97, 8388608\n"
    "buffer_store_dwordx4 v[224:227], v244, s[16:19], s97 offen sc1\n"
    "s_mov_b32 s97, 8519680\n"
    "buffer_store_dwordx4 v[228:231], v244, s[16:19], s97 offen sc1\n"
    "s_mov_b32 s97, 8650752\n"
    "buffer_store_dwordx4 v[232:235], v244, s[16:19], s97 offen sc1\n"
    "s_mov_b32 s97, 8781824\n"
    "buffer_store_dwordx4 v[236:239], v244, s[16:19], s97 offen sc1\n"
    "s_waitcnt vmcnt(19)\n"
    "s_barrier\n"
    "ds_read_b64_tr_b16 v[32:33], v2 offset:0\n"
    "ds_read_b64_tr_b16 v[34:35], v2 offset:4096\n"
    "ds_read_b64_tr_b16 v[36:37], v3 offset:0\n"
    "ds_read_b64_tr_b16 v[38:39], v3 offset:4096\n"
    "ds_read_b64_tr_b16 v[40:41], v2 offset:256\n"
    "ds_read_b64_tr_b16 v[42:43], v2 offset:4352\n"
    "ds_read_b64_tr_b16 v[44:45], v3 offset:256\n"
    "ds_read_b64_tr_b16 v[46:47], v3 offset:4352\n"
    "ds_read_b64_tr_b16 v[48:49], v2 offset:512\n"
    "ds_read_b64_tr_b16 v[50:51], v2 offset:4608\n"
    "s_waitcnt lgkmcnt(6)\n"
    "v_mfma_f32_16x16x32_bf16 v[68:71], v[32:35], a[4:7], v[64:67]\n"
    "s_add_u32 s85, s65, 0\n"
    "ds_read_b64_tr_b16 v[52:53], v3 offset:512\n"
    "ds_read_b64_tr_b16 v[54:55], v3 offset:4608\n"
    "v_mfma_f32_16x16x32_bf16 v[64:67], v[32:35], a[0:3], v[64:67]\n"
    "s_nop 0\n"
    "buffer_load_dwordx4 a[32:35], v20, s[16:19], s85 offen offset:0 sc1\n"
    "v_mfma_f32_16x16x32_bf16 v[124:127], v[36:39], a[4:7], v[120:123]\n"
    "ds_read_b64_tr_b16 v[56:57], v2 offset:768\n"
    "s_add_u32 s44, s43, 0x100\n"
    "v_mfma_f32_16x16x32_bf16 v[120:123], v[36:39], a[0:3], v[120:123]\n"
    "s_waitcnt lgkmcnt(5)\n"
    "ds_read_b64_tr_b16 v[58:59], v2 offset:4864\n"
    "v_mfma_f32_16x16x32_bf16 v[76:79], v[40:43], a[4:7], v[72:75]\n"
    "s_and_b32 s94, s78, 1\n"
    "ds_read_b64_tr_b16 v[32:33], v3 offset:768\n"
    "v_mfma_f32_16x16x32_bf16 v[72:75], v[40:43], a[0:3], v[72:75]\n"
    "s_mul_i32 s94, s94, 0x180\n"
    "ds_read_b64_tr_b16 v[34:35], v3 offset:4864\n"
    "ds_read_b64_tr_b16 v[36:37], v2 offset:8192\n"
    "v_mfma_f32_16x16x32_bf16 v[132:135], v[44:47], a[4:7], v[128:131]\n"
    "s_sub_u32 s44, s44, s94\n"
    "buffer_load_dwordx4 a[36:39], v20, s[16:19], s85 offen offset:1024 sc1\n"
    "v_mfma_f32_16x16x32_bf16 v[128:131], v[44:47], a[0:3], v[128:131]\n"
    "s_waitcnt lgkmcnt(5)\n"
    "ds_read_b64_tr_b16 v[38:39], v2 offset:12288\n"
    "v_mfma_f32_16x16x32_bf16 v[84:87], v[48:51], a[4:7], v[80:83]\n"
    "s_add_u32 s45, s43, 0x180\n"
    "ds_read_b64_tr_b16 v[40:41], v3 offset:8192\n"
    "v_mfma_f32_16x16x32_bf16 v[80:83], v[48:51], a[0:3], v[80:83]\n"
    "s_and_b32 s94, s78, 1\n"
    "ds_read_b64_tr_b16 v[42:43], v3 offset:12288\n"
    "s_mul_i32 s94, s94, 0x240\n"
    "v_mfma_f32_16x16x32_bf16 v[140:143], v[52:55], a[4:7], v[136:139]\n"
    "s_add_u32 s85, s65, 8192\n"
    "ds_read_b64_tr_b16 v[44:45], v2 offset:8448\n"
    "ds_read_b64_tr_b16 v[46:47], v2 offset:12544\n"
    "v_mfma_f32_16x16x32_bf16 v[136:139], v[52:55], a[0:3], v[136:139]\n"
    "s_waitcnt lgkmcnt(6)\n"
    "buffer_load_dwordx4 a[40:43], v20, s[16:19], s85 offen offset:0 sc1\n"
    "v_mfma_f32_16x16x32_bf16 v[92:95], v[56:59], a[4:7], v[88:91]\n"
    "s_sub_u32 s45, s45, s94\n"
    "ds_read_b64_tr_b16 v[48:49], v3 offset:8448\n"
    "v_lshrrev_b32 v15, 5, v30\n"
    "v_mfma_f32_16x16x32_bf16 v[88:91], v[56:59], a[0:3], v[88:91]\n"
    "ds_read_b64_tr_b16 v[50:51], v3 offset:12544\n"
    "v_mul_u32_u24 v15, 0xe000, v15\n"
    "v_mfma_f32_16x16x32_bf16 v[148:151], v[32:35], a[4:7], v[144:147]\n"
    "s_mov_b32 s94, 0x1c00\n"
    "ds_read_b64_tr_b16 v[52:53], v2 offset:8704\n"
    "v_mad_u32_u24 v15, v193, s94, v15\n"
    "v_mfma_f32_16x16x32_bf16 v[144:147], v[32:35], a[0:3], v[144:147]\n"
    "s_waitcnt vmcnt(20) lgkmcnt(5)\n"
    "ds_read_b64_tr_b16 v[54:55], v2 offset:12800\n"
    "ds_read_b64_tr_b16 v[56:57], v3 offset:8704\n"
    "v_mfma_f32_16x16x32_bf16 v[64:67], v[36:39], a[8:11], v[64:67]\n"
    "s_nop 0\n"
    "buffer_load_dwordx4 a[44:47], v20, s[16:19], s85 offen offset:1024 sc1\n"
    "v_mfma_f32_16x16x32_bf16 v[68:71], v[36:39], a[12:15], v[68:71]\n"
    "ds_read_b64_tr_b16 v[58:59], v3 offset:12800\n"
    "v_and_b32_e64 v197, 1, v192\n"
    "v_mfma_f32_16x16x32_bf16 v[120:123], v[40:43], a[8:11], v[120:123]\n"
    "s_add_u32 s85, s65, 16384\n"
    "ds_read_b64_tr_b16 v[32:33], v2 offset:8960\n"
    "v_lshl_add_u32 v15, v197, 6, v15\n"
    "v_mfma_f32_16x16x32_bf16 v[124:127], v[40:43], a[12:15], v[124:127]\n"
    "s_waitcnt lgkmcnt(5)\n"
    "ds_read_b64_tr_b16 v[34:35], v2 offset:13056\n"
    "v_and_b32 v197, 2, v192\n"
    "v_mfma_f32_16x16x32_bf16 v[72:75], v[44:47], a[8:11], v[72:75]\n"
    "s_barrier\n"
    "ds_read_b64_tr_b16 v[36:37], v3 offset:8960\n"
    "ds_read_b64_tr_b16 v[38:39], v3 offset:13056\n"
    "v_mfma_f32_16x16x32_bf16 v[76:79], v[44:47], a[12:15], v[76:79]\n"
    "s_nop 0\n"
    "buffer_load_dwordx4 a[48:51], v20, s[16:19], s85 offen offset:0 sc1\n"
    "v_mfma_f32_16x16x32_bf16 v[128:131], v[48:51], a[8:11], v[128:131]\n"
    "ds_read_b64_tr_b16 v[40:41], v2 offset:16384\n"
    "v_xor_b32_e64 v197, v194, v197\n"
    "v_mfma_f32_16x16x32_bf16 v[132:135], v[48:51], a[12:15], v[132:135]\n"
    "s_waitcnt lgkmcnt(5)\n"
    "ds_read_b64_tr_b16 v[42:43], v2 offset:20480\n"
    "v_lshl_add_u32 v15, v197, 4, v15\n"
    "v_mfma_f32_16x16x32_bf16 v[80:83], v[52:55], a[8:11], v[80:83]\n"
    "s_and_b32 s94, s48, 1\n"
    "ds_read_b64_tr_b16 v[44:45], v3 offset:16384\n"
    "ds_read_b64_tr_b16 v[46:47], v3 offset:20480\n"
    "v_mfma_f32_16x16x32_bf16 v[84:87], v[52:55], a[12:15], v[84:87]\n"
    "s_mul_i32 s94, s94, 0x1c000\n"
    "buffer_load_dwordx4 a[52:55], v20, s[16:19], s85 offen offset:1024 sc1\n"
    "v_mfma_f32_16x16x32_bf16 v[136:139], v[56:59], a[8:11], v[136:139]\n"
    "s_lshr_b32 s89, s48, 1\n"
    "ds_read_b64_tr_b16 v[48:49], v2 offset:16640\n"
    "s_mul_i32 s89, s89, 0x7000\n"
    "LPAD1_%=:\n"
    "s_cbranch_execz LPAD5_%=\n"
    "v_mfma_f32_16x16x32_bf16 v[140:143], v[56:59], a[12:15], v[140:143]\n"
    "s_waitcnt lgkmcnt(5)\n"
    "ds_read_b64_tr_b16 v[50:51], v2 offset:20736\n"
    "v_mfma_f32_16x16x32_bf16 v[88:91], v[32:35], a[8:11], v[88:91]\n"
    "s_add_u32 s94, s94, s89\n"
    "ds_read_b64_tr_b16 v[52:53], v3 offset:16640\n"
    "s_add_u32 s94, s94, s44\n"
    "v_mfma_f32_16x16x32_bf16 v[92:95], v[32:35], a[12:15], v[92:95]\n"
    "s_add_u32 s85, s65, 24576\n"
    "ds_read_b64_tr_b16 v[54:55], v3 offset:20736\n"
    "ds_read_b64_tr_b16 v[56:57], v2 offset:16896\n"
    "v_mfma_f32_16x16x32_bf16 v[144:147], v[36:39], a[8:11], v[144:147]\n"
    "s_nop 0\n"
    "buffer_load_dwordx4 a[56:59], v20, s[16:19], s85 offen offset:0 sc1\n"
    "v_mfma_f32_16x16x32_bf16 v[148:151], v[36:39], a[12:15], v[148:151]\n"
    "s_waitcnt vmcnt(22) lgkmcnt(5)\n"
    "ds_read_b64_tr_b16 v[58:59], v2 offset:20992\n"
    "v_add_u32 v15, s94, v15\n"
    "v_mfma_f32_16x16x32_bf16 v[64:67], v[40:43], a[16:19], v[64:67]\n"
    "ds_read_b64_tr_b16 v[32:33], v3 offset:16896\n"
    "v_lshrrev_b32_e64 v21, 1, v30\n"
    "v_mfma_f32_16x16x32_bf16 v[68:71], v[40:43], a[20:23], v[68:71]\n"
    "s_mul_i32 s94, s76, 0x380\n"
    "ds_read_b64_tr_b16 v[34:35], v3 offset:20992\n"
    "v_mfma_f32_16x16x32_bf16 v[120:123], v[44:47], a[16:19], v[120:123]\n"
    "s_lshr_b32 s89, s100, 3\n"
    "ds_read_b64_tr_b16 v[36:37], v2 offset:17152\n"
    "ds_read_b64_tr_b16 v[38:39], v2 offset:21248\n"
    "v_mfma_f32_16x16x32_bf16 v[124:127], v[44:47], a[20:23], v[124:127]\n"
    "s_waitcnt lgkmcnt(6)\n"
    "buffer_load_dwordx4 a[60:63], v20, s[16:19], s85 offen offset:1024 sc1\n"
    "v_mfma_f32_16x16x32_bf16 v[72:75], v[48:51], a[16:19], v[72:75]\n"
    "s_mul_i32 s89, s89, 28\n"
    "ds_read_b64_tr_b16 v[40:41], v3 offset:17152\n"
    "v_mfma_f32_16x16x32_bf16 v[76:79], v[48:51], a[20:23], v[76:79]\n"
    "s_add_u32 s94, s94, s89\n"
    "ds_read_b64_tr_b16 v[42:43], v3 offset:21248\n"
    "s_mul_i32 s89, s48, 7\n"
    "v_mfma_f32_16x16x32_bf16 v[128:131], v[52:55], a[16:19], v[128:131]\n"
    "s_add_u32 s85, s66, 0\n"
    "ds_read_b64_tr_b16 v[44:45], v2 offset:24576\n"
    "s_add_u32 s94, s94, s89\n"
    "v_mfma_f32_16x16x32_bf16 v[132:135], v[52:55], a[20:23], v[132:135]\n"
    "s_waitcnt lgkmcnt(5)\n"
    "ds_read_b64_tr_b16 v[46:47], v2 offset:28672\n"
    "ds_read_b64_tr_b16 v[48:49], v3 offset:24576\n"
    "v_mfma_f32_16x16x32_bf16 v[80:83], v[56:59], a[16:19], v[80:83]\n"
    "s_lshl_b32 s94, s94, 11\n"
    "buffer_load_dwordx4 a[64:67], v20, s[16:19], s85 offen offset:0 sc1\n"
    "v_mfma_f32_16x16x32_bf16 v[84:87], v[56:59], a[20:23], v[84:87]\n"
    "s_and_b32 s89, s100, 1\n"
    "ds_read_b64_tr_b16 v[50:51], v3 offset:28672\n"
    "v_mfma_f32_16x16x32_bf16 v[136:139], v[32:35], a[16:19], v[136:139]\n"
    "s_lshl_b32 s89, s89, 8\n"
    "ds_read_b64_tr_b16 v[52:53], v2 offset:24832\n"
    "s_add_u32 s94, s94, s89\n"
    "v_mfma_f32_16x16x32_bf16 v[140:143], v[32:35], a[20:23], v[140:143]\n"
    "s_waitcnt lgkmcnt(5)\n"
    "ds_read_b64_tr_b16 v[54:55], v2 offset:28928\n"
    "v_lshl_add_u32 v21, v21, 9, s94\n"
    "v_mfma_f32_16x16x32_bf16 v[88:91], v[36:39], a[16:19], v[88:91]\n"
    "s_nop 0\n"
    "ds_read_b64_tr_b16 v[56:57], v3 offset:24832\n"
    "ds_read_b64_tr_b16 v[58:59], v3 offset:28928\n"
    "v_mfma_f32_16x16x32_bf16 v[92:95], v[36:39], a[20:23], v[92:95]\n"
    "buffer_load_dwordx4 a[68:71], v20, s[16:19], s85 offen offset:1024 sc1\n"
    "v_mfma_f32_16x16x32_bf16 v[144:147], v[40:43], a[16:19], v[144:147]\n"
    "ds_read_b64_tr_b16 v[32:33], v2 offset:25088\n"
    "v_and_b32_e64 v197, 1, v30\n"
    "v_mfma_f32_16x16x32_bf16 v[148:151], v[40:43], a[20:23], v[148:151]\n"
    "s_waitcnt vmcnt(23) lgkmcnt(5)\n"
    "ds_read_b64_tr_b16 v[34:35], v2 offset:29184\n"
    "v_lshl_add_u32 v21, v197, 7, v21\n"
    "v_mfma_f32_16x16x32_bf16 v[64:67], v[44:47], a[24:27], v[64:67]\n"
    "s_add_u32 s85, s66, 8192\n"
    "ds_read_b64_tr_b16 v[36:37], v3 offset:25088\n"
    "ds_read_b64_tr_b16 v[38:39], v3 offset:29184\n"
    "v_mfma_f32_16x16x32_bf16 v[68:71], v[44:47], a[28:31], v[68:71]\n"
    "s_nop 0\n"
    "buffer_load_dwordx4 a[72:75], v20, s[16:19], s85 offen offset:0 sc1\n"
    "v_mfma_f32_16x16x32_bf16 v[120:123], v[48:51], a[24:27], v[120:123]\n"
    "ds_read_b64_tr_b16 v[40:41], v2 offset:25344\n"
    "v_add_u32_e64 v197, 8, v30\n"
    "v_mfma_f32_16x16x32_bf16 v[124:127], v[48:51], a[28:31], v[124:127]\n"
    "s_waitcnt lgkmcnt(5)\n"
    "ds_read_b64_tr_b16 v[42:43], v2 offset:29440\n"
    "v_lshrrev_b32 v197, 6, v197\n"
    "v_mfma_f32_16x16x32_bf16 v[72:75], v[52:55], a[24:27], v[72:75]\n"
    "s_barrier\n"
    "ds_read_b64_tr_b16 v[44:45], v3 offset:25344\n"
    "v_lshl_add_u32 v21, v197, 30, v21\n"
    "v_mfma_f32_16x16x32_bf16 v[76:79], v[52:55], a[28:31], v[76:79]\n"
    "s_lshl_b32 s94, s48, 9\n"
    "ds_read_b64_tr_b16 v[46:47], v3 offset:29440\n"
    "ds_read_b64_tr_b16 v[48:49], v2 offset:32768\n"
    "v_mfma_f32_16x16x32_bf16 v[128:131], v[56:59], a[24:27], v[128:131]\n"
    "buffer_load_dwordx4 a[76:79], v20, s[16:19], s85 offen offset:1024 sc1\n"
    "v_mfma_f32_16x16x32_bf16 v[132:135], v[56:59], a[28:31], v[132:135]\n"
    "s_waitcnt lgkmcnt(5)\n"
    "ds_read_b64_tr_b16 v[50:51], v2 offset:36864\n"
    "v_lshl_add_u32 v25, v192, 11, s94\n"
    "v_mfma_f32_16x16x32_bf16 v[80:83], v[32:35], a[24:27], v[80:83]\n"
    "s_nop 0\n"
    "ds_read_b64_tr_b16 v[52:53], v3 offset:32768\n"
    "v_and_b32_e64 v197, 15, v30\n"
    "v_mfma_f32_16x16x32_bf16 v[84:87], v[32:35], a[28:31], v[84:87]\n"
    "s_add_u32 s84, s2, 458752\n"
    "ds_read_b64_tr_b16 v[54:55], v3 offset:36864\n"
    "v_lshl_add_u32 v25, v197, 5, v25\n"
    "v_mfma_f32_16x16x32_bf16 v[136:139], v[36:39], a[24:27], v[136:139]\n"
    "s_add_u32 m0, s36, 81920\n"
    "ds_read_b64_tr_b16 v[56:57], v2 offset:33024\n"
    "ds_read_b64_tr_b16 v[58:59], v2 offset:37120\n"
    "v_mfma_f32_16x16x32_bf16 v[140:143], v[36:39], a[28:31], v[140:143]\n"
    "s_waitcnt lgkmcnt(6)\n"
    "buffer_load_dwordx4 v14, s[8:11], s84 offen lds\n"
    "v_mfma_f32_16x16x32_bf16 v[88:91], v[40:43], a[24:27], v[88:91]\n"
    "s_nop 0\n"
    "ds_read_b64_tr_b16 v[32:33], v3 offset:33024\n"
    "v_lshlrev_b32_e64 v6, 6, v193\n"
    "v_mfma_f32_16x16x32_bf16 v[92:95], v[40:43], a[28:31], v[92:95]\n"
    "s_add_u32 s84, s2, 487424\n"
    "ds_read_b64_tr_b16 v[34:35], v3 offset:37120\n"
    "v_lshl_add_u32 v6, v194, 3, v6\n"
    "v_mfma_f32_16x16x32_bf16 v[144:147], v[44:47], a[24:27], v[144:147]\n"
    "s_add_u32 m0, s36, 86016\n"
    "ds_read_b64_tr_b16 v[36:37], v2 offset:33280\n"
    "v_and_b32_e64 v197, 1, v192\n"
    "v_mfma_f32_16x16x32_bf16 v[148:151], v[44:47], a[28:31], v[148:151]\n"
    "s_waitcnt vmcnt(11) lgkmcnt(5)\n"
    "ds_read_b64_tr_b16 v[38:39], v2 offset:37376\n"
    "ds_read_b64_tr_b16 v[40:41], v3 offset:33280\n"
    "v_mfma_f32_16x16x32_bf16 v[64:67], v[48:51], a[32:35], v[64:67]\n"
    "s_nop 0\n"
    "buffer_load_dwordx4 v14, s[8:11], s84 offen lds\n"
    "v_mfma_f32_16x16x32_bf16 v[68:71], v[48:51], a[36:39], v[68:71]\n"
    "ds_read_b64_tr_b16 v[42:43], v3 offset:37376\n"
    "v_lshl_add_u32 v6, v197, 5, v6\n"
    "v_mfma_f32_16x16x32_bf16 v[120:123], v[52:55], a[32:35], v[120:123]\n"
    "s_mov_b32 s97, 4194304\n"
    "ds_read_b64_tr_b16 v[44:45], v2 offset:33536\n"
    "v_lshl_add_u32 v6, v192, 9, v6\n"
    "v_mfma_f32_16x16x32_bf16 v[124:127], v[52:55], a[36:39], v[124:127]\n"
    "s_waitcnt lgkmcnt(5)\n"
    "ds_read_b64_tr_b16 v[46:47], v2 offset:37632\n"
    "ds_read_b64_tr_b16 v[48:49], v3 offset:33536\n"
    "v_mfma_f32_16x16x32_bf16 v[72:75], v[56:59], a[32:35], v[72:75]\n"
    "s_nop 0\n"
    "buffer_store_dwordx4 v[176:179], v26, s[16:19], s97 offen sc1\n"
    "v_mfma_f32_16x16x32_bf16 v[76:79], v[56:59], a[36:39], v[76:79]\n"
    "ds_read_b64_tr_b16 v[50:51], v3 offset:37632\n"
    "v_xor_b32_e64 v7, 32, v6\n"
    "v_mfma_f32_16x16x32_bf16 v[128:131], v[32:35], a[32:35], v[128:131]\n"
    "ds_read_b64_tr_b16 v[52:53], v2 offset:40960\n"
    "v_add_u32 v8, 0x10000, v6\n"
    "v_mfma_f32_16x16x32_bf16 v[132:135], v[32:35], a[36:39], v[132:135]\n"
    "s_waitcnt lgkmcnt(5)\n"
    "ds_read_b64_tr_b16 v[54:55], v2 offset:45056\n"
    "v_add_u32 v9, 0x10000, v7\n"
    "v_mfma_f32_16x16x32_bf16 v[80:83], v[36:39], a[32:35], v[80:83]\n"
    "s_nop 0\n"
    "ds_read_b64_tr_b16 v[56:57], v3 offset:40960\n"
    "v_lshlrev_b32_e64 v10, 6, v193\n"
    "v_mfma_f32_16x16x32_bf16 v[84:87], v[36:39], a[36:39], v[84:87]\n"
    "ds_read_b64_tr_b16 v[58:59], v3 offset:45056\n"
    "v_lshl_add_u32 v10, v194, 3, v10\n"
    "v_mfma_f32_16x16x32_bf16 v[136:139], v[40:43], a[32:35], v[136:139]\n"
    "s_mov_b32 s97, 4456448\n"
    "ds_read_b64_tr_b16 v[32:33], v2 offset:41216\n"
    "ds_read_b64_tr_b16 v[34:35], v2 offset:45312\n"
    "v_mfma_f32_16x16x32_bf16 v[140:143], v[40:43], a[36:39], v[140:143]\n"
    "s_waitcnt lgkmcnt(6)\n"
    "buffer_store_dwordx4 v[180:183], v26, s[16:19], s97 offen sc1\n"
    "v_mfma_f32_16x16x32_bf16 v[88:91], v[44:47], a[32:35], v[88:91]\n"
    "s_nop 0\n"
    "ds_read_b64_tr_b16 v[36:37], v3 offset:41216\n"
    "v_and_b32_e64 v197, 1, v192\n"
    "v_mfma_f32_16x16x32_bf16 v[92:95], v[44:47], a[36:39], v[92:95]\n"
    "ds_read_b64_tr_b16 v[38:39], v3 offset:45312\n"
    "v_lshl_add_u32 v10, v197, 5, v10\n"
    "v_mfma_f32_16x16x32_bf16 v[144:147], v[48:51], a[32:35], v[144:147]\n"
    "ds_read_b64_tr_b16 v[40:41], v2 offset:41472\n"
    "v_lshl_add_u32 v10, v192, 8, v10\n"
    "v_mfma_f32_16x16x32_bf16 v[148:151], v[48:51], a[36:39], v[148:151]\n"
    "s_waitcnt vmcnt(12) lgkmcnt(5)\n"
    "ds_read_b64_tr_b16 v[42:43], v2 offset:45568\n"
    "v_xor_b32 v11, 32, v10\n"
    "v_mfma_f32_16x16x32_bf16 v[64:67], v[52:55], a[40:43], v[64:67]\n"
    "ds_read_b64_tr_b16 v[44:45], v3 offset:41472\n"
    "v_add_u32 v12, 0x10000, v10\n"
    "v_mfma_f32_16x16x32_bf16 v[68:71], v[52:55], a[44:47], v[68:71]\n"
    "s_mov_b32 s97, 4718592\n"
    "ds_read_b64_tr_b16 v[46:47], v3 offset:45568\n"
    "ds_read_b64_tr_b16 v[48:49], v2 offset:41728\n"
    "v_mfma_f32_16x16x32_bf16 v[120:123], v[56:59], a[40:43], v[120:123]\n"
    "buffer_store_dwordx4 v[184:187], v26, s[16:19], s97 offen sc1\n"
    "v_mfma_f32_16x16x32_bf16 v[124:127], v[56:59], a[44:47], v[124:127]\n"
    "s_waitcnt lgkmcnt(5)\n"
    "ds_read_b64_tr_b16 v[50:51], v2 offset:45824\n"
    "v_add_u32 v13, 0x10000, v11\n"
    "v_mfma_f32_16x16x32_bf16 v[72:75], v[32:35], a[40:43], v[72:75]\n"
    "s_barrier\n"
    "ds_read_b64_tr_b16 v[52:53], v3 offset:41728\n"
    "v_mfma_f32_16x16x32_bf16 v[76:79], v[32:35], a[44:47], v[76:79]\n"
    "s_lshr_b32 s94, s48, 1\n"
    "ds_read_b64_tr_b16 v[54:55], v3 offset:45824\n"
    "v_mfma_f32_16x16x32_bf16 v[128:131], v[36:39], a[40:43], v[128:131]\n"
    "s_lshl_b32 s94, s94, 11\n"
    "ds_read_b64_tr_b16 v[56:57], v2 offset:49152\n"
    "s_and_b32 s89, s48, 1\n"
    "v_mfma_f32_16x16x32_bf16 v[132:135], v[36:39], a[44:47], v[132:135]\n"
    "s_waitcnt lgkmcnt(5)\n"
    "ds_read_b64_tr_b16 v[58:59], v2 offset:53248\n"
    "s_lshl_b32 s89, s89, 8\n"
    "v_mfma_f32_16x16x32_bf16 v[80:83], v[40:43], a[40:43], v[80:83]\n"
    "s_mov_b32 s97, 4980736\n"
    "ds_read_b64_tr_b16 v[32:33], v3 offset:49152\n"
    "ds_read_b64_tr_b16 v[34:35], v3 offset:53248\n"
    "v_mfma_f32_16x16x32_bf16 v[84:87], v[40:43], a[44:47], v[84:87]\n"
    "s_add_u32 s94, s94, s89\n"
    "buffer_store_dwordx4 v[188:191], v26, s[16:19], s97 offen sc1\n"
    "v_mfma_f32_16x16x32_bf16 v[136:139], v[44:47], a[40:43], v[136:139]\n"
    "s_lshl_b32 s89, s76, 2\n"
    "ds_read_b64_tr_b16 v[36:37], v2 offset:49408\n"
    "s_add_u32 s89, s89, s78\n"
    "v_mfma_f32_16x16x32_bf16 v[140:143], v[44:47], a[44:47], v[140:143]\n"
    "s_waitcnt lgkmcnt(5)\n"
    "ds_read_b64_tr_b16 v[38:39], v2 offset:53504\n"
    "v_mfma_f32_16x16x32_bf16 v[88:91], v[48:51], a[40:43], v[88:91]\n"
    "s_lshl_b32 s89, s89, 16\n"
    "ds_read_b64_tr_b16 v[40:41], v3 offset:49408\n"
    "s_add_u32 s94, s94, s89\n"
    "v_mfma_f32_16x16x32_bf16 v[92:95], v[48:51], a[44:47], v[92:95]\n"
    "s_add_u32 s84, s2, 688128\n"
    "ds_read_b64_tr_b16 v[42:43], v3 offset:53504\n"
    "s_add_u32 s94, s94, 0xe00000\n"
    "v_mfma_f32_16x16x32_bf16 v[144:147], v[52:55], a[40:43], v[144:147]\n"
    "s_add_u32 m0, s36, 90112\n"
    "ds_read_b64_tr_b16 v[44:45], v2 offset:49664\n"
    "v_lshl_add_u32 v16, v192, 9, s94\n"
    "v_mfma_f32_16x16x32_bf16 v[148:151], v[52:55], a[44:47], v[148:151]\n"
    "s_waitcnt vmcnt(12) lgkmcnt(5)\n"
    "ds_read_b64_tr_b16 v[46:47], v2 offset:53760\n"
    "ds_read_b64_tr_b16 v[48:49], v3 offset:49664\n"
    "v_mfma_f32_16x16x32_bf16 v[64:67], v[56:59], a[48:51], v[64:67]\n"
    "buffer_load_dwordx4 v14, s[8:11], s84 offen lds\n"
    "v_mfma_f32_16x16x32_bf16 v[68:71], v[56:59], a[52:55], v[68:71]\n"
    "ds_read_b64_tr_b16 v[50:51], v3 offset:53760\n"
    "v_lshl_add_u32 v16, v193, 6, v16\n"
    "v_mfma_f32_16x16x32_bf16 v[120:123], v[32:35], a[48:51], v[120:123]\n"
    "s_add_u32 s84, s2, 716800\n"
    "ds_read_b64_tr_b16 v[52:53], v2 offset:49920\n"
    "v_and_b32_e64 v197, 1, v192\n"
    "v_mfma_f32_16x16x32_bf16 v[124:127], v[32:35], a[52:55], v[124:127]\n"
    "s_waitcnt lgkmcnt(5)\n"
    "ds_read_b64_tr_b16 v[54:55], v2 offset:54016\n"
    "v_lshlrev_b32 v197, 1, v197\n"
    "v_mfma_f32_16x16x32_bf16 v[72:75], v[36:39], a[48:51], v[72:75]\n"
    "s_add_u32 m0, s36, 94208\n"
    "ds_read_b64_tr_b16 v[56:57], v3 offset:49920\n"
    "ds_read_b64_tr_b16 v[58:59], v3 offset:54016\n"
    "v_mfma_f32_16x16x32_bf16 v[76:79], v[36:39], a[52:55], v[76:79]\n"
    "buffer_load_dwordx4 v14, s[8:11], s84 offen lds\n"
    "v_mfma_f32_16x16x32_bf16 v[128:131], v[40:43], a[48:51], v[128:131]\n"
    "ds_read_b64_tr_b16 v[32:33], v2 offset:57344\n"
    "v_xor_b32_e64 v197, v194, v197\n"
    "v_mfma_f32_16x16x32_bf16 v[132:135], v[40:43], a[52:55], v[132:135]\n"
    "s_waitcnt lgkmcnt(5)\n"
    "ds_read_b64_tr_b16 v[34:35], v2 offset:61440\n"
    "v_lshl_add_u32 v16, v197, 4, v16\n"
    "v_mfma_f32_16x16x32_bf16 v[80:83], v[44:47], a[48:51], v[80:83]\n"
    "s_add_u32 s85, s66, 16384\n"
    "ds_read_b64_tr_b16 v[36:37], v3 offset:57344\n"
    "ds_read_b64_tr_b16 v[38:39], v3 offset:61440\n"
    "v_mfma_f32_16x16x32_bf16 v[84:87], v[44:47], a[52:55], v[84:87]\n"
    "s_lshl_b32 s46, s78, 9\n"
    "buffer_load_dwordx4 a[80:83], v20, s[16:19], s85 offen offset:0 sc1\n"
    "v_mfma_f32_16x16x32_bf16 v[136:139], v[48:51], a[48:51], v[136:139]\n"
    "s_lshl_b32 s94, s76, 7\n"
    "ds_read_b64_tr_b16 v[40:41], v2 offset:57600\n"
    "s_add_u32 s46, s46, s94\n"
    "v_mfma_f32_16x16x32_bf16 v[140:143], v[48:51], a[52:55], v[140:143]\n"
    "s_waitcnt lgkmcnt(5)\n"
    "ds_read_b64_tr_b16 v[42:43], v2 offset:61696\n"
    "v_mfma_f32_16x16x32_bf16 v[88:91], v[52:55], a[48:51], v[88:91]\n"
    "s_lshl_b32 s94, s48, 13\n"
    "ds_read_b64_tr_b16 v[44:45], v3 offset:57600\n"
    "v_lshl_add_u32 v17, v193, 11, s94\n"
    "v_mfma_f32_16x16x32_bf16 v[92:95], v[52:55], a[52:55], v[92:95]\n"
    "ds_read_b64_tr_b16 v[46:47], v3 offset:61696\n"
    "ds_read_b64_tr_b16 v[48:49], v2 offset:57856\n"
    "v_mfma_f32_16x16x32_bf16 v[144:147], v[56:59], a[48:51], v[144:147]\n"
    "buffer_load_dwordx4 a[84:87], v20, s[16:19], s85 offen offset:1024 sc1\n"
    "v_mfma_f32_16x16x32_bf16 v[148:151], v[56:59], a[52:55], v[148:151]\n"
    "s_waitcnt vmcnt(14) lgkmcnt(5)\n"
    "ds_read_b64_tr_b16 v[50:51], v2 offset:61952\n"
    "v_lshl_add_u32 v17, v192, 6, v17\n"
    "v_mfma_f32_16x16x32_bf16 v[64:67], v[32:35], a[56:59], v[64:67]\n"
    "s_and_b32 s94, s48, 1\n"
    "ds_read_b64_tr_b16 v[52:53], v3 offset:57856\n"
    "v_mfma_f32_16x16x32_bf16 v[68:71], v[32:35], a[60:63], v[68:71]\n"
    "s_lshl_b32 s94, s94, 1\n"
    "ds_read_b64_tr_b16 v[54:55], v3 offset:61952\n"
    "v_xor_b32 v197, s94, v194\n"
    "v_mfma_f32_16x16x32_bf16 v[120:123], v[36:39], a[56:59], v[120:123]\n"
    "s_add_u32 s85, s66, 24576\n"
    "ds_read_b64_tr_b16 v[56:57], v2 offset:58112\n"
    "ds_read_b64_tr_b16 v[58:59], v2 offset:62208\n"
    "v_mfma_f32_16x16x32_bf16 v[124:127], v[36:39], a[60:63], v[124:127]\n"
    "s_waitcnt lgkmcnt(6)\n"
    "buffer_load_dwordx4 a[88:91], v20, s[16:19], s85 offen offset:0 sc1\n"
    "v_mfma_f32_16x16x32_bf16 v[72:75], v[40:43], a[56:59], v[72:75]\n"
    "s_barrier\n"
    "ds_read_b64_tr_b16 v[32:33], v3 offset:58112\n"
    "v_lshl_add_u32 v17, v197, 4, v17\n"
    "v_mfma_f32_16x16x32_bf16 v[76:79], v[40:43], a[60:63], v[76:79]\n"
    "s_xor_b32 s94, s46, 0x100\n"
    "ds_read_b64_tr_b16 v[34:35], v3 offset:62208\n"
    "v_xor_b32_e64 v17, s94, v17\n"
    "v_mfma_f32_16x16x32_bf16 v[128:131], v[44:47], a[56:59], v[128:131]\n"
    "ds_read_b64_tr_b16 v[36:37], v4 offset:0\n"
    "ds_read_b64_tr_b16 v[38:39], v4 offset:4096\n"
    "v_mfma_f32_16x16x32_bf16 v[132:135], v[44:47], a[60:63], v[132:135]\n"
    "s_waitcnt lgkmcnt(6)\n"
    "buffer_load_dwordx4 a[92:95], v20, s[16:19], s85 offen offset:1024 sc1\n"
    "v_mfma_f32_16x16x32_bf16 v[80:83], v[48:51], a[56:59], v[80:83]\n"
    "s_nop 0\n"
    "ds_read_b64_tr_b16 v[40:41], v5 offset:0\n"
    "v_lshrrev_b32_e64 v19, 5, v30\n"
    "v_mfma_f32_16x16x32_bf16 v[84:87], v[48:51], a[60:63], v[84:87]\n"
    "s_add_u32 s84, s3, 0\n"
    "ds_read_b64_tr_b16 v[42:43], v5 offset:4096\n"
    "s_lshl_b32 s94, s48, 14\n"
    "v_mfma_f32_16x16x32_bf16 v[136:139], v[52:55], a[56:59], v[136:139]\n"
    "s_add_u32 m0, s36, 98304\n"
    "ds_read_b64_tr_b16 v[44:45], v4 offset:256\n"
    "s_add_u32 s94, s94, s46\n"
    "v_mfma_f32_16x16x32_bf16 v[140:143], v[52:55], a[60:63], v[140:143]\n"
    "s_waitcnt lgkmcnt(5)\n"
    "ds_read_b64_tr_b16 v[46:47], v4 offset:4352\n"
    "ds_read_b64_tr_b16 v[48:49], v5 offset:256\n"
    "v_mfma_f32_16x16x32_bf16 v[88:91], v[56:59], a[56:59], v[88:91]\n"
    "buffer_load_dwordx4 v14, s[8:11], s84 offen lds\n"
    "v_mfma_f32_16x16x32_bf16 v[92:95], v[56:59], a[60:63], v[92:95]\n"
    "ds_read_b64_tr_b16 v[50:51], v5 offset:4352\n"
    "v_lshl_add_u32 v19, v19, 13, s94\n"
    "v_mfma_f32_16x16x32_bf16 v[144:147], v[32:35], a[56:59], v[144:147]\n"
    "s_add_u32 s84, s3, 28672\n"
    "ds_read_b64_tr_b16 v[52:53], v4 offset:512\n"
    "v_lshl_add_u32 v19, v193, 11, v19\n"
    "v_mfma_f32_16x16x32_bf16 v[148:151], v[32:35], a[60:63], v[148:151]\n"
    "s_waitcnt vmcnt(15) lgkmcnt(5)\n"
    "ds_read_b64_tr_b16 v[54:55], v4 offset:4608\n"
    "v_and_b32 v197, 1, v192\n"
    "v_mfma_f32_16x16x32_bf16 v[64:67], v[36:39], a[64:67], v[64:67]\n"
    "s_add_u32 m0, s36, 102400\n"
    "ds_read_b64_tr_b16 v[56:57], v5 offset:512\n"
    "ds_read_b64_tr_b16 v[58:59], v5 offset:4608\n"
    "v_mfma_f32_16x16x32_bf16 v[68:71], v[36:39], a[68:71], v[68:71]\n"
    "buffer_load_dwordx4 v14, s[8:11], s84 offen lds\n"
    "v_mfma_f32_16x16x32_bf16 v[120:123], v[40:43], a[64:67], v[120:123]\n"
    "s_add_u32 s84, s3, 229376\n"
    "ds_read_b64_tr_b16 v[32:33], v4 offset:768\n"
    "v_lshl_add_u32 v19, v197, 6, v19\n"
    "v_mfma_f32_16x16x32_bf16 v[124:127], v[40:43], a[68:71], v[124:127]\n"
    "s_waitcnt lgkmcnt(5)\n"
    "ds_read_b64_tr_b16 v[34:35], v4 offset:4864\n"
    "v_and_b32 v197, 2, v192\n"
    "v_mfma_f32_16x16x32_bf16 v[72:75], v[44:47], a[64:67], v[72:75]\n"
    "s_add_u32 m0, s36, 106496\n"
    "ds_read_b64_tr_b16 v[36:37], v5 offset:768\n"
    "ds_read_b64_tr_b16 v[38:39], v5 offset:4864\n"
    "v_mfma_f32_16x16x32_bf16 v[76:79], v[44:47], a[68:71], v[76:79]\n"
    "buffer_load_dwordx4 v14, s[8:11], s84 offen lds\n"
    "v_mfma_f32_16x16x32_bf16 v[128:131], v[48:51], a[64:67], v[128:131]\n"
    "s_add_u32 m0, s36, 110592\n"
    "ds_read_b64_tr_b16 v[40:41], v4 offset:8192\n"
    "v_xor_b32_e64 v197, v194, v197\n"
    "v_mfma_f32_16x16x32_bf16 v[132:135], v[48:51], a[68:71], v[132:135]\n"
    "s_waitcnt lgkmcnt(5)\n"
    "ds_read_b64_tr_b16 v[42:43], v4 offset:12288\n"
    "ds_read_b64_tr_b16 v[44:45], v5 offset:8192\n"
    "v_mfma_f32_16x16x32_bf16 v[80:83], v[52:55], a[64:67], v[80:83]\n"
    "s_add_u32 s84, s3, 258048\n"
    "buffer_load_dwordx4 v14, s[8:11], s84 offen lds\n"
    "v_mfma_f32_16x16x32_bf16 v[84:87], v[52:55], a[68:71], v[84:87]\n"
    "s_nop 0\n"
    "ds_read_b64_tr_b16 v[46:47], v5 offset:12288\n"
    "v_lshl_add_u32 v19, v197, 4, v19\n"
    "v_mfma_f32_16x16x32_bf16 v[136:139], v[56:59], a[64:67], v[136:139]\n"
    "ds_read_b64_tr_b16 v[48:49], v4 offset:8448\n"
    "v_xor_b32 v18, 0x80, v19\n"
    "v_mfma_f32_16x16x32_bf16 v[140:143], v[56:59], a[68:71], v[140:143]\n"
    "s_waitcnt lgkmcnt(5)\n"
    "ds_read_b64_tr_b16 v[50:51], v4 offset:12544\n"
    "v_max_u32 v24, 1, v192\n"
    "v_mfma_f32_16x16x32_bf16 v[88:91], v[32:35], a[64:67], v[88:91]\n"
    "s_add_u32 s85, s67, 0\n"
    "ds_read_b64_tr_b16 v[52:53], v5 offset:8448\n"
    "ds_read_b64_tr_b16 v[54:55], v5 offset:12544\n"
    "v_mfma_f32_16x16x32_bf16 v[92:95], v[32:35], a[68:71], v[92:95]\n"
    "s_nop 0\n"
    "buffer_load_dwordx4 a[96:99], v20, s[16:19], s85 offen offset:0 sc1\n"
    "v_mfma_f32_16x16x32_bf16 v[144:147], v[36:39], a[64:67], v[144:147]\n"
    "ds_read_b64_tr_b16 v[56:57], v4 offset:8704\n"
    "v_lshlrev_b32_e64 v24, 9, v24\n"
    "v_mfma_f32_16x16x32_bf16 v[148:151], v[36:39], a[68:71], v[148:151]\n"
    "s_waitcnt vmcnt(17) lgkmcnt(5)\n"
    "ds_read_b64_tr_b16 v[58:59], v4 offset:12800\n"
    "v_xor_b32 v24, v23, v24\n"
    "v_mfma_f32_16x16x32_bf16 v[64:67], v[40:43], a[72:75], v[64:67]\n"
    "s_mul_i32 s94, s48, 0xc0000\n"
    "ds_read_b64_tr_b16 v[32:33], v5 offset:8704\n"
    "v_mfma_f32_16x16x32_bf16 v[68:71], v[40:43], a[76:79], v[68:71]\n"
    "s_sub_u32 s29, 0xf00000, s94\n"
    "ds_read_b64_tr_b16 v[34:35], v5 offset:12800\n"
    "ds_read_b64_tr_b16 v[36:37], v4 offset:8960\n"
    "v_mfma_f32_16x16x32_bf16 v[120:123], v[44:47], a[72:75], v[120:123]\n"
    "s_and_b32 s94, s48, 1\n"
    "buffer_load_dwordx4 a[100:103], v20, s[16:19], s85 offen offset:1024 sc1\n"
    "v_mfma_f32_16x16x32_bf16 v[124:127], v[44:47], a[76:79], v[124:127]\n"
    "s_waitcnt vmcnt(10) lgkmcnt(5)\n"
    "ds_read_b64_tr_b16 v[38:39], v4 offset:13056\n"
    "s_or_b32 s101, s100, s94\n"
    "v_mfma_f32_16x16x32_bf16 v[72:75], v[48:51], a[72:75], v[72:75]\n"
    "s_barrier\n"
    "ds_read_b64_tr_b16 v[40:41], v5 offset:8960\n"
    "v_mfma_f32_16x16x32_bf16 v[76:79], v[48:51], a[76:79], v[76:79]\n"
    "s_lshr_b32 s93, s100, 3\n"
    "ds_read_b64_tr_b16 v[42:43], v5 offset:13056\n"
    "s_and_b32 s94, s100, 7\n"
    "v_mfma_f32_16x16x32_bf16 v[128:131], v[52:55], a[72:75], v[128:131]\n"
    "s_add_u32 s83, s96, 13893632\n"
    "ds_read_b64_tr_b16 v[44:45], v4 offset:16384\n"
    "ds_read_b64_tr_b16 v[46:47], v4 offset:20480\n"
    "v_mfma_f32_16x16x32_bf16 v[132:135], v[52:55], a[76:79], v[132:135]\n"
    "s_waitcnt lgkmcnt(6)\n"
    "buffer_store_dwordx2 v[60:61], off, s[16:19], s83 sc1\n"
    "v_mfma_f32_16x16x32_bf16 v[80:83], v[56:59], a[72:75], v[80:83]\n"
    "s_lshl_b32 s94, s94, 6\n"
    "ds_read_b64_tr_b16 v[48:49], v5 offset:16384\n"
    "v_mfma_f32_16x16x32_bf16 v[84:87], v[56:59], a[76:79], v[84:87]\n"
    "s_add_u32 s93, s93, s94\n"
    "ds_read_b64_tr_b16 v[50:51], v5 offset:20480\n"
    "s_and_b32 s94, s48, 1\n"
    "v_mfma_f32_16x16x32_bf16 v[136:139], v[32:35], a[72:75], v[136:139]\n"
    "s_add_u32 s85, s67, 8192\n"
    "ds_read_b64_tr_b16 v[52:53], v4 offset:16640\n"
    "ds_read_b64_tr_b16 v[54:55], v4 offset:20736\n"
    "v_mfma_f32_16x16x32_bf16 v[140:143], v[32:35], a[76:79], v[140:143]\n"
    "s_waitcnt lgkmcnt(6)\n"
    "buffer_load_dwordx4 a[104:107], v20, s[16:19], s85 offen offset:0 sc1\n"
    "v_mfma_f32_16x16x32_bf16 v[88:91], v[36:39], a[72:75], v[88:91]\n"
    "s_lshl_b32 s94, s94, 5\n"
    "ds_read_b64_tr_b16 v[56:57], v5 offset:16640\n"
    "v_mfma_f32_16x16x32_bf16 v[92:95], v[36:39], a[76:79], v[92:95]\n"
    "s_add_u32 s93, s93, s94\n"
    "ds_read_b64_tr_b16 v[58:59], v5 offset:20736\n"
    "v_mfma_f32_16x16x32_bf16 v[144:147], v[40:43], a[72:75], v[144:147]\n"
    "s_lshl_b32 s93, s93, 13\n"
    "ds_read_b64_tr_b16 v[32:33], v4 offset:16896\n"
    "s_lshr_b32 s94, s48, 1\n"
    "v_mfma_f32_16x16x32_bf16 v[148:151], v[40:43], a[76:79], v[148:151]\n"
    "s_waitcnt vmcnt(10) lgkmcnt(5)\n"
    "ds_read_b64_tr_b16 v[34:35], v4 offset:20992\n"
    "ds_read_b64_tr_b16 v[36:37], v5 offset:16896\n"
    "v_mfma_f32_16x16x32_bf16 v[64:67], v[44:47], a[80:83], v[64:67]\n"
    "s_lshl_b32 s94, s94, 12\n"
    "buffer_load_dwordx4 a[108:111], v20, s[16:19], s85 offen offset:1024 sc1\n"
    "v_mfma_f32_16x16x32_bf16 v[68:71], v[44:47], a[84:87], v[68:71]\n"
    "s_add_u32 s93, s93, s94\n"
    "ds_read_b64_tr_b16 v[38:39], v5 offset:20992\n"
    "s_add_u32 s93, s93, 0x800000\n"
    "v_mfma_f32_16x16x32_bf16 v[120:123], v[48:51], a[80:83], v[120:123]\n"
    "s_add_u32 s84, s3, 458752\n"
    "ds_read_b64_tr_b16 v[40:41], v4 offset:17152\n"
    "s_lshl_b32 s94, s48, 10\n"
    "v_mfma_f32_16x16x32_bf16 v[124:127], v[48:51], a[84:87], v[124:127]\n"
    "s_waitcnt lgkmcnt(5)\n"
    "ds_read_b64_tr_b16 v[42:43], v4 offset:21248\n"
    "s_sub_u32 s93, s93, s94\n"
    "v_mfma_f32_16x16x32_bf16 v[72:75], v[52:55], a[80:83], v[72:75]\n"
    "s_add_u32 m0, s36, 114688\n"
    "ds_read_b64_tr_b16 v[44:45], v5 offset:17152\n"
    "ds_read_b64_tr_b16 v[46:47], v5 offset:21248\n"
    "v_mfma_f32_16x16x32_bf16 v[76:79], v[52:55], a[84:87], v[76:79]\n"
    "s_add_u32 s94, s101, -1\n"
    "buffer_load_dwordx4 v14, s[8:11], s84 offen lds\n"
    "v_mfma_f32_16x16x32_bf16 v[128:131], v[56:59], a[80:83], v[128:131]\n"
    "s_add_u32 m0, s36, 118784\n"
    "ds_read_b64_tr_b16 v[48:49], v4 offset:24576\n"
    "s_lshr_b32 s94, s94, 31\n"
    "v_mfma_f32_16x16x32_bf16 v[132:135], v[56:59], a[84:87], v[132:135]\n"
    "s_waitcnt lgkmcnt(5)\n"
    "ds_read_b64_tr_b16 v[50:51], v4 offset:28672\n"
    "ds_read_b64_tr_b16 v[52:53], v5 offset:24576\n"
    "v_mfma_f32_16x16x32_bf16 v[80:83], v[32:35], a[80:83], v[80:83]\n"
    "s_add_u32 s84, s3, 487424\n"
    "buffer_load_dwordx4 v14, s[8:11], s84 offen lds\n"
    "v_mfma_f32_16x16x32_bf16 v[84:87], v[32:35], a[84:87], v[84:87]\n"
    "s_add_u32 m0, s36, 122880\n"
    "ds_read_b64_tr_b16 v[54:55], v5 offset:28672\n"
    "ds_read_b64_tr_b16 v[56:57], v4 offset:24832\n"
    "v_mfma_f32_16x16x32_bf16 v[136:139], v[36:39], a[80:83], v[136:139]\n"
    "s_add_u32 s84, s3, 688128\n"
    "buffer_load_dwordx4 v14, s[8:11], s84 offen lds\n"
    "v_mfma_f32_16x16x32_bf16 v[140:143], v[36:39], a[84:87], v[140:143]\n"
    "s_waitcnt lgkmcnt(5)\n"
    "ds_read_b64_tr_b16 v[58:59], v4 offset:28928\n"
    "ds_read_b64_tr_b16 v[32:33], v5 offset:24832\n"
    "v_mfma_f32_16x16x32_bf16 v[88:91], v[40:43], a[80:83], v[88:91]\n"
    "s_add_u32 m0, s36, 126976\n"
    "s_add_u32 s84, s3, 716800\n"
    "buffer_load_dwordx4 v14, s[8:11], s84 offen lds\n"
    "v_mfma_f32_16x16x32_bf16 v[92:95], v[40:43], a[84:87], v[92:95]\n"
    "s_lshl_b32 s94, s94, 22\n"
    "ds_read_b64_tr_b16 v[34:35], v5 offset:28928\n"
    "v_mfma_f32_16x16x32_bf16 v[144:147], v[44:47], a[80:83], v[144:147]\n"
    "s_add_u32 s93, s93, s94\n"
    "ds_read_b64_tr_b16 v[36:37], v4 offset:25088\n"
    "v_lshrrev_b32 v216, 5, v30\n"
    "v_mfma_f32_16x16x32_bf16 v[148:151], v[44:47], a[84:87], v[148:151]\n"
    "s_waitcnt vmcnt(13) lgkmcnt(5)\n"
    "ds_read_b64_tr_b16 v[38:39], v4 offset:29184\n"
    "s_lshr_b32 s94, s48, 1\n"
    "v_mfma_f32_16x16x32_bf16 v[64:67], v[48:51], a[88:91], v[64:67]\n"
    "s_add_u32 s85, s67, 16384\n"
    "ds_read_b64_tr_b16 v[40:41], v5 offset:25088\n"
    "ds_read_b64_tr_b16 v[42:43], v5 offset:29184\n"
    "v_mfma_f32_16x16x32_bf16 v[68:71], v[48:51], a[92:95], v[68:71]\n"
    "s_lshl_b32 s94, s94, 3\n"
    "buffer_load_dwordx4 a[112:115], v20, s[16:19], s85 offen offset:0 sc1\n"
    "v_mfma_f32_16x16x32_bf16 v[120:123], v[52:55], a[88:91], v[120:123]\n"
    "s_add_u32 s94, s94, 0x24000\n"
    "ds_read_b64_tr_b16 v[44:45], v4 offset:25344\n"
    "v_lshl_add_u32 v216, v216, 11, s94\n"
    "v_mfma_f32_16x16x32_bf16 v[124:127], v[52:55], a[92:95], v[124:127]\n"
    "s_waitcnt vmcnt(10) lgkmcnt(5)\n"
    "ds_read_b64_tr_b16 v[46:47], v4 offset:29440\n"
    "ds_read_b64_tr_b16 v[48:49], v5 offset:25344\n"
    "v_mfma_f32_16x16x32_bf16 v[72:75], v[56:59], a[88:91], v[72:75]\n"
    "s_barrier\n"
    "buffer_load_dwordx4 a[116:119], v20, s[16:19], s85 offen offset:1024 sc1\n"
    "v_mfma_f32_16x16x32_bf16 v[76:79], v[56:59], a[92:95], v[76:79]\n"
    "s_nop 0\n"
    "ds_read_b64_tr_b16 v[50:51], v5 offset:29440\n"
    "v_and_b32_e64 v197, 31, v30\n"
    "v_mfma_f32_16x16x32_bf16 v[128:131], v[32:35], a[88:91], v[128:131]\n"
    "ds_read_b64_tr_b16 v[52:53], v4 offset:32768\n"
    "v_lshl_add_u32 v216, v197, 6, v216\n"
    "v_mfma_f32_16x16x32_bf16 v[132:135], v[32:35], a[92:95], v[132:135]\n"
    "s_waitcnt lgkmcnt(5)\n"
    "ds_read_b64_tr_b16 v[54:55], v4 offset:36864\n"
    "s_and_b32 s94, s48, 1\n"
    "v_mfma_f32_16x16x32_bf16 v[80:83], v[36:39], a[88:91], v[80:83]\n"
    "s_add_u32 s85, s67, 24576\n"
    "ds_read_b64_tr_b16 v[56:57], v5 offset:32768\n"
    "ds_read_b64_tr_b16 v[58:59], v5 offset:36864\n"
    "v_mfma_f32_16x16x32_bf16 v[84:87], v[36:39], a[92:95], v[84:87]\n"
    "s_lshl_b32 s94, s94, 1\n"
    "buffer_load_dwordx4 a[120:123], v20, s[16:19], s85 offen offset:0 sc1\n"
    "v_mfma_f32_16x16x32_bf16 v[136:139], v[40:43], a[88:91], v[136:139]\n"
    "s_nop 0\n"
    "ds_read_b64_tr_b16 v[32:33], v4 offset:33024\n"
    "v_xor_b32_e64 v197, s94, v193\n"
    "v_mfma_f32_16x16x32_bf16 v[140:143], v[40:43], a[92:95], v[140:143]\n"
    "s_waitcnt lgkmcnt(5)\n"
    "ds_read_b64_tr_b16 v[34:35], v4 offset:37120\n"
    "v_lshl_add_u32 v216, v197, 4, v216\n"
    "v_mfma_f32_16x16x32_bf16 v[88:91], v[44:47], a[88:91], v[88:91]\n"
    "LPAD2_%=:\n"
    "s_cbranch_execz LPAD6_%=\n"
    "ds_read_b64_tr_b16 v[36:37], v5 offset:33024\n"
    "v_xor_b32_e64 v217, 16, v216\n"
    "v_mfma_f32_16x16x32_bf16 v[92:95], v[44:47], a[92:95], v[92:95]\n"
    "ds_read_b64_tr_b16 v[38:39], v5 offset:37120\n"
    "ds_read_b64_tr_b16 v[40:41], v4 offset:33280\n"
    "v_mfma_f32_16x16x32_bf16 v[144:147], v[48:51], a[88:91], v[144:147]\n"
    "buffer_load_dwordx4 a[124:127], v20, s[16:19], s85 offen offset:1024 sc1\n"
    "v_mfma_f32_16x16x32_bf16 v[148:151], v[48:51], a[92:95], v[148:151]\n"
    "s_waitcnt vmcnt(11) lgkmcnt(5)\n"
    "ds_read_b64_tr_b16 v[42:43], v4 offset:37376\n"
    "v_lshlrev_b32 v218, 4, v30\n"
    "v_mfma_f32_16x16x32_bf16 v[64:67], v[52:55], a[96:99], v[64:67]\n"
    "ds_read_b64_tr_b16 v[44:45], v5 offset:33280\n"
    "v_add_u32 v218, 0x20000, v218\n"
    "s_cmp_eq_u32 s28, 0\n"
    "s_cbranch_scc0 LTB0r_%=\n"
    "buffer_load_dword v22, v21, s[12:15], 0 offen\n"
    "LTB0r_%=:\n"
    "v_mfma_f32_16x16x32_bf16 v[68:71], v[52:55], a[100:103], v[68:71]\n"
    "s_add_u32 s84, s4, 0\n"
    "ds_read_b64_tr_b16 v[46:47], v5 offset:37376\n"
    "s_add_u32 s94, s36, 0x24000\n"
    "v_mfma_f32_16x16x32_bf16 v[120:123], v[56:59], a[96:99], v[120:123]\n"
    "s_add_u32 m0, s36, 0\n"
    "ds_read_b64_tr_b16 v[48:49], v4 offset:33536\n"
    "ds_read_b64_tr_b16 v[50:51], v4 offset:37632\n"
    "v_mfma_f32_16x16x32_bf16 v[124:127], v[56:59], a[100:103], v[124:127]\n"
    "s_waitcnt lgkmcnt(6)\n"
    "buffer_load_dwordx4 v14, s[8:11], s84 offen lds\n"
    "v_mfma_f32_16x16x32_bf16 v[72:75], v[32:35], a[96:99], v[72:75]\n"
    "s_nop 0\n"
    "ds_read_b64_tr_b16 v[52:53], v5 offset:33536\n"
    "v_lshl_add_u32 v219, v30, 2, s94\n"
    "v_mfma_f32_16x16x32_bf16 v[76:79], v[32:35], a[100:103], v[76:79]\n"
    "s_add_u32 s84, s4, 28672\n"
    "ds_read_b64_tr_b16 v[54:55], v5 offset:37632\n"
    "v_xor_b32_e64 v220, 32, v219\n"
    "v_mfma_f32_16x16x32_bf16 v[128:131], v[36:39], a[96:99], v[128:131]\n"
    "s_add_u32 m0, s36, 4096\n"
    "ds_read_b64_tr_b16 v[56:57], v4 offset:40960\n"
    "ds_read_b64_tr_b16 v[58:59], v4 offset:45056\n"
    "v_mfma_f32_16x16x32_bf16 v[132:135], v[36:39], a[100:103], v[132:135]\n"
    "s_waitcnt lgkmcnt(6)\n"
    "buffer_load_dwordx4 v14, s[8:11], s84 offen lds\n"
    "v_mfma_f32_16x16x32_bf16 v[80:83], v[40:43], a[96:99], v[80:83]\n"
    "s_add_u32 m0, s36, 8192\n"
    "ds_read_b64_tr_b16 v[32:33], v5 offset:40960\n"
    "ds_read_b64_tr_b16 v[34:35], v5 offset:45056\n"
    "v_mfma_f32_16x16x32_bf16 v[84:87], v[40:43], a[100:103], v[84:87]\n"
    "s_add_u32 s84, s4, 229376\n"
    "buffer_load_dwordx4 v14, s[8:11], s84 offen lds\n"
    "v_mfma_f32_16x16x32_bf16 v[136:139], v[44:47], a[96:99], v[136:139]\n"
    "s_add_u32 m0, s36, 12288\n"
    "ds_read_b64_tr_b16 v[36:37], v4 offset:41216\n"
    "ds_read_b64_tr_b16 v[38:39], v4 offset:45312\n"
    "v_mfma_f32_16x16x32_bf16 v[140:143], v[44:47], a[100:103], v[140:143]\n"
    "s_waitcnt lgkmcnt(6)\n"
    "s_add_u32 s84, s4, 258048\n"
    "buffer_load_dwordx4 v14, s[8:11], s84 offen lds\n"
    "v_mfma_f32_16x16x32_bf16 v[88:91], v[48:51], a[96:99], v[88:91]\n"
    "ds_read_b64_tr_b16 v[40:41], v5 offset:41216\n"
    "v_xor_b32_e64 v221, 64, v219\n"
    "v_mfma_f32_16x16x32_bf16 v[92:95], v[48:51], a[100:103], v[92:95]\n"
    "ds_read_b64_tr_b16 v[42:43], v5 offset:45312\n"
    "v_xor_b32 v222, 0x60, v219\n"
    "v_mfma_f32_16x16x32_bf16 v[144:147], v[52:55], a[96:99], v[144:147]\n"
    "s_add_u32 s85, s80, 0\n"
    "ds_read_b64_tr_b16 v[44:45], v4 offset:41472\n"
    "v_lshlrev_b32 v223, 12, v194\n"
    "v_mfma_f32_16x16x32_bf16 v[148:151], v[52:55], a[100:103], v[148:151]\n"
    "s_waitcnt vmcnt(12) lgkmcnt(5)\n"
    "ds_read_b64_tr_b16 v[46:47], v4 offset:45568\n"
    "ds_read_b64_tr_b16 v[48:49], v5 offset:41472\n"
    "v_mfma_f32_16x16x32_bf16 v[64:67], v[56:59], a[104:107], v[64:67]\n"
    "s_nop 0\n"
    "buffer_load_dwordx4 a[128:131], v20, s[16:19], s85 offen offset:0 sc1\n"
    "v_mfma_f32_16x16x32_bf16 v[68:71], v[56:59], a[108:111], v[68:71]\n"
    "ds_read_b64_tr_b16 v[50:51], v5 offset:45568\n"
    "v_lshlrev_b32_e64 v197, 3, v194\n"
    "v_mfma_f32_16x16x32_bf16 v[120:123], v[32:35], a[104:107], v[120:123]\n"
    "ds_read_b64_tr_b16 v[52:53], v4 offset:41728\n"
    "v_lshrrev_b32_e64 v196, 2, v30\n"
    "v_mfma_f32_16x16x32_bf16 v[124:127], v[32:35], a[108:111], v[124:127]\n"
    "s_waitcnt vmcnt(9) lgkmcnt(5)\n"
    "ds_read_b64_tr_b16 v[54:55], v4 offset:45824\n"
    "ds_read_b64_tr_b16 v[56:57], v5 offset:41728\n"
    "v_mfma_f32_16x16x32_bf16 v[72:75], v[36:39], a[104:107], v[72:75]\n"
    "s_barrier\n"
    "buffer_load_dwordx4 a[132:135], v20, s[16:19], s85 offen offset:1024 sc1\n"
    "v_mfma_f32_16x16x32_bf16 v[76:79], v[36:39], a[108:111], v[76:79]\n"
    "s_lshl_b32 s94, s48, 4\n"
    "ds_read_b64_tr_b16 v[58:59], v5 offset:45824\n"
    "v_add_u32 v196, s94, v196\n"
    "v_mfma_f32_16x16x32_bf16 v[128:131], v[40:43], a[104:107], v[128:131]\n"
    "ds_read_b64_tr_b16 v[32:33], v4 offset:49152\n"
    "v_xor_b32_e64 v197, v196, v197\n"
    "v_mfma_f32_16x16x32_bf16 v[132:135], v[40:43], a[108:111], v[132:135]\n"
    "s_waitcnt lgkmcnt(5)\n"
    "ds_read_b64_tr_b16 v[34:35], v4 offset:53248\n"
    "v_lshl_add_u32 v223, v197, 2, v223\n"
    "v_mfma_f32_16x16x32_bf16 v[80:83], v[44:47], a[104:107], v[80:83]\n"
    "s_add_u32 s85, s80, 8192\n"
    "ds_read_b64_tr_b16 v[36:37], v5 offset:49152\n"
    "ds_read_b64_tr_b16 v[38:39], v5 offset:53248\n"
    "v_mfma_f32_16x16x32_bf16 v[84:87], v[44:47], a[108:111], v[84:87]\n"
    "s_nop 0\n"
    "buffer_load_dwordx4 a[136:139], v20, s[16:19], s85 offen offset:0 sc1\n"
    "v_mfma_f32_16x16x32_bf16 v[136:139], v[48:51], a[104:107], v[136:139]\n"
    "ds_read_b64_tr_b16 v[40:41], v4 offset:49408\n"
    "v_add_u32 v223, 0x24000, v223\n"
    "v_mfma_f32_16x16x32_bf16 v[140:143], v[48:51], a[108:111], v[140:143]\n"
    "s_waitcnt lgkmcnt(5)\n"
    "ds_read_b64_tr_b16 v[42:43], v4 offset:53504\n"
    "v_mfma_f32_16x16x32_bf16 v[88:91], v[52:55], a[104:107], v[88:91]\n"
    "s_mov_b32 s26, 0x400000\n"
    "ds_read_b64_tr_b16 v[44:45], v5 offset:49408\n"
    "v_mfma_f32_16x16x32_bf16 v[92:95], v[52:55], a[108:111], v[92:95]\n"
    "s_mov_b32 s27, 0x20000\n"
    "ds_read_b64_tr_b16 v[46:47], v5 offset:53504\n"
    "ds_read_b64_tr_b16 v[48:49], v4 offset:49664\n"
    "v_mfma_f32_16x16x32_bf16 v[144:147], v[56:59], a[104:107], v[144:147]\n"
    "s_mov_b32 s34, 0x800\n"
    "buffer_load_dwordx4 a[140:143], v20, s[16:19], s85 offen offset:1024 sc1\n"
    "v_mfma_f32_16x16x32_bf16 v[148:151], v[56:59], a[108:111], v[148:151]\n"
    "s_waitcnt vmcnt(10) lgkmcnt(5)\n"
    "ds_read_b64_tr_b16 v[50:51], v4 offset:53760\n"
    "v_mfma_f32_16x16x32_bf16 v[64:67], v[32:35], a[112:115], v[64:67]\n"
    "s_mov_b32 s35, 0x20000\n"
    "ds_read_b64_tr_b16 v[52:53], v5 offset:49664\n"
    "s_mov_b32 s49, 0x8000\n"
    "s_cmp_eq_u32 s28, 1\n"
    "s_cbranch_scc0 LTB1r_%=\n"
    "buffer_load_dword v22, v21, s[12:15], 0 offen\n"
    "LTB1r_%=:\n"
    "v_mfma_f32_16x16x32_bf16 v[68:71], v[32:35], a[116:119], v[68:71]\n"
    "s_add_u32 s84, s4, 458752\n"
    "ds_read_b64_tr_b16 v[54:55], v5 offset:53760\n"
    "s_xor_b32 s38, s100, 4\n"
    "v_mfma_f32_16x16x32_bf16 v[120:123], v[36:39], a[112:115], v[120:123]\n"
    "s_add_u32 m0, s36, 16384\n"
    "ds_read_b64_tr_b16 v[56:57], v4 offset:49920\n"
    "ds_read_b64_tr_b16 v[58:59], v4 offset:54016\n"
    "v_mfma_f32_16x16x32_bf16 v[124:127], v[36:39], a[116:119], v[124:127]\n"
    "s_waitcnt lgkmcnt(6)\n"
    "buffer_load_dwordx4 v14, s[8:11], s84 offen lds\n"
    "v_mfma_f32_16x16x32_bf16 v[72:75], v[40:43], a[112:115], v[72:75]\n"
    "s_mul_i32 s38, s38, 0xc000\n"
    "ds_read_b64_tr_b16 v[32:33], v5 offset:49920\n"
    "s_add_u32 s38, s38, 0x2500000\n"
    "v_mfma_f32_16x16x32_bf16 v[76:79], v[40:43], a[116:119], v[76:79]\n"
    "s_add_u32 s84, s4, 487424\n"
    "ds_read_b64_tr_b16 v[34:35], v5 offset:54016\n"
    "s_add_u32 s38, s38, 0x4000\n"
    "v_mfma_f32_16x16x32_bf16 v[128:131], v[44:47], a[112:115], v[128:131]\n"
    "s_add_u32 m0, s36, 20480\n"
    "ds_read_b64_tr_b16 v[36:37], v4 offset:57344\n"
    "ds_read_b64_tr_b16 v[38:39], v4 offset:61440\n"
    "v_mfma_f32_16x16x32_bf16 v[132:135], v[44:47], a[116:119], v[132:135]\n"
    "s_waitcnt lgkmcnt(6)\n"
    "buffer_load_dwordx4 v14, s[8:11], s84 offen lds\n"
    "v_mfma_f32_16x16x32_bf16 v[80:83], v[48:51], a[112:115], v[80:83]\n"
    "s_add_u32 m0, s36, 24576\n"
    "ds_read_b64_tr_b16 v[40:41], v5 offset:57344\n"
    "ds_read_b64_tr_b16 v[42:43], v5 offset:61440\n"
    "v_mfma_f32_16x16x32_bf16 v[84:87], v[48:51], a[116:119], v[84:87]\n"
    "s_add_u32 s84, s4, 688128\n"
    "buffer_load_dwordx4 v14, s[8:11], s84 offen lds\n"
    "v_mfma_f32_16x16x32_bf16 v[136:139], v[52:55], a[112:115], v[136:139]\n"
    "s_add_u32 m0, s36, 28672\n"
    "ds_read_b64_tr_b16 v[44:45], v4 offset:57600\n"
    "ds_read_b64_tr_b16 v[46:47], v4 offset:61696\n"
    "v_mfma_f32_16x16x32_bf16 v[140:143], v[52:55], a[116:119], v[140:143]\n"
    "s_waitcnt lgkmcnt(6)\n"
    "s_add_u32 s84, s4, 716800\n"
    "buffer_load_dwordx4 v14, s[8:11], s84 offen lds\n"
    "v_mfma_f32_16x16x32_bf16 v[88:91], v[56:59], a[112:115], v[88:91]\n"
    "s_xor_b32 s39, s100, 6\n"
    "ds_read_b64_tr_b16 v[48:49], v5 offset:57600\n"
    "v_mfma_f32_16x16x32_bf16 v[92:95], v[56:59], a[116:119], v[92:95]\n"
    "s_mul_i32 s39, s39, 0xc000\n"
    "ds_read_b64_tr_b16 v[50:51], v5 offset:61696\n"
    "s_add_u32 s39, s39, 0x2500000\n"
    "v_mfma_f32_16x16x32_bf16 v[144:147], v[32:35], a[112:115], v[144:147]\n"
    "s_add_u32 s85, s80, 16384\n"
    "ds_read_b64_tr_b16 v[52:53], v4 offset:57856\n"
    "s_add_u32 s39, s39, 0x8000\n"
    "v_mfma_f32_16x16x32_bf16 v[148:151], v[32:35], a[116:119], v[148:151]\n"
    "s_waitcnt vmcnt(12) lgkmcnt(5)\n"
    "ds_read_b64_tr_b16 v[54:55], v4 offset:61952\n"
    "ds_read_b64_tr_b16 v[56:57], v5 offset:57856\n"
    "v_mfma_f32_16x16x32_bf16 v[64:67], v[36:39], a[120:123], v[64:67]\n"
    "s_xor_b32 s40, s100, 2\n"
    "buffer_load_dwordx4 a[144:147], v20, s[16:19], s85 offen offset:0 sc1\n"
    "v_mfma_f32_16x16x32_bf16 v[68:71], v[36:39], a[124:127], v[68:71]\n"
    "s_mul_i32 s40, s40, 0xc000\n"
    "ds_read_b64_tr_b16 v[58:59], v5 offset:61952\n"
    "v_mfma_f32_16x16x32_bf16 v[120:123], v[40:43], a[120:123], v[120:123]\n"
    "s_add_u32 s40, s40, 0x2500000\n"
    "ds_read_b64_tr_b16 v[32:33], v4 offset:58112\n"
    "s_mul_i32 s41, s100, 0xc000\n"
    "v_mfma_f32_16x16x32_bf16 v[124:127], v[40:43], a[124:127], v[124:127]\n"
    "s_waitcnt vmcnt(9) lgkmcnt(5)\n"
    "ds_read_b64_tr_b16 v[34:35], v4 offset:62208\n"
    "ds_read_b64_tr_b16 v[36:37], v5 offset:58112\n"
    "v_mfma_f32_16x16x32_bf16 v[72:75], v[44:47], a[120:123], v[72:75]\n"
    "s_barrier\n"
    "buffer_load_dwordx4 a[148:151], v20, s[16:19], s85 offen offset:1024 sc1\n"
    "v_mfma_f32_16x16x32_bf16 v[76:79], v[44:47], a[124:127], v[76:79]\n"
    "s_add_u32 s41, s41, 0x2500000\n"
    "ds_read_b64_tr_b16 v[38:39], v5 offset:62208\n"
    "v_mfma_f32_16x16x32_bf16 v[128:131], v[48:51], a[120:123], v[128:131]\n"
    "s_lshl_b32 s42, s100, 14\n"
    "ds_read_b64_tr_b16 v[40:41], v2 offset:0\n"
    "s_add_u32 s42, s42, 0x1300000\n"
    "v_mfma_f32_16x16x32_bf16 v[132:135], v[48:51], a[124:127], v[132:135]\n"
    "s_waitcnt lgkmcnt(5)\n"
    "ds_read_b64_tr_b16 v[42:43], v2 offset:4096\n"
    "s_and_b32 s95, s100, 57\n"
    "v_mfma_f32_16x16x32_bf16 v[80:83], v[52:55], a[120:123], v[80:83]\n"
    "s_add_u32 s85, s80, 24576\n"
    "ds_read_b64_tr_b16 v[44:45], v3 offset:0\n"
    "ds_read_b64_tr_b16 v[46:47], v3 offset:4096\n"
    "v_mfma_f32_16x16x32_bf16 v[84:87], v[52:55], a[124:127], v[84:87]\n"
    "s_lshl_b32 s95, s95, 14\n"
    "buffer_load_dwordx4 a[152:155], v20, s[16:19], s85 offen offset:0 sc1\n"
    "v_mfma_f32_16x16x32_bf16 v[136:139], v[56:59], a[120:123], v[136:139]\n"
    "s_lshl_b32 s69, s76, 2\n"
    "ds_read_b64_tr_b16 v[48:49], v2 offset:256\n"
    "s_and_b32 s68, s78, 2\n"
    "v_mfma_f32_16x16x32_bf16 v[140:143], v[56:59], a[124:127], v[140:143]\n"
    "s_waitcnt lgkmcnt(5)\n"
    "ds_read_b64_tr_b16 v[50:51], v2 offset:4352\n"
    "v_mfma_f32_16x16x32_bf16 v[88:91], v[32:35], a[120:123], v[88:91]\n"
    "s_add_u32 s69, s69, s68\n"
    "ds_read_b64_tr_b16 v[52:53], v3 offset:256\n"
    "v_mfma_f32_16x16x32_bf16 v[92:95], v[32:35], a[124:127], v[92:95]\n"
    "s_lshl_b32 s69, s69, 9\n"
    "ds_read_b64_tr_b16 v[54:55], v3 offset:4352\n"
    "ds_read_b64_tr_b16 v[56:57], v2 offset:512\n"
    "v_mfma_f32_16x16x32_bf16 v[144:147], v[36:39], a[120:123], v[144:147]\n"
    "s_add_u32 s95, s95, s69\n"
    "buffer_load_dwordx4 a[156:159], v20, s[16:19], s85 offen offset:1024 sc1\n"
    "v_mfma_f32_16x16x32_bf16 v[148:151], v[36:39], a[124:127], v[148:151]\n"
    "s_waitcnt vmcnt(10) lgkmcnt(5)\n"
    "ds_read_b64_tr_b16 v[58:59], v2 offset:4608\n"
    "v_mfma_f32_16x16x32_bf16 v[64:67], v[40:43], a[128:131], v[64:67]\n"
    "s_and_b32 s69, s78, 1\n"
    "ds_read_b64_tr_b16 v[32:33], v3 offset:512\n"
    "s_lshl_b32 s69, s69, 7\n"
    "s_cmp_eq_u32 s28, 2\n"
    "s_cbranch_scc0 LTB2r_%=\n"
    "buffer_load_dword v22, v21, s[12:15], 0 offen\n"
    "LTB2r_%=:\n"
    "v_mfma_f32_16x16x32_bf16 v[68:71], v[40:43], a[132:135], v[68:71]\n"
    "s_add_u32 s84, s5, 0\n"
    "ds_read_b64_tr_b16 v[34:35], v3 offset:4608\n"
    "s_add_u32 s95, s95, s69\n"
    "v_mfma_f32_16x16x32_bf16 v[120:123], v[44:47], a[128:131], v[120:123]\n"
    "s_add_u32 m0, s36, 32768\n"
    "ds_read_b64_tr_b16 v[36:37], v2 offset:768\n"
    "ds_read_b64_tr_b16 v[38:39], v2 offset:4864\n"
    "v_mfma_f32_16x16x32_bf16 v[124:127], v[44:47], a[132:135], v[124:127]\n"
    "s_waitcnt lgkmcnt(6)\n"
    "buffer_load_dwordx4 v14, s[8:11], s84 offen lds\n"
    "v_mfma_f32_16x16x32_bf16 v[72:75], v[48:51], a[128:131], v[72:75]\n"
    "s_lshl_b32 s69, s48, 8\n"
    "ds_read_b64_tr_b16 v[40:41], v3 offset:768\n"
    "s_add_u32 s95, s95, s69\n"
    "v_mfma_f32_16x16x32_bf16 v[76:79], v[48:51], a[132:135], v[76:79]\n"
    "s_add_u32 s84, s5, 28672\n"
    "ds_read_b64_tr_b16 v[42:43], v3 offset:4864\n"
    "s_add_u32 s95, s95, 0x1300000\n"
    "v_mfma_f32_16x16x32_bf16 v[128:131], v[52:55], a[128:131], v[128:131]\n"
    "s_add_u32 m0, s36, 36864\n"
    "ds_read_b64_tr_b16 v[44:45], v2 offset:8192\n"
    "ds_read_b64_tr_b16 v[46:47], v2 offset:12288\n"
    "v_mfma_f32_16x16x32_bf16 v[132:135], v[52:55], a[132:135], v[132:135]\n"
    "s_waitcnt lgkmcnt(6)\n"
    "buffer_load_dwordx4 v14, s[8:11], s84 offen lds\n"
    "v_mfma_f32_16x16x32_bf16 v[80:83], v[56:59], a[128:131], v[80:83]\n"
    "s_add_u32 m0, s36, 40960\n"
    "ds_read_b64_tr_b16 v[48:49], v3 offset:8192\n"
    "ds_read_b64_tr_b16 v[50:51], v3 offset:12288\n"
    "v_mfma_f32_16x16x32_bf16 v[84:87], v[56:59], a[132:135], v[84:87]\n"
    "s_add_u32 s84, s5, 229376\n"
    "buffer_load_dwordx4 v14, s[8:11], s84 offen lds\n"
    "v_mfma_f32_16x16x32_bf16 v[136:139], v[32:35], a[128:131], v[136:139]\n"
    "s_add_u32 m0, s36, 45056\n"
    "ds_read_b64_tr_b16 v[52:53], v2 offset:8448\n"
    "ds_read_b64_tr_b16 v[54:55], v2 offset:12544\n"
    "v_mfma_f32_16x16x32_bf16 v[140:143], v[32:35], a[132:135], v[140:143]\n"
    "s_waitcnt lgkmcnt(6)\n"
    "s_add_u32 s84, s5, 258048\n"
    "buffer_load_dwordx4 v14, s[8:11], s84 offen lds\n"
    "v_mfma_f32_16x16x32_bf16 v[88:91], v[36:39], a[128:131], v[88:91]\n"
    "s_lshl_b32 s52, s43, 10\n"
    "ds_read_b64_tr_b16 v[56:57], v3 offset:8448\n"
    "v_mfma_f32_16x16x32_bf16 v[92:95], v[36:39], a[132:135], v[92:95]\n"
    "s_lshl_b32 s53, s44, 10\n"
    "ds_read_b64_tr_b16 v[58:59], v3 offset:12544\n"
    "s_lshl_b32 s54, s45, 10\n"
    "v_mfma_f32_16x16x32_bf16 v[144:147], v[40:43], a[128:131], v[144:147]\n"
    "s_add_u32 s85, s81, 0\n"
    "ds_read_b64_tr_b16 v[32:33], v2 offset:8704\n"
    "s_and_b32 s69, s78, 1\n"
    "v_mfma_f32_16x16x32_bf16 v[148:151], v[40:43], a[132:135], v[148:151]\n"
    "s_waitcnt vmcnt(12) lgkmcnt(5)\n"
    "ds_read_b64_tr_b16 v[34:35], v2 offset:12800\n"
    "ds_read_b64_tr_b16 v[36:37], v3 offset:8704\n"
    "v_mfma_f32_16x16x32_bf16 v[64:67], v[44:47], a[136:139], v[64:67]\n"
    "s_mul_i32 s69, s69, 20\n"
    "buffer_load_dwordx4 a[160:163], v20, s[16:19], s85 offen offset:0 sc1\n"
    "v_mfma_f32_16x16x32_bf16 v[68:71], v[44:47], a[140:143], v[68:71]\n"
    "s_sub_u32 s88, 10, s69\n"
    "ds_read_b64_tr_b16 v[38:39], v3 offset:12800\n"
    "v_mfma_f32_16x16x32_bf16 v[120:123], v[48:51], a[136:139], v[120:123]\n"
    "s_lshl_b32 s88, s88, 16\n"
    "ds_read_b64_tr_b16 v[40:41], v2 offset:8960\n"
    "s_and_b32 s69, s78, 2\n"
    "v_mfma_f32_16x16x32_bf16 v[124:127], v[48:51], a[140:143], v[124:127]\n"
    "s_waitcnt vmcnt(9) lgkmcnt(5)\n"
    "ds_read_b64_tr_b16 v[42:43], v2 offset:13056\n"
    "ds_read_b64_tr_b16 v[44:45], v3 offset:8960\n"
    "v_mfma_f32_16x16x32_bf16 v[72:75], v[52:55], a[136:139], v[72:75]\n"
    "s_barrier\n"
    "buffer_load_dwordx4 a[164:167], v20, s[16:19], s85 offen offset:1024 sc1\n"
    "v_mfma_f32_16x16x32_bf16 v[76:79], v[52:55], a[140:143], v[76:79]\n"
    "s_mul_i32 s69, s69, 14\n"
    "ds_read_b64_tr_b16 v[46:47], v3 offset:13056\n"
    "v_mfma_f32_16x16x32_bf16 v[128:131], v[56:59], a[136:139], v[128:131]\n"
    "s_sub_u32 s89, 14, s69\n"
    "ds_read_b64_tr_b16 v[48:49], v2 offset:16384\n"
    "s_lshl_b32 s89, s89, 16\n"
    "v_mfma_f32_16x16x32_bf16 v[132:135], v[56:59], a[140:143], v[132:135]\n"
    "s_waitcnt lgkmcnt(5)\n"
    "ds_read_b64_tr_b16 v[50:51], v2 offset:20480\n"
    "s_mul_i32 s94, s77, 0xe0000\n"
    "v_mfma_f32_16x16x32_bf16 v[80:83], v[32:35], a[136:139], v[80:83]\n"
    "s_add_u32 s85, s81, 8192\n"
    "ds_read_b64_tr_b16 v[52:53], v3 offset:16384\n"
    "ds_read_b64_tr_b16 v[54:55], v3 offset:20480\n"
    "v_mfma_f32_16x16x32_bf16 v[84:87], v[32:35], a[140:143], v[84:87]\n"
    "s_add_u32 s94, s94, 0x1700000\n"
    "buffer_load_dwordx4 a[168:171], v20, s[16:19], s85 offen offset:0 sc1\n"
    "v_mfma_f32_16x16x32_bf16 v[136:139], v[36:39], a[136:139], v[136:139]\n"
    "s_add_u32 s55, s52, s88\n"
    "ds_read_b64_tr_b16 v[56:57], v2 offset:16640\n"
    "s_and_b32 s69, s78, 1\n"
    "v_mfma_f32_16x16x32_bf16 v[140:143], v[36:39], a[140:143], v[140:143]\n"
    "s_waitcnt lgkmcnt(5)\n"
    "ds_read_b64_tr_b16 v[58:59], v2 offset:20736\n"
    "v_mfma_f32_16x16x32_bf16 v[88:91], v[40:43], a[136:139], v[88:91]\n"
    "s_lshl_b32 s69, s69, 3\n"
    "ds_read_b64_tr_b16 v[32:33], v3 offset:16640\n"
    "v_mfma_f32_16x16x32_bf16 v[92:95], v[40:43], a[140:143], v[92:95]\n"
    "s_sub_u32 s88, 4, s69\n"
    "ds_read_b64_tr_b16 v[34:35], v3 offset:20736\n"
    "ds_read_b64_tr_b16 v[36:37], v2 offset:16896\n"
    "v_mfma_f32_16x16x32_bf16 v[144:147], v[44:47], a[136:139], v[144:147]\n"
    "s_lshl_b32 s88, s88, 16\n"
    "buffer_load_dwordx4 a[172:175], v20, s[16:19], s85 offen offset:1024 sc1\n"
    "v_mfma_f32_16x16x32_bf16 v[148:151], v[44:47], a[140:143], v[148:151]\n"
    "s_waitcnt vmcnt(10) lgkmcnt(5)\n"
    "ds_read_b64_tr_b16 v[38:39], v2 offset:20992\n"
    "v_mfma_f32_16x16x32_bf16 v[64:67], v[48:51], a[144:147], v[64:67]\n"
    "s_add_u32 s56, s53, s88\n"
    "ds_read_b64_tr_b16 v[40:41], v3 offset:16896\n"
    "s_and_b32 s69, s78, 1\n"
    "s_cmp_eq_u32 s28, 3\n"
    "s_cbranch_scc0 LTB3r_%=\n"
    "buffer_load_dword v22, v21, s[12:15], 0 offen\n"
    "LTB3r_%=:\n"
    "v_mfma_f32_16x16x32_bf16 v[68:71], v[48:51], a[148:151], v[68:71]\n"
    "s_add_u32 s84, s5, 458752\n"
    "ds_read_b64_tr_b16 v[42:43], v3 offset:20992\n"
    "s_lshl_b32 s69, s69, 1\n"
    "v_mfma_f32_16x16x32_bf16 v[120:123], v[52:55], a[144:147], v[120:123]\n"
    "s_add_u32 m0, s36, 49152\n"
    "ds_read_b64_tr_b16 v[44:45], v2 offset:17152\n"
    "ds_read_b64_tr_b16 v[46:47], v2 offset:21248\n"
    "v_mfma_f32_16x16x32_bf16 v[124:127], v[52:55], a[148:151], v[124:127]\n"
    "s_waitcnt lgkmcnt(6)\n"
    "buffer_load_dwordx4 v14, s[8:11], s84 offen lds\n"
    "v_mfma_f32_16x16x32_bf16 v[72:75], v[56:59], a[144:147], v[72:75]\n"
    "s_sub_u32 s88, 1, s69\n"
    "ds_read_b64_tr_b16 v[48:49], v3 offset:17152\n"
    "s_lshl_b32 s88, s88, 16\n"
    "v_mfma_f32_16x16x32_bf16 v[76:79], v[56:59], a[148:151], v[76:79]\n"
    "s_add_u32 s84, s5, 487424\n"
    "ds_read_b64_tr_b16 v[50:51], v3 offset:21248\n"
    "s_add_u32 s57, s54, s88\n"
    "v_mfma_f32_16x16x32_bf16 v[128:131], v[32:35], a[144:147], v[128:131]\n"
    "s_add_u32 m0, s36, 53248\n"
    "ds_read_b64_tr_b16 v[52:53], v2 offset:24576\n"
    "ds_read_b64_tr_b16 v[54:55], v2 offset:28672\n"
    "v_mfma_f32_16x16x32_bf16 v[132:135], v[32:35], a[148:151], v[132:135]\n"
    "s_waitcnt lgkmcnt(6)\n"
    "buffer_load_dwordx4 v14, s[8:11], s84 offen lds\n"
    "v_mfma_f32_16x16x32_bf16 v[80:83], v[36:39], a[144:147], v[80:83]\n"
    "s_add_u32 m0, s36, 57344\n"
    "ds_read_b64_tr_b16 v[56:57], v3 offset:24576\n"
    "ds_read_b64_tr_b16 v[58:59], v3 offset:28672\n"
    "v_mfma_f32_16x16x32_bf16 v[84:87], v[36:39], a[148:151], v[84:87]\n"
    "s_add_u32 s84, s5, 688128\n"
    "buffer_load_dwordx4 v14, s[8:11], s84 offen lds\n"
    "v_mfma_f32_16x16x32_bf16 v[136:139], v[40:43], a[144:147], v[136:139]\n"
    "s_add_u32 m0, s36, 61440\n"
    "ds_read_b64_tr_b16 v[32:33], v2 offset:24832\n"
    "ds_read_b64_tr_b16 v[34:35], v2 offset:28928\n"
    "v_mfma_f32_16x16x32_bf16 v[140:143], v[40:43], a[148:151], v[140:143]\n"
    "s_waitcnt lgkmcnt(6)\n"
    "s_add_u32 s84, s5, 716800\n"
    "buffer_load_dwordx4 v14, s[8:11], s84 offen lds\n"
    "v_mfma_f32_16x16x32_bf16 v[88:91], v[44:47], a[144:147], v[88:91]\n"
    "s_add_u32 s58, s52, s89\n"
    "ds_read_b64_tr_b16 v[36:37], v3 offset:24832\n"
    "v_mfma_f32_16x16x32_bf16 v[92:95], v[44:47], a[148:151], v[92:95]\n"
    "s_add_u32 s59, s53, s89\n"
    "ds_read_b64_tr_b16 v[38:39], v3 offset:28928\n"
    "s_add_u32 s60, s54, s89\n"
    "v_mfma_f32_16x16x32_bf16 v[144:147], v[48:51], a[144:147], v[144:147]\n"
    "s_add_u32 s85, s81, 16384\n"
    "ds_read_b64_tr_b16 v[40:41], v2 offset:25088\n"
    "s_add_u32 s61, s55, s89\n"
    "v_mfma_f32_16x16x32_bf16 v[148:151], v[48:51], a[148:151], v[148:151]\n"
    "s_waitcnt vmcnt(12) lgkmcnt(5)\n"
    "ds_read_b64_tr_b16 v[42:43], v2 offset:29184\n"
    "ds_read_b64_tr_b16 v[44:45], v3 offset:25088\n"
    "v_mfma_f32_16x16x32_bf16 v[64:67], v[52:55], a[152:155], v[64:67]\n"
    "s_add_u32 s62, s56, s89\n"
    "buffer_load_dwordx4 a[176:179], v20, s[16:19], s85 offen offset:0 sc1\n"
    "v_mfma_f32_16x16x32_bf16 v[68:71], v[52:55], a[156:159], v[68:71]\n"
    "s_add_u32 s63, s57, s89\n"
    "ds_read_b64_tr_b16 v[46:47], v3 offset:29184\n"
    "v_mfma_f32_16x16x32_bf16 v[120:123], v[56:59], a[152:155], v[120:123]\n"
    "s_lshr_b32 s68, s52, 3\n"
    "ds_read_b64_tr_b16 v[48:49], v2 offset:25344\n"
    "s_add_u32 s68, s68, s94\n"
    "v_mfma_f32_16x16x32_bf16 v[124:127], v[56:59], a[156:159], v[124:127]\n"
    "s_waitcnt vmcnt(9) lgkmcnt(5)\n"
    "ds_read_b64_tr_b16 v[50:51], v2 offset:29440\n"
    "ds_read_b64_tr_b16 v[52:53], v3 offset:25344\n"
    "v_mfma_f32_16x16x32_bf16 v[72:75], v[32:35], a[152:155], v[72:75]\n"
    "s_barrier\n"
    "buffer_load_dwordx4 a[180:183], v20, s[16:19], s85 offen offset:1024 sc1\n"
    "v_mfma_f32_16x16x32_bf16 v[76:79], v[32:35], a[156:159], v[76:79]\n"
    "s_lshr_b32 s69, s53, 3\n"
    "ds_read_b64_tr_b16 v[54:55], v3 offset:29440\n"
    "v_mfma_f32_16x16x32_bf16 v[128:131], v[36:39], a[152:155], v[128:131]\n"
    "s_add_u32 s69, s69, s94\n"
    "ds_read_b64_tr_b16 v[56:57], v2 offset:32768\n"
    "s_lshr_b32 s70, s54, 3\n"
    "v_mfma_f32_16x16x32_bf16 v[132:135], v[36:39], a[156:159], v[132:135]\n"
    "s_waitcnt lgkmcnt(5)\n"
    "ds_read_b64_tr_b16 v[58:59], v2 offset:36864\n"
    "s_add_u32 s70, s70, s94\n"
    "v_mfma_f32_16x16x32_bf16 v[80:83], v[40:43], a[152:155], v[80:83]\n"
    "s_add_u32 s85, s81, 24576\n"
    "ds_read_b64_tr_b16 v[32:33], v3 offset:32768\n"
    "ds_read_b64_tr_b16 v[34:35], v3 offset:36864\n"
    "v_mfma_f32_16x16x32_bf16 v[84:87], v[40:43], a[156:159], v[84:87]\n"
    "s_lshr_b32 s71, s55, 3\n"
    "buffer_load_dwordx4 a[184:187], v20, s[16:19], s85 offen offset:0 sc1\n"
    "v_mfma_f32_16x16x32_bf16 v[136:139], v[44:47], a[152:155], v[136:139]\n"
    "s_add_u32 s71, s71, s94\n"
    "ds_read_b64_tr_b16 v[36:37], v2 offset:33024\n"
    "s_lshr_b32 s72, s56, 3\n"
    "v_mfma_f32_16x16x32_bf16 v[140:143], v[44:47], a[156:159], v[140:143]\n"
    "s_waitcnt lgkmcnt(5)\n"
    "ds_read_b64_tr_b16 v[38:39], v2 offset:37120\n"
    "v_mfma_f32_16x16x32_bf16 v[88:91], v[48:51], a[152:155], v[88:91]\n"
    "s_add_u32 s72, s72, s94\n"
    "ds_read_b64_tr_b16 v[40:41], v3 offset:33024\n"
    "v_mfma_f32_16x16x32_bf16 v[92:95], v[48:51], a[156:159], v[92:95]\n"
    "s_lshr_b32 s73, s57, 3\n"
    "ds_read_b64_tr_b16 v[42:43], v3 offset:37120\n"
    "ds_read_b64_tr_b16 v[44:45], v2 offset:33280\n"
    "v_mfma_f32_16x16x32_bf16 v[144:147], v[52:55], a[152:155], v[144:147]\n"
    "s_add_u32 s73, s73, s94\n"
    "buffer_load_dwordx4 a[188:191], v20, s[16:19], s85 offen offset:1024 sc1\n"
    "v_mfma_f32_16x16x32_bf16 v[148:151], v[52:55], a[156:159], v[148:151]\n"
    "s_waitcnt vmcnt(10) lgkmcnt(5)\n"
    "ds_read_b64_tr_b16 v[46:47], v2 offset:37376\n"
    "v_mfma_f32_16x16x32_bf16 v[64:67], v[56:59], a[160:163], v[64:67]\n"
    "s_lshr_b32 s74, s58, 3\n"
    "ds_read_b64_tr_b16 v[48:49], v3 offset:33280\n"
    "s_add_u32 s74, s74, s94\n"
    "s_cmp_eq_u32 s28, 4\n"
    "s_cbranch_scc0 LTB4r_%=\n"
    "buffer_load_dword v22, v21, s[12:15], 0 offen\n"
    "LTB4r_%=:\n"
    "v_mfma_f32_16x16x32_bf16 v[68:71], v[56:59], a[164:167], v[68:71]\n"
    "s_add_u32 s84, s6, 0\n"
    "ds_read_b64_tr_b16 v[50:51], v3 offset:37376\n"
    "s_lshr_b32 s75, s59, 3\n"
    "v_mfma_f32_16x16x32_bf16 v[120:123], v[32:35], a[160:163], v[120:123]\n"
    "s_add_u32 m0, s36, 65536\n"
    "ds_read_b64_tr_b16 v[52:53], v2 offset:33536\n"
    "ds_read_b64_tr_b16 v[54:55], v2 offset:37632\n"
    "v_mfma_f32_16x16x32_bf16 v[124:127], v[32:35], a[164:167], v[124:127]\n"
    "s_waitcnt lgkmcnt(6)\n"
    "buffer_load_dwordx4 v14, s[8:11], s84 offen lds\n"
    "v_mfma_f32_16x16x32_bf16 v[72:75], v[36:39], a[160:163], v[72:75]\n"
    "s_add_u32 s75, s75, s94\n"
    "ds_read_b64_tr_b16 v[56:57], v3 offset:33536\n"
    "s_lshr_b32 s76, s60, 3\n"
    "v_mfma_f32_16x16x32_bf16 v[76:79], v[36:39], a[164:167], v[76:79]\n"
    "s_add_u32 s84, s6, 28672\n"
    "ds_read_b64_tr_b16 v[58:59], v3 offset:37632\n"
    "s_add_u32 s76, s76, s94\n"
    "v_mfma_f32_16x16x32_bf16 v[128:131], v[40:43], a[160:163], v[128:131]\n"
    "s_add_u32 m0, s36, 69632\n"
    "ds_read_b64_tr_b16 v[32:33], v2 offset:40960\n"
    "ds_read_b64_tr_b16 v[34:35], v2 offset:45056\n"
    "v_mfma_f32_16x16x32_bf16 v[132:135], v[40:43], a[164:167], v[132:135]\n"
    "s_waitcnt lgkmcnt(6)\n"
    "buffer_load_dwordx4 v14, s[8:11], s84 offen lds\n"
    "v_mfma_f32_16x16x32_bf16 v[80:83], v[44:47], a[160:163], v[80:83]\n"
    "s_add_u32 m0, s36, 73728\n"
    "ds_read_b64_tr_b16 v[36:37], v3 offset:40960\n"
    "ds_read_b64_tr_b16 v[38:39], v3 offset:45056\n"
    "v_mfma_f32_16x16x32_bf16 v[84:87], v[44:47], a[164:167], v[84:87]\n"
    "s_add_u32 s84, s6, 229376\n"
    "buffer_load_dwordx4 v14, s[8:11], s84 offen lds\n"
    "v_mfma_f32_16x16x32_bf16 v[136:139], v[48:51], a[160:163], v[136:139]\n"
    "s_add_u32 m0, s36, 77824\n"
    "ds_read_b64_tr_b16 v[40:41], v2 offset:41216\n"
    "ds_read_b64_tr_b16 v[42:43], v2 offset:45312\n"
    "v_mfma_f32_16x16x32_bf16 v[140:143], v[48:51], a[164:167], v[140:143]\n"
    "s_waitcnt lgkmcnt(6)\n"
    "s_add_u32 s84, s6, 258048\n"
    "buffer_load_dwordx4 v14, s[8:11], s84 offen lds\n"
    "v_mfma_f32_16x16x32_bf16 v[88:91], v[52:55], a[160:163], v[88:91]\n"
    "s_lshr_b32 s77, s61, 3\n"
    "ds_read_b64_tr_b16 v[44:45], v3 offset:41216\n"
    "v_mfma_f32_16x16x32_bf16 v[92:95], v[52:55], a[164:167], v[92:95]\n"
    "s_add_u32 s77, s77, s94\n"
    "ds_read_b64_tr_b16 v[46:47], v3 offset:45312\n"
    "s_lshr_b32 s78, s62, 3\n"
    "v_mfma_f32_16x16x32_bf16 v[144:147], v[56:59], a[160:163], v[144:147]\n"
    "s_add_u32 s85, s82, 0\n"
    "ds_read_b64_tr_b16 v[48:49], v2 offset:41472\n"
    "s_add_u32 s78, s78, s94\n"
    "v_mfma_f32_16x16x32_bf16 v[148:151], v[56:59], a[164:167], v[148:151]\n"
    "s_waitcnt vmcnt(12) lgkmcnt(5)\n"
    "ds_read_b64_tr_b16 v[50:51], v2 offset:45568\n"
    "ds_read_b64_tr_b16 v[52:53], v3 offset:41472\n"
    "v_mfma_f32_16x16x32_bf16 v[64:67], v[32:35], a[168:171], v[64:67]\n"
    "s_lshr_b32 s79, s63, 3\n"
    "buffer_load_dwordx4 a[192:195], v20, s[16:19], s85 offen offset:0 sc1\n"
    "v_mfma_f32_16x16x32_bf16 v[68:71], v[32:35], a[172:175], v[68:71]\n"
    "s_add_u32 s79, s79, s94\n"
    "ds_read_b64_tr_b16 v[54:55], v3 offset:45568\n"
    "v_mfma_f32_16x16x32_bf16 v[120:123], v[36:39], a[168:171], v[120:123]\n"
    "s_nop 0\n"
    "ds_read_b64_tr_b16 v[56:57], v2 offset:41728\n"
    "v_mfma_f32_16x16x32_bf16 v[124:127], v[36:39], a[172:175], v[124:127]\n"
    "s_waitcnt vmcnt(9) lgkmcnt(5)\n"
    "ds_read_b64_tr_b16 v[58:59], v2 offset:45824\n"
    "ds_read_b64_tr_b16 v[32:33], v3 offset:41728\n"
    "v_mfma_f32_16x16x32_bf16 v[72:75], v[40:43], a[168:171], v[72:75]\n"
    "s_barrier\n"
    "buffer_load_dwordx4 a[196:199], v20, s[16:19], s85 offen offset:1024 sc1\n"
    "v_mfma_f32_16x16x32_bf16 v[76:79], v[40:43], a[172:175], v[76:79]\n"
    "ds_read_b64_tr_b16 v[34:35], v3 offset:45824\n"
    "v_mfma_f32_16x16x32_bf16 v[128:131], v[44:47], a[168:171], v[128:131]\n"
    "ds_read_b64_tr_b16 v[36:37], v2 offset:49152\n"
    "v_mfma_f32_16x16x32_bf16 v[132:135], v[44:47], a[172:175], v[132:135]\n"
    "s_waitcnt lgkmcnt(5)\n"
    "ds_read_b64_tr_b16 v[38:39], v2 offset:53248\n"
    "v_mfma_f32_16x16x32_bf16 v[80:83], v[48:51], a[168:171], v[80:83]\n"
    "s_add_u32 s85, s82, 8192\n"
    "ds_read_b64_tr_b16 v[40:41], v3 offset:49152\n"
    "ds_read_b64_tr_b16 v[42:43], v3 offset:53248\n"
    "v_mfma_f32_16x16x32_bf16 v[84:87], v[48:51], a[172:175], v[84:87]\n"
    "s_nop 0\n"
    "buffer_load_dwordx4 a[200:203], v20, s[16:19], s85 offen offset:0 sc1\n"
    "v_mfma_f32_16x16x32_bf16 v[136:139], v[52:55], a[168:171], v[136:139]\n"
    "ds_read_b64_tr_b16 v[44:45], v2 offset:49408\n"
    "v_mfma_f32_16x16x32_bf16 v[140:143], v[52:55], a[172:175], v[140:143]\n"
    "s_waitcnt lgkmcnt(5)\n"
    "ds_read_b64_tr_b16 v[46:47], v2 offset:53504\n"
    "v_mfma_f32_16x16x32_bf16 v[88:91], v[56:59], a[168:171], v[88:91]\n"
    "s_nop 0\n"
    "ds_read_b64_tr_b16 v[48:49], v3 offset:49408\n"
    "v_mfma_f32_16x16x32_bf16 v[92:95], v[56:59], a[172:175], v[92:95]\n"
    "LPAD3_%=:\n"
    "s_cbranch_execz LPAD7_%=\n"
    "ds_read_b64_tr_b16 v[50:51], v3 offset:53504\n"
    "ds_read_b64_tr_b16 v[52:53], v2 offset:49664\n"
    "v_mfma_f32_16x16x32_bf16 v[144:147], v[32:35], a[168:171], v[144:147]\n"
    "s_nop 0\n"
    "buffer_load_dwordx4 a[204:207], v20, s[16:19], s85 offen offset:1024 sc1\n"
    "v_mfma_f32_16x16x32_bf16 v[148:151], v[32:35], a[172:175], v[148:151]\n"
    "s_waitcnt vmcnt(10) lgkmcnt(5)\n"
    "ds_read_b64_tr_b16 v[54:55], v2 offset:53760\n"
    "v_mfma_f32_16x16x32_bf16 v[64:67], v[36:39], a[176:179], v[64:67]\n"
    "s_nop 0\n"
    "ds_read_b64_tr_b16 v[56:57], v3 offset:49664\n"
    "s_cmp_eq_u32 s28, 5\n"
    "s_cbranch_scc0 LTB5r_%=\n"
    "buffer_load_dword v22, v21, s[12:15], 0 offen\n"
    "LTB5r_%=:\n"
    "v_mfma_f32_16x16x32_bf16 v[68:71], v[36:39], a[180:183], v[68:71]\n"
    "s_add_u32 s84, s6, 458752\n"
    "ds_read_b64_tr_b16 v[58:59], v3 offset:53760\n"
    "v_mfma_f32_16x16x32_bf16 v[120:123], v[40:43], a[176:179], v[120:123]\n"
    "s_add_u32 m0, s36, 81920\n"
    "ds_read_b64_tr_b16 v[32:33], v2 offset:49920\n"
    "ds_read_b64_tr_b16 v[34:35], v2 offset:54016\n"
    "v_mfma_f32_16x16x32_bf16 v[124:127], v[40:43], a[180:183], v[124:127]\n"
    "s_waitcnt lgkmcnt(6)\n"
    "buffer_load_dwordx4 v14, s[8:11], s84 offen lds\n"
    "v_mfma_f32_16x16x32_bf16 v[72:75], v[44:47], a[176:179], v[72:75]\n"
    "s_nop 0\n"
    "ds_read_b64_tr_b16 v[36:37], v3 offset:49920\n"
    "v_mfma_f32_16x16x32_bf16 v[76:79], v[44:47], a[180:183], v[76:79]\n"
    "s_add_u32 s84, s6, 487424\n"
    "ds_read_b64_tr_b16 v[38:39], v3 offset:54016\n"
    "v_mfma_f32_16x16x32_bf16 v[128:131], v[48:51], a[176:179], v[128:131]\n"
    "s_add_u32 m0, s36, 86016\n"
    "ds_read_b64_tr_b16 v[40:41], v2 offset:57344\n"
    "ds_read_b64_tr_b16 v[42:43], v2 offset:61440\n"
    "v_mfma_f32_16x16x32_bf16 v[132:135], v[48:51], a[180:183], v[132:135]\n"
    "s_waitcnt lgkmcnt(6)\n"
    "buffer_load_dwordx4 v14, s[8:11], s84 offen lds\n"
    "v_mfma_f32_16x16x32_bf16 v[80:83], v[52:55], a[176:179], v[80:83]\n"
    "s_add_u32 m0, s36, 90112\n"
    "ds_read_b64_tr_b16 v[44:45], v3 offset:57344\n"
    "ds_read_b64_tr_b16 v[46:47], v3 offset:61440\n"
    "v_mfma_f32_16x16x32_bf16 v[84:87], v[52:55], a[180:183], v[84:87]\n"
    "s_add_u32 s84, s6, 688128\n"
    "buffer_load_dwordx4 v14, s[8:11], s84 offen lds\n"
    "v_mfma_f32_16x16x32_bf16 v[136:139], v[56:59], a[176:179], v[136:139]\n"
    "s_add_u32 m0, s36, 94208\n"
    "ds_read_b64_tr_b16 v[48:49], v2 offset:57600\n"
    "ds_read_b64_tr_b16 v[50:51], v2 offset:61696\n"
    "v_mfma_f32_16x16x32_bf16 v[140:143], v[56:59], a[180:183], v[140:143]\n"
    "s_waitcnt lgkmcnt(6)\n"
    "s_add_u32 s84, s6, 716800\n"
    "buffer_load_dwordx4 v14, s[8:11], s84 offen lds\n"
    "v_mfma_f32_16x16x32_bf16 v[88:91], v[32:35], a[176:179], v[88:91]\n"
    "ds_read_b64_tr_b16 v[52:53], v3 offset:57600\n"
    "v_mov_b32 v198, 0xffff0000\n"
    "v_mfma_f32_16x16x32_bf16 v[92:95], v[32:35], a[180:183], v[92:95]\n"
    "ds_read_b64_tr_b16 v[54:55], v3 offset:61696\n"
    "v_mfma_f32_16x16x32_bf16 v[144:147], v[36:39], a[176:179], v[144:147]\n"
    "s_add_u32 s85, s82, 16384\n"
    "ds_read_b64_tr_b16 v[56:57], v2 offset:57856\n"
    "v_mov_b32 v199, 0x10000\n"
    "v_mfma_f32_16x16x32_bf16 v[148:151], v[36:39], a[180:183], v[148:151]\n"
    "s_waitcnt vmcnt(12) lgkmcnt(5)\n"
    "ds_read_b64_tr_b16 v[58:59], v2 offset:61952\n"
    "ds_read_b64_tr_b16 v[32:33], v3 offset:57856\n"
    "v_mfma_f32_16x16x32_bf16 v[64:67], v[40:43], a[184:187], v[64:67]\n"
    "s_nop 0\n"
    "buffer_load_dwordx4 a[208:211], v20, s[16:19], s85 offen offset:0 sc1\n"
    "v_mfma_f32_16x16x32_bf16 v[68:71], v[40:43], a[188:191], v[68:71]\n"
    "ds_read_b64_tr_b16 v[34:35], v3 offset:61952\n"
    "v_mfma_f32_16x16x32_bf16 v[120:123], v[44:47], a[184:187], v[120:123]\n"
    "ds_read_b64_tr_b16 v[36:37], v2 offset:58112\n"
    "v_mul_u32_u24_dpp v96, v28, v199 row_newbcast:8 row_mask:0xf bank_mask:0xf\n"
    "v_mfma_f32_16x16x32_bf16 v[124:127], v[44:47], a[188:191], v[124:127]\n"
    "s_waitcnt vmcnt(9) lgkmcnt(5)\n"
    "ds_read_b64_tr_b16 v[38:39], v2 offset:62208\n"
    "ds_read_b64_tr_b16 v[40:41], v3 offset:58112\n"
    "v_mfma_f32_16x16x32_bf16 v[72:75], v[48:51], a[184:187], v[72:75]\n"
    "s_barrier\n"
    "buffer_load_dwordx4 a[212:215], v20, s[16:19], s85 offen offset:1024 sc1\n"
    "v_mfma_f32_16x16x32_bf16 v[76:79], v[48:51], a[188:191], v[76:79]\n"
    "ds_read_b64_tr_b16 v[42:43], v3 offset:62208\n"
    "v_mfma_f32_16x16x32_bf16 v[128:131], v[52:55], a[184:187], v[128:131]\n"
    "ds_read_b64_tr_b16 v[44:45], v4 offset:0\n"
    "v_mfma_f32_16x16x32_bf16 v[132:135], v[52:55], a[188:191], v[132:135]\n"
    "s_waitcnt lgkmcnt(5)\n"
    "ds_read_b64_tr_b16 v[46:47], v4 offset:4096\n"
    "v_and_b32_dpp v97, v28, v198 row_newbcast:8 row_mask:0xf bank_mask:0xf\n"
    "v_mfma_f32_16x16x32_bf16 v[80:83], v[56:59], a[184:187], v[80:83]\n"
    "s_add_u32 s85, s82, 24576\n"
    "ds_read_b64_tr_b16 v[48:49], v5 offset:0\n"
    "ds_read_b64_tr_b16 v[50:51], v5 offset:4096\n"
    "v_mfma_f32_16x16x32_bf16 v[84:87], v[56:59], a[188:191], v[84:87]\n"
    "s_nop 0\n"
    "buffer_load_dwordx4 a[216:219], v20, s[16:19], s85 offen offset:0 sc1\n"
    "v_mfma_f32_16x16x32_bf16 v[136:139], v[32:35], a[184:187], v[136:139]\n"
    "ds_read_b64_tr_b16 v[52:53], v4 offset:256\n"
    "v_mfma_f32_16x16x32_bf16 v[140:143], v[32:35], a[188:191], v[140:143]\n"
    "s_waitcnt lgkmcnt(5)\n"
    "ds_read_b64_tr_b16 v[54:55], v4 offset:4352\n"
    "v_mul_u32_u24_dpp v98, v29, v199 row_newbcast:8 row_mask:0xf bank_mask:0xf\n"
    "v_mfma_f32_16x16x32_bf16 v[88:91], v[36:39], a[184:187], v[88:91]\n"
    "s_nop 0\n"
    "ds_read_b64_tr_b16 v[56:57], v5 offset:256\n"
    "v_mfma_f32_16x16x32_bf16 v[92:95], v[36:39], a[188:191], v[92:95]\n"
    "ds_read_b64_tr_b16 v[58:59], v5 offset:4352\n"
    "ds_read_b64_tr_b16 v[32:33], v4 offset:512\n"
    "v_mfma_f32_16x16x32_bf16 v[144:147], v[40:43], a[184:187], v[144:147]\n"
    "buffer_load_dwordx4 a[220:223], v20, s[16:19], s85 offen offset:1024 sc1\n"
    "v_mfma_f32_16x16x32_bf16 v[148:151], v[40:43], a[188:191], v[148:151]\n"
    "s_waitcnt vmcnt(10) lgkmcnt(5)\n"
    "ds_read_b64_tr_b16 v[34:35], v4 offset:4608\n"
    "v_mfma_f32_16x16x32_bf16 v[64:67], v[44:47], a[192:195], v[64:67]\n"
    "s_nop 0\n"
    "ds_read_b64_tr_b16 v[36:37], v5 offset:512\n"
    "v_and_b32_dpp v99, v29, v198 row_newbcast:8 row_mask:0xf bank_mask:0xf\n"
    "s_cmp_eq_u32 s28, 6\n"
    "s_cbranch_scc0 LTB6r_%=\n"
    "buffer_load_dword v22, v21, s[12:15], 0 offen\n"
    "LTB6r_%=:\n"
    "v_mfma_f32_16x16x32_bf16 v[68:71], v[44:47], a[196:199], v[68:71]\n"
    "s_add_u32 s84, s7, 0\n"
    "ds_read_b64_tr_b16 v[38:39], v5 offset:4608\n"
    "v_mfma_f32_16x16x32_bf16 v[120:123], v[48:51], a[192:195], v[120:123]\n"
    "s_add_u32 m0, s36, 98304\n"
    "ds_read_b64_tr_b16 v[40:41], v4 offset:768\n"
    "ds_read_b64_tr_b16 v[42:43], v4 offset:4864\n"
    "v_mfma_f32_16x16x32_bf16 v[124:127], v[48:51], a[196:199], v[124:127]\n"
    "s_waitcnt lgkmcnt(6)\n"
    "buffer_load_dwordx4 v14, s[8:11], s84 offen lds\n"
    "v_mfma_f32_16x16x32_bf16 v[72:75], v[52:55], a[192:195], v[72:75]\n"
    "ds_read_b64_tr_b16 v[44:45], v5 offset:768\n"
    "v_mul_u32_u24_dpp v152, v28, v199 row_newbcast:9 row_mask:0xf bank_mask:0xf\n"
    "v_mfma_f32_16x16x32_bf16 v[76:79], v[52:55], a[196:199], v[76:79]\n"
    "s_add_u32 s84, s7, 28672\n"
    "ds_read_b64_tr_b16 v[46:47], v5 offset:4864\n"
    "v_mfma_f32_16x16x32_bf16 v[128:131], v[56:59], a[192:195], v[128:131]\n"
    "s_add_u32 m0, s36, 102400\n"
    "ds_read_b64_tr_b16 v[48:49], v4 offset:8192\n"
    "ds_read_b64_tr_b16 v[50:51], v4 offset:12288\n"
    "v_mfma_f32_16x16x32_bf16 v[132:135], v[56:59], a[196:199], v[132:135]\n"
    "s_waitcnt lgkmcnt(6)\n"
    "buffer_load_dwordx4 v14, s[8:11], s84 offen lds\n"
    "v_mfma_f32_16x16x32_bf16 v[80:83], v[32:35], a[192:195], v[80:83]\n"
    "s_add_u32 m0, s36, 106496\n"
    "ds_read_b64_tr_b16 v[52:53], v5 offset:8192\n"
    "ds_read_b64_tr_b16 v[54:55], v5 offset:12288\n"
    "v_mfma_f32_16x16x32_bf16 v[84:87], v[32:35], a[196:199], v[84:87]\n"
    "s_add_u32 s84, s7, 229376\n"
    "buffer_load_dwordx4 v14, s[8:11], s84 offen lds\n"
    "v_mfma_f32_16x16x32_bf16 v[136:139], v[36:39], a[192:195], v[136:139]\n"
    "s_add_u32 m0, s36, 110592\n"
    "ds_read_b64_tr_b16 v[56:57], v4 offset:8448\n"
    "ds_read_b64_tr_b16 v[58:59], v4 offset:12544\n"
    "v_mfma_f32_16x16x32_bf16 v[140:143], v[36:39], a[196:199], v[140:143]\n"
    "s_waitcnt lgkmcnt(6)\n"
    "s_add_u32 s84, s7, 258048\n"
    "buffer_load_dwordx4 v14, s[8:11], s84 offen lds\n"
    "v_mfma_f32_16x16x32_bf16 v[88:91], v[40:43], a[192:195], v[88:91]\n"
    "ds_read_b64_tr_b16 v[32:33], v5 offset:8448\n"
    "v_and_b32_dpp v153, v28, v198 row_newbcast:9 row_mask:0xf bank_mask:0xf\n"
    "v_mfma_f32_16x16x32_bf16 v[92:95], v[40:43], a[196:199], v[92:95]\n"
    "ds_read_b64_tr_b16 v[34:35], v5 offset:12544\n"
    "v_mfma_f32_16x16x32_bf16 v[144:147], v[44:47], a[192:195], v[144:147]\n"
    "s_add_u32 s85, s37, 0\n"
    "ds_read_b64_tr_b16 v[36:37], v4 offset:8704\n"
    "v_mfma_f32_16x16x32_bf16 v[148:151], v[44:47], a[196:199], v[148:151]\n"
    "s_waitcnt vmcnt(12) lgkmcnt(5)\n"
    "ds_read_b64_tr_b16 v[38:39], v4 offset:12800\n"
    "ds_read_b64_tr_b16 v[40:41], v5 offset:8704\n"
    "v_mfma_f32_16x16x32_bf16 v[64:67], v[48:51], a[200:203], v[64:67]\n"
    "buffer_load_dwordx4 a[224:227], v20, s[16:19], s85 offen offset:0 sc1\n"
    "v_mfma_f32_16x16x32_bf16 v[68:71], v[48:51], a[204:207], v[68:71]\n"
    "ds_read_b64_tr_b16 v[42:43], v5 offset:12800\n"
    "v_mul_u32_u24_dpp v154, v29, v199 row_newbcast:9 row_mask:0xf bank_mask:0xf\n"
    "v_mfma_f32_16x16x32_bf16 v[120:123], v[52:55], a[200:203], v[120:123]\n"
    "ds_read_b64_tr_b16 v[44:45], v4 offset:8960\n"
    "v_mfma_f32_16x16x32_bf16 v[124:127], v[52:55], a[204:207], v[124:127]\n"
    "s_waitcnt vmcnt(9) lgkmcnt(5)\n"
    "ds_read_b64_tr_b16 v[46:47], v4 offset:13056\n"
    "ds_read_b64_tr_b16 v[48:49], v5 offset:8960\n"
    "v_mfma_f32_16x16x32_bf16 v[72:75], v[56:59], a[200:203], v[72:75]\n"
    "s_barrier\n"
    "buffer_load_dwordx4 a[228:231], v20, s[16:19], s85 offen offset:1024 sc1\n"
    "v_mfma_f32_16x16x32_bf16 v[76:79], v[56:59], a[204:207], v[76:79]\n"
    "ds_read_b64_tr_b16 v[50:51], v5 offset:13056\n"
    "v_and_b32_dpp v155, v29, v198 row_newbcast:9 row_mask:0xf bank_mask:0xf\n"
    "v_mfma_f32_16x16x32_bf16 v[128:131], v[32:35], a[200:203], v[128:131]\n"
    "ds_read_b64_tr_b16 v[52:53], v4 offset:16384\n"
    "v_mfma_f32_16x16x32_bf16 v[132:135], v[32:35], a[204:207], v[132:135]\n"
    "s_waitcnt lgkmcnt(5)\n"
    "ds_read_b64_tr_b16 v[54:55], v4 offset:20480\n"
    "v_mfma_f32_16x16x32_bf16 v[80:83], v[36:39], a[200:203], v[80:83]\n"
    "s_add_u32 s85, s37, 8192\n"
    "ds_read_b64_tr_b16 v[56:57], v5 offset:16384\n"
    "ds_read_b64_tr_b16 v[58:59], v5 offset:20480\n"
    "v_mfma_f32_16x16x32_bf16 v[84:87], v[36:39], a[204:207], v[84:87]\n"
    "s_nop 0\n"
    "buffer_load_dwordx4 a[232:235], v20, s[16:19], s85 offen offset:0 sc1\n"
    "v_mfma_f32_16x16x32_bf16 v[136:139], v[40:43], a[200:203], v[136:139]\n"
    "ds_read_b64_tr_b16 v[32:33], v4 offset:16640\n"
    "v_mul_u32_u24_dpp v104, v28, v199 row_newbcast:10 row_mask:0xf bank_mask:0xf\n"
    "v_mfma_f32_16x16x32_bf16 v[140:143], v[40:43], a[204:207], v[140:143]\n"
    "s_waitcnt lgkmcnt(5)\n"
    "ds_read_b64_tr_b16 v[34:35], v4 offset:20736\n"
    "v_mfma_f32_16x16x32_bf16 v[88:91], v[44:47], a[200:203], v[88:91]\n"
    "s_nop 0\n"
    "ds_read_b64_tr_b16 v[36:37], v5 offset:16640\n"
    "v_and_b32_dpp v105, v28, v198 row_newbcast:10 row_mask:0xf bank_mask:0xf\n"
    "v_mfma_f32_16x16x32_bf16 v[92:95], v[44:47], a[204:207], v[92:95]\n"
    "s_mov_b32 s87, 13893632\n"
    "ds_read_b64_tr_b16 v[38:39], v5 offset:20736\n"
    "ds_read_b64_tr_b16 v[40:41], v4 offset:16896\n"
    "v_mfma_f32_16x16x32_bf16 v[144:147], v[48:51], a[200:203], v[144:147]\n"
    "buffer_load_dwordx2 v[62:63], v25, s[16:19], s87 offen sc1\n"
    "v_mfma_f32_16x16x32_bf16 v[148:151], v[48:51], a[204:207], v[148:151]\n"
    "s_waitcnt vmcnt(10) lgkmcnt(5)\n"
    "ds_read_b64_tr_b16 v[42:43], v4 offset:20992\n"
    "v_mfma_f32_16x16x32_bf16 v[64:67], v[52:55], a[208:211], v[64:67]\n"
    "s_nop 0\n"
    "ds_read_b64_tr_b16 v[44:45], v5 offset:16896\n"
    "v_mul_u32_u24_dpp v106, v29, v199 row_newbcast:10 row_mask:0xf bank_mask:0xf\n"
    "s_cmp_eq_u32 s28, 7\n"
    "s_cbranch_scc0 LTB7r_%=\n"
    "buffer_load_dword v22, v21, s[12:15], 0 offen\n"
    "LTB7r_%=:\n"
    "v_mfma_f32_16x16x32_bf16 v[68:71], v[52:55], a[212:215], v[68:71]\n"
    "ds_read_b64_tr_b16 v[46:47], v5 offset:20992\n"
    "v_mfma_f32_16x16x32_bf16 v[120:123], v[56:59], a[208:211], v[120:123]\n"
    "ds_read_b64_tr_b16 v[48:49], v4 offset:17152\n"
    "ds_read_b64_tr_b16 v[50:51], v4 offset:21248\n"
    "v_mfma_f32_16x16x32_bf16 v[124:127], v[56:59], a[212:215], v[124:127]\n"
    "s_waitcnt lgkmcnt(6)\n"
    "buffer_load_dwordx4 a[236:239], v20, s[16:19], s85 offen offset:1024 sc1\n"
    "v_mfma_f32_16x16x32_bf16 v[72:75], v[32:35], a[208:211], v[72:75]\n"
    "s_nop 0\n"
    "ds_read_b64_tr_b16 v[52:53], v5 offset:17152\n"
    "v_mfma_f32_16x16x32_bf16 v[76:79], v[32:35], a[212:215], v[76:79]\n"
    "s_add_u32 s84, s7, 458752\n"
    "ds_read_b64_tr_b16 v[54:55], v5 offset:21248\n"
    "v_and_b32_dpp v107, v29, v198 row_newbcast:10 row_mask:0xf bank_mask:0xf\n"
    "v_mfma_f32_16x16x32_bf16 v[128:131], v[36:39], a[208:211], v[128:131]\n"
    "s_add_u32 m0, s36, 114688\n"
    "ds_read_b64_tr_b16 v[56:57], v4 offset:24576\n"
    "ds_read_b64_tr_b16 v[58:59], v4 offset:28672\n"
    "v_mfma_f32_16x16x32_bf16 v[132:135], v[36:39], a[212:215], v[132:135]\n"
    "s_waitcnt lgkmcnt(6)\n"
    "buffer_load_dwordx4 v14, s[8:11], s84 offen lds\n"
    "v_mfma_f32_16x16x32_bf16 v[80:83], v[40:43], a[208:211], v[80:83]\n"
    "s_add_u32 m0, s36, 118784\n"
    "ds_read_b64_tr_b16 v[32:33], v5 offset:24576\n"
    "ds_read_b64_tr_b16 v[34:35], v5 offset:28672\n"
    "v_mfma_f32_16x16x32_bf16 v[84:87], v[40:43], a[212:215], v[84:87]\n"
    "s_add_u32 s84, s7, 487424\n"
    "buffer_load_dwordx4 v14, s[8:11], s84 offen lds\n"
    "v_mfma_f32_16x16x32_bf16 v[136:139], v[44:47], a[208:211], v[136:139]\n"
    "s_add_u32 m0, s36, 122880\n"
    "ds_read_b64_tr_b16 v[36:37], v4 offset:24832\n"
    "ds_read_b64_tr_b16 v[38:39], v4 offset:28928\n"
    "v_mfma_f32_16x16x32_bf16 v[140:143], v[44:47], a[212:215], v[140:143]\n"
    "s_waitcnt lgkmcnt(6)\n"
    "s_add_u32 s84, s7, 688128\n"
    "buffer_load_dwordx4 v14, s[8:11], s84 offen lds\n"
    "v_mfma_f32_16x16x32_bf16 v[88:91], v[48:51], a[208:211], v[88:91]\n"
    "s_add_u32 m0, s36, 126976\n"
    "ds_read_b64_tr_b16 v[40:41], v5 offset:24832\n"
    "ds_read_b64_tr_b16 v[42:43], v5 offset:28928\n"
    "v_mfma_f32_16x16x32_bf16 v[92:95], v[48:51], a[212:215], v[92:95]\n"
    "s_add_u32 s84, s7, 716800\n"
    "buffer_load_dwordx4 v14, s[8:11], s84 offen lds\n"
    "v_mfma_f32_16x16x32_bf16 v[144:147], v[52:55], a[208:211], v[144:147]\n"
    "ds_read_b64_tr_b16 v[44:45], v4 offset:25088\n"
    "v_mfma_f32_16x16x32_bf16 v[148:151], v[52:55], a[212:215], v[148:151]\n"
    "s_waitcnt vmcnt(13) lgkmcnt(5)\n"
    "ds_read_b64_tr_b16 v[46:47], v4 offset:29184\n"
    "v_mul_u32_u24_dpp v160, v28, v199 row_newbcast:11 row_mask:0xf bank_mask:0xf\n"
    "v_mfma_f32_16x16x32_bf16 v[64:67], v[56:59], a[216:219], v[64:67]\n"
    "s_nop 0\n"
    "ds_read_b64_tr_b16 v[48:49], v5 offset:25088\n"
    "v_mfma_f32_16x16x32_bf16 v[68:71], v[56:59], a[220:223], v[68:71]\n"
    "s_add_u32 s85, s37, 16384\n"
    "ds_read_b64_tr_b16 v[50:51], v5 offset:29184\n"
    "ds_read_b64_tr_b16 v[52:53], v4 offset:25344\n"
    "v_mfma_f32_16x16x32_bf16 v[120:123], v[32:35], a[216:219], v[120:123]\n"
    "buffer_load_dwordx4 a[240:243], v20, s[16:19], s85 offen offset:0 sc1\n"
    "v_mfma_f32_16x16x32_bf16 v[124:127], v[32:35], a[220:223], v[124:127]\n"
    "s_waitcnt vmcnt(10) lgkmcnt(5)\n"
    "ds_read_b64_tr_b16 v[54:55], v4 offset:29440\n"
    "ds_read_b64_tr_b16 v[56:57], v5 offset:25344\n"
    "v_mfma_f32_16x16x32_bf16 v[72:75], v[36:39], a[216:219], v[72:75]\n"
    "s_barrier\n"
    "buffer_load_dwordx4 a[244:247], v20, s[16:19], s85 offen offset:1024 sc1\n"
    "v_mfma_f32_16x16x32_bf16 v[76:79], v[36:39], a[220:223], v[76:79]\n"
    "ds_read_b64_tr_b16 v[58:59], v5 offset:29440\n"
    "v_mfma_f32_16x16x32_bf16 v[128:131], v[40:43], a[216:219], v[128:131]\n"
    "ds_read_b64_tr_b16 v[32:33], v4 offset:32768\n"
    "v_and_b32_dpp v161, v28, v198 row_newbcast:11 row_mask:0xf bank_mask:0xf\n"
    "v_mfma_f32_16x16x32_bf16 v[132:135], v[40:43], a[220:223], v[132:135]\n"
    "s_waitcnt lgkmcnt(5)\n"
    "ds_read_b64_tr_b16 v[34:35], v4 offset:36864\n"
    "v_mfma_f32_16x16x32_bf16 v[80:83], v[44:47], a[216:219], v[80:83]\n"
    "s_add_u32 s85, s37, 24576\n"
    "ds_read_b64_tr_b16 v[36:37], v5 offset:32768\n"
    "ds_read_b64_tr_b16 v[38:39], v5 offset:36864\n"
    "v_mfma_f32_16x16x32_bf16 v[84:87], v[44:47], a[220:223], v[84:87]\n"
    "s_nop 0\n"
    "buffer_load_dwordx4 a[248:251], v20, s[16:19], s85 offen offset:0 sc1\n"
    "v_mfma_f32_16x16x32_bf16 v[136:139], v[48:51], a[216:219], v[136:139]\n"
    "ds_read_b64_tr_b16 v[40:41], v4 offset:33024\n"
    "v_mul_u32_u24_dpp v162, v29, v199 row_newbcast:11 row_mask:0xf bank_mask:0xf\n"
    "v_mfma_f32_16x16x32_bf16 v[140:143], v[48:51], a[220:223], v[140:143]\n"
    "s_waitcnt lgkmcnt(5)\n"
    "ds_read_b64_tr_b16 v[42:43], v4 offset:37120\n"
    "v_mfma_f32_16x16x32_bf16 v[88:91], v[52:55], a[216:219], v[88:91]\n"
    "s_nop 0\n"
    "ds_read_b64_tr_b16 v[44:45], v5 offset:33024\n"
    "v_and_b32_dpp v163, v29, v198 row_newbcast:11 row_mask:0xf bank_mask:0xf\n"
    "v_mfma_f32_16x16x32_bf16 v[92:95], v[52:55], a[220:223], v[92:95]\n"
    "ds_read_b64_tr_b16 v[46:47], v5 offset:37120\n"
    "ds_read_b64_tr_b16 v[48:49], v4 offset:33280\n"
    "v_mfma_f32_16x16x32_bf16 v[144:147], v[56:59], a[216:219], v[144:147]\n"
    "buffer_load_dwordx4 a[252:255], v20, s[16:19], s85 offen offset:1024 sc1\n"
    "v_mfma_f32_16x16x32_bf16 v[148:151], v[56:59], a[220:223], v[148:151]\n"
    "s_waitcnt vmcnt(11) lgkmcnt(5)\n"
    "ds_read_b64_tr_b16 v[50:51], v4 offset:37376\n"
    "v_mfma_f32_16x16x32_bf16 v[64:67], v[32:35], a[224:227], v[64:67]\n"
    "s_bfe_u32 s64, s64, 0x100002\n"
    "ds_read_b64_tr_b16 v[52:53], v5 offset:33280\n"
    "s_bfe_u32 s65, s65, 0x100002\n"
    "v_mfma_f32_16x16x32_bf16 v[68:71], v[32:35], a[228:231], v[68:71]\n"
    "s_add_u32 s84, s0, 0\n"
    "ds_read_b64_tr_b16 v[54:55], v5 offset:37376\n"
    "v_mul_u32_u24_dpp v112, v28, v199 row_newbcast:12 row_mask:0xf bank_mask:0xf\n"
    "v_mfma_f32_16x16x32_bf16 v[120:123], v[36:39], a[224:227], v[120:123]\n"
    "s_add_u32 m0, s36, 0\n"
    "ds_read_b64_tr_b16 v[56:57], v4 offset:33536\n"
    "ds_read_b64_tr_b16 v[58:59], v4 offset:37632\n"
    "v_mfma_f32_16x16x32_bf16 v[124:127], v[36:39], a[228:231], v[124:127]\n"
    "s_waitcnt lgkmcnt(6)\n"
    "buffer_load_dwordx4 v15, s[8:11], s84 offen lds\n"
    "v_mfma_f32_16x16x32_bf16 v[72:75], v[40:43], a[224:227], v[72:75]\n"
    "s_bfe_u32 s66, s66, 0x100002\n"
    "ds_read_b64_tr_b16 v[32:33], v5 offset:33536\n"
    "s_bfe_u32 s67, s67, 0x100002\n"
    "v_mfma_f32_16x16x32_bf16 v[76:79], v[40:43], a[228:231], v[76:79]\n"
    "s_add_u32 s84, s0, 229376\n"
    "ds_read_b64_tr_b16 v[34:35], v5 offset:37632\n"
    "v_and_b32_dpp v113, v28, v198 row_newbcast:12 row_mask:0xf bank_mask:0xf\n"
    "v_mfma_f32_16x16x32_bf16 v[128:131], v[44:47], a[224:227], v[128:131]\n"
    "s_add_u32 m0, s36, 4096\n"
    "ds_read_b64_tr_b16 v[36:37], v4 offset:40960\n"
    "ds_read_b64_tr_b16 v[38:39], v4 offset:45056\n"
    "v_mfma_f32_16x16x32_bf16 v[132:135], v[44:47], a[228:231], v[132:135]\n"
    "s_waitcnt lgkmcnt(6)\n"
    "buffer_load_dwordx4 v15, s[8:11], s84 offen lds\n"
    "v_mfma_f32_16x16x32_bf16 v[80:83], v[48:51], a[224:227], v[80:83]\n"
    "s_add_u32 m0, s36, 8192\n"
    "ds_read_b64_tr_b16 v[40:41], v5 offset:40960\n"
    "ds_read_b64_tr_b16 v[42:43], v5 offset:45056\n"
    "v_mfma_f32_16x16x32_bf16 v[84:87], v[48:51], a[228:231], v[84:87]\n"
    "s_add_u32 s84, s0, 458752\n"
    "buffer_load_dwordx4 v15, s[8:11], s84 offen lds\n"
    "v_mfma_f32_16x16x32_bf16 v[136:139], v[52:55], a[224:227], v[136:139]\n"
    "s_add_u32 m0, s36, 12288\n"
    "ds_read_b64_tr_b16 v[44:45], v4 offset:41216\n"
    "ds_read_b64_tr_b16 v[46:47], v4 offset:45312\n"
    "v_mfma_f32_16x16x32_bf16 v[140:143], v[52:55], a[228:231], v[140:143]\n"
    "s_waitcnt vmcnt(12) lgkmcnt(6)\n"
    "s_add_u32 s84, s0, 688128\n"
    "buffer_load_dwordx4 v15, s[8:11], s84 offen lds\n"
    "v_cmp_ne_u64 vcc, s[98:99], v[62:63]\n"
    "v_mfma_f32_16x16x32_bf16 v[88:91], v[56:59], a[224:227], v[88:91]\n"
    "s_bfe_u32 s80, s80, 0x100002\n"
    "ds_read_b64_tr_b16 v[48:49], v5 offset:41216\n"
    "s_bfe_u32 s81, s81, 0x100002\n"
    "v_mfma_f32_16x16x32_bf16 v[92:95], v[56:59], a[228:231], v[92:95]\n"
    "s_add_u32 s84, s1, 0\n"
    "ds_read_b64_tr_b16 v[50:51], v5 offset:45312\n"
    "s_bfe_u32 s82, s82, 0x100002\n"
    "s_cbranch_vccnz LPLZ_%=s\n"
    "LPLZ_%=:\n"
    "v_mfma_f32_16x16x32_bf16 v[144:147], v[32:35], a[224:227], v[144:147]\n"
    "s_add_u32 m0, s36, 16384\n"
    "ds_read_b64_tr_b16 v[52:53], v4 offset:41472\n"
    "v_mul_u32_u24_dpp v114, v29, v199 row_newbcast:12 row_mask:0xf bank_mask:0xf\n"
    "v_mfma_f32_16x16x32_bf16 v[148:151], v[32:35], a[228:231], v[148:151]\n"
    "s_waitcnt vmcnt(12) lgkmcnt(5)\n"
    "ds_read_b64_tr_b16 v[54:55], v4 offset:45568\n"
    "ds_read_b64_tr_b16 v[56:57], v5 offset:41472\n"
    "v_mfma_f32_16x16x32_bf16 v[64:67], v[36:39], a[232:235], v[64:67]\n"
    "s_bfe_u32 s37, s37, 0x100002\n"
    "buffer_load_dwordx4 v15, s[8:11], s84 offen lds\n"
    "v_mfma_f32_16x16x32_bf16 v[68:71], v[36:39], a[236:239], v[68:71]\n"
    "s_add_u32 s84, s1, 229376\n"
    "ds_read_b64_tr_b16 v[58:59], v5 offset:45568\n"
    "v_mfma_f32_16x16x32_bf16 v[120:123], v[40:43], a[232:235], v[120:123]\n"
    "s_add_u32 m0, s36, 20480\n"
    "ds_read_b64_tr_b16 v[32:33], v4 offset:41728\n"
    "v_and_b32_dpp v115, v29, v198 row_newbcast:12 row_mask:0xf bank_mask:0xf\n"
    "v_mfma_f32_16x16x32_bf16 v[124:127], v[40:43], a[236:239], v[124:127]\n"
    "s_waitcnt vmcnt(9) lgkmcnt(5)\n"
    "ds_read_b64_tr_b16 v[34:35], v4 offset:45824\n"
    "ds_read_b64_tr_b16 v[36:37], v5 offset:41728\n"
    "v_mfma_f32_16x16x32_bf16 v[72:75], v[44:47], a[232:235], v[72:75]\n"
    "s_barrier\n"
    "buffer_load_dwordx4 v15, s[8:11], s84 offen lds\n"
    "v_mfma_f32_16x16x32_bf16 v[76:79], v[44:47], a[236:239], v[76:79]\n"
    "ds_read_b64_tr_b16 v[38:39], v5 offset:45824\n"
    "v_mfma_f32_16x16x32_bf16 v[128:131], v[48:51], a[232:235], v[128:131]\n"
    "s_add_u32 s84, s1, 458752\n"
    "ds_read_b64_tr_b16 v[40:41], v4 offset:49152\n"
    "v_mul_u32_u24_dpp v168, v28, v199 row_newbcast:13 row_mask:0xf bank_mask:0xf\n"
    "v_mfma_f32_16x16x32_bf16 v[132:135], v[48:51], a[236:239], v[132:135]\n"
    "s_waitcnt lgkmcnt(5)\n"
    "ds_read_b64_tr_b16 v[42:43], v4 offset:53248\n"
    "v_mfma_f32_16x16x32_bf16 v[80:83], v[52:55], a[232:235], v[80:83]\n"
    "s_add_u32 m0, s36, 24576\n"
    "ds_read_b64_tr_b16 v[44:45], v5 offset:49152\n"
    "ds_read_b64_tr_b16 v[46:47], v5 offset:53248\n"
    "v_mfma_f32_16x16x32_bf16 v[84:87], v[52:55], a[236:239], v[84:87]\n"
    "s_nop 0\n"
    "buffer_load_dwordx4 v15, s[8:11], s84 offen lds\n"
    "v_mfma_f32_16x16x32_bf16 v[136:139], v[56:59], a[232:235], v[136:139]\n"
    "s_add_u32 s84, s1, 688128\n"
    "ds_read_b64_tr_b16 v[48:49], v4 offset:49408\n"
    "v_mfma_f32_16x16x32_bf16 v[140:143], v[56:59], a[236:239], v[140:143]\n"
    "s_waitcnt lgkmcnt(5)\n"
    "ds_read_b64_tr_b16 v[50:51], v4 offset:53504\n"
    "v_and_b32_dpp v169, v28, v198 row_newbcast:13 row_mask:0xf bank_mask:0xf\n"
    "v_mfma_f32_16x16x32_bf16 v[88:91], v[32:35], a[232:235], v[88:91]\n"
    "s_add_u32 m0, s36, 28672\n"
    "ds_read_b64_tr_b16 v[52:53], v5 offset:49408\n"
    "ds_read_b64_tr_b16 v[54:55], v5 offset:53504\n"
    "v_mfma_f32_16x16x32_bf16 v[92:95], v[32:35], a[236:239], v[92:95]\n"
    "s_nop 0\n"
    "buffer_load_dwordx4 v15, s[8:11], s84 offen lds\n"
    "v_mfma_f32_16x16x32_bf16 v[144:147], v[36:39], a[232:235], v[144:147]\n"
    "ds_read_b64_tr_b16 v[56:57], v4 offset:49664\n"
    "v_mfma_f32_16x16x32_bf16 v[148:151], v[36:39], a[236:239], v[148:151]\n"
    "s_waitcnt vmcnt(10) lgkmcnt(5)\n"
    "ds_read_b64_tr_b16 v[58:59], v4 offset:53760\n"
    "v_mul_u32_u24_dpp v170, v29, v199 row_newbcast:13 row_mask:0xf bank_mask:0xf\n"
    "v_mfma_f32_16x16x32_bf16 v[64:67], v[40:43], a[240:243], v[64:67]\n"
    "s_add_u32 s84, s2, 0\n"
    "ds_read_b64_tr_b16 v[32:33], v5 offset:49664\n"
    "v_mfma_f32_16x16x32_bf16 v[68:71], v[40:43], a[244:247], v[68:71]\n"
    "s_add_u32 m0, s36, 32768\n"
    "ds_read_b64_tr_b16 v[34:35], v5 offset:53760\n"
    "ds_read_b64_tr_b16 v[36:37], v4 offset:49920\n"
    "v_mfma_f32_16x16x32_bf16 v[120:123], v[44:47], a[240:243], v[120:123]\n"
    "buffer_load_dwordx4 v15, s[8:11], s84 offen lds\n"
    "v_mfma_f32_16x16x32_bf16 v[124:127], v[44:47], a[244:247], v[124:127]\n"
    "s_waitcnt lgkmcnt(5)\n"
    "ds_read_b64_tr_b16 v[38:39], v4 offset:54016\n"
    "v_mfma_f32_16x16x32_bf16 v[72:75], v[48:51], a[240:243], v[72:75]\n"
    "s_add_u32 s84, s2, 229376\n"
    "ds_read_b64_tr_b16 v[40:41], v5 offset:49920\n"
    "v_and_b32_dpp v171, v29, v198 row_newbcast:13 row_mask:0xf bank_mask:0xf\n"
    "v_mfma_f32_16x16x32_bf16 v[76:79], v[48:51], a[244:247], v[76:79]\n"
    "s_add_u32 m0, s36, 36864\n"
    "ds_read_b64_tr_b16 v[42:43], v5 offset:54016\n"
    "ds_read_b64_tr_b16 v[44:45], v4 offset:57344\n"
    "v_mfma_f32_16x16x32_bf16 v[128:131], v[52:55], a[240:243], v[128:131]\n"
    "s_nop 0\n"
    "buffer_load_dwordx4 v15, s[8:11], s84 offen lds\n"
    "v_mfma_f32_16x16x32_bf16 v[132:135], v[52:55], a[244:247], v[132:135]\n"
    "s_waitcnt lgkmcnt(5)\n"
    "ds_read_b64_tr_b16 v[46:47], v4 offset:61440\n"
    "v_mfma_f32_16x16x32_bf16 v[80:83], v[56:59], a[240:243], v[80:83]\n"
    "s_nop 0\n"
    "ds_read_b64_tr_b16 v[48:49], v5 offset:57344\n"
    "v_and_b32_e64 v28, 3, v30\n"
    "v_mfma_f32_16x16x32_bf16 v[84:87], v[56:59], a[244:247], v[84:87]\n"
    "s_add_u32 s84, s2, 458752\n"
    "ds_read_b64_tr_b16 v[50:51], v5 offset:61440\n"
    "v_mfma_f32_16x16x32_bf16 v[136:139], v[32:35], a[240:243], v[136:139]\n"
    "s_add_u32 m0, s36, 40960\n"
    "ds_read_b64_tr_b16 v[52:53], v4 offset:57600\n"
    "ds_read_b64_tr_b16 v[54:55], v4 offset:61696\n"
    "v_mfma_f32_16x16x32_bf16 v[140:143], v[32:35], a[244:247], v[140:143]\n"
    "s_waitcnt lgkmcnt(6)\n"
    "buffer_load_dwordx4 v15, s[8:11], s84 offen lds\n"
    "v_mfma_f32_16x16x32_bf16 v[88:91], v[36:39], a[240:243], v[88:91]\n"
    "s_nop 0\n"
    "ds_read_b64_tr_b16 v[56:57], v5 offset:57600\n"
    "v_lshlrev_b32_e64 v28, 5, v28\n"
    "v_mfma_f32_16x16x32_bf16 v[92:95], v[36:39], a[244:247], v[92:95]\n"
    "s_add_u32 s84, s2, 688128\n"
    "ds_read_b64_tr_b16 v[58:59], v5 offset:61696\n"
    "v_mfma_f32_16x16x32_bf16 v[144:147], v[40:43], a[240:243], v[144:147]\n"
    "s_add_u32 m0, s36, 45056\n"
    "ds_read_b64_tr_b16 v[32:33], v4 offset:57856\n"
    "ds_read_b64_tr_b16 v[34:35], v4 offset:61952\n"
    "v_mfma_f32_16x16x32_bf16 v[148:151], v[40:43], a[244:247], v[148:151]\n"
    "s_waitcnt vmcnt(11) lgkmcnt(6)\n"
    "buffer_load_dwordx4 v15, s[8:11], s84 offen lds\n"
    "v_mfma_f32_16x16x32_bf16 v[64:67], v[44:47], a[248:251], v[64:67]\n"
    "s_nop 0\n"
    "ds_read_b64_tr_b16 v[36:37], v5 offset:57856\n"
    "v_mfma_f32_16x16x32_bf16 v[68:71], v[44:47], a[252:255], v[68:71]\n"
    "s_add_u32 s84, s3, 0\n"
    "ds_read_b64_tr_b16 v[38:39], v5 offset:61952\n"
    "v_lshrrev_b32 v198, 4, v30\n"
    "v_mfma_f32_16x16x32_bf16 v[120:123], v[48:51], a[248:251], v[120:123]\n"
    "s_add_u32 m0, s36, 49152\n"
    "ds_read_b64_tr_b16 v[40:41], v4 offset:58112\n"
    "v_mfma_f32_16x16x32_bf16 v[124:127], v[48:51], a[252:255], v[124:127]\n"
    "s_waitcnt vmcnt(8) lgkmcnt(5)\n"
    "ds_read_b64_tr_b16 v[42:43], v4 offset:62208\n"
    "ds_read_b64_tr_b16 v[44:45], v5 offset:58112\n"
    "v_mfma_f32_16x16x32_bf16 v[72:75], v[52:55], a[248:251], v[72:75]\n"
    "s_barrier\n"
    "buffer_load_dwordx4 v15, s[8:11], s84 offen lds\n"
    "v_mfma_f32_16x16x32_bf16 v[76:79], v[52:55], a[252:255], v[76:79]\n"
    "s_add_u32 s84, s3, 229376\n"
    "ds_read_b64_tr_b16 v[46:47], v5 offset:62208\n"
    "v_lshl_add_u32 v28, v198, 3, v28\n"
    "v_mfma_f32_16x16x32_bf16 v[128:131], v[56:59], a[248:251], v[128:131]\n"
    "s_add_u32 m0, s36, 53248\n"
    "ds_read_b64_tr_b16 v[48:49], v6 offset:0\n"
    "v_mfma_f32_16x16x32_bf16 v[132:135], v[56:59], a[252:255], v[132:135]\n"
    "s_waitcnt lgkmcnt(5)\n"
    "ds_read_b64_tr_b16 v[50:51], v6 offset:2048\n"
    "ds_read_b64_tr_b16 v[52:53], v7 offset:0\n"
    "v_mfma_f32_16x16x32_bf16 v[80:83], v[32:35], a[248:251], v[80:83]\n"
    "s_nop 0\n"
    "buffer_load_dwordx4 v15, s[8:11], s84 offen lds\n"
    "v_mfma_f32_16x16x32_bf16 v[84:87], v[32:35], a[252:255], v[84:87]\n"
    "ds_read_b64_tr_b16 v[54:55], v7 offset:2048\n"
    "v_mfma_f32_16x16x32_bf16 v[136:139], v[36:39], a[248:251], v[136:139]\n"
    "s_add_u32 s84, s3, 458752\n"
    "ds_read_b64_tr_b16 v[56:57], v6 offset:256\n"
    "v_mfma_f32_16x16x32_bf16 v[140:143], v[36:39], a[252:255], v[140:143]\n"
    "s_waitcnt lgkmcnt(5)\n"
    "ds_read_b64_tr_b16 v[58:59], v6 offset:2304\n"
    "v_mfma_f32_16x16x32_bf16 v[88:91], v[40:43], a[248:251], v[88:91]\n"
    "s_add_u32 m0, s36, 57344\n"
    "ds_read_b64_tr_b16 v[32:33], v7 offset:256\n"
    "ds_read_b64_tr_b16 v[34:35], v7 offset:2304\n"
    "v_mfma_f32_16x16x32_bf16 v[92:95], v[40:43], a[252:255], v[92:95]\n"
    "s_nop 0\n"
    "buffer_load_dwordx4 v15, s[8:11], s84 offen lds\n"
    "v_mfma_f32_16x16x32_bf16 v[144:147], v[44:47], a[248:251], v[144:147]\n"
    "s_add_u32 s84, s3, 688128\n"
    "ds_read_b64_tr_b16 v[36:37], v6 offset:4096\n"
    "v_mfma_f32_16x16x32_bf16 v[148:151], v[44:47], a[252:255], v[148:151]\n"
    "s_waitcnt lgkmcnt(5)\n"
    "ds_read_b64_tr_b16 v[38:39], v6 offset:6144\n"
    "v_mfma_f32_16x16x32_bf16 v[100:103], v[48:51], a[4:7], v[96:99]\n"
    "s_add_u32 m0, s36, 61440\n"
    "ds_read_b64_tr_b16 v[40:41], v7 offset:4096\n"
    "ds_read_b64_tr_b16 v[42:43], v7 offset:6144\n"
    "v_mfma_f32_16x16x32_bf16 v[96:99], v[48:51], a[0:3], v[96:99]\n"
    "s_nop 0\n"
    "buffer_load_dwordx4 v15, s[8:11], s84 offen lds\n"
    "v_mfma_f32_16x16x32_bf16 v[156:159], v[52:55], a[4:7], v[152:155]\n"
    "LPAD4_%=:\n"
    "s_cbranch_execz LPADR_%=\n"
    "ds_read_b64_tr_b16 v[44:45], v6 offset:4352\n"
    "v_mfma_f32_16x16x32_bf16 v[152:155], v[52:55], a[0:3], v[152:155]\n"
    "s_waitcnt lgkmcnt(5)\n"
    "ds_read_b64_tr_b16 v[46:47], v6 offset:6400\n"
    "v_mfma_f32_16x16x32_bf16 v[108:111], v[56:59], a[4:7], v[104:107]\n"
    "s_add_u32 s84, s4, 0\n"
    "ds_read_b64_tr_b16 v[48:49], v7 offset:4352\n"
    "v_mul_f32_e64 v64, v64, |v64|\n"
    "v_mul_f32_e64 v65, v65, |v65|\n"
    "v_mfma_f32_16x16x32_bf16 v[104:107], v[56:59], a[0:3], v[104:107]\n"
    "s_add_u32 m0, s36, 65536\n"
    "ds_read_b64_tr_b16 v[50:51], v7 offset:6400\n"
    "ds_read_b64_tr_b16 v[52:53], v6 offset:8192\n"
    "v_mfma_f32_16x16x32_bf16 v[164:167], v[32:35], a[4:7], v[160:163]\n"
    "s_nop 0\n"
    "buffer_load_dwordx4 v15, s[8:11], s84 offen lds\n"
    "v_mfma_f32_16x16x32_bf16 v[160:163], v[32:35], a[0:3], v[160:163]\n"
    "s_waitcnt lgkmcnt(5)\n"
    "ds_read_b64_tr_b16 v[54:55], v6 offset:10240\n"
    "v_mul_f32_e64 v66, v66, |v66|\n"
    "v_mfma_f32_16x16x32_bf16 v[96:99], v[36:39], a[8:11], v[96:99]\n"
    "s_add_u32 s84, s4, 229376\n"
    "ds_read_b64_tr_b16 v[56:57], v7 offset:8192\n"
    "v_mul_f32_e64 v67, v67, |v67|\n"
    "v_mfma_f32_16x16x32_bf16 v[100:103], v[36:39], a[12:15], v[100:103]\n"
    "s_add_u32 m0, s36, 69632\n"
    "ds_read_b64_tr_b16 v[58:59], v7 offset:10240\n"
    "ds_read_b64_tr_b16 v[32:33], v6 offset:8448\n"
    "v_mfma_f32_16x16x32_bf16 v[152:155], v[40:43], a[8:11], v[152:155]\n"
    "s_nop 0\n"
    "buffer_load_dwordx4 v15, s[8:11], s84 offen lds\n"
    "v_mfma_f32_16x16x32_bf16 v[156:159], v[40:43], a[12:15], v[156:159]\n"
    "s_waitcnt lgkmcnt(5)\n"
    "ds_read_b64_tr_b16 v[34:35], v6 offset:10496\n"
    "v_mul_f32_e64 v120, v120, |v120|\n"
    "v_mfma_f32_16x16x32_bf16 v[104:107], v[44:47], a[8:11], v[104:107]\n"
    "s_nop 0\n"
    "ds_read_b64_tr_b16 v[36:37], v7 offset:8448\n"
    "v_mul_f32_e64 v121, v121, |v121|\n"
    "v_mul_f32_e64 v122, v122, |v122|\n"
    "v_mfma_f32_16x16x32_bf16 v[108:111], v[44:47], a[12:15], v[108:111]\n"
    "s_add_u32 s84, s4, 458752\n"
    "ds_read_b64_tr_b16 v[38:39], v7 offset:10496\n"
    "v_mul_f32_e64 v123, v123, |v123|\n"
    "v_mfma_f32_16x16x32_bf16 v[160:163], v[48:51], a[8:11], v[160:163]\n"
    "s_add_u32 m0, s36, 73728\n"
    "ds_read_b64_tr_b16 v[40:41], v6 offset:12288\n"
    "ds_read_b64_tr_b16 v[42:43], v6 offset:14336\n"
    "v_mfma_f32_16x16x32_bf16 v[164:167], v[48:51], a[12:15], v[164:167]\n"
    "s_waitcnt lgkmcnt(6)\n"
    "buffer_load_dwordx4 v15, s[8:11], s84 offen lds\n"
    "v_mfma_f32_16x16x32_bf16 v[96:99], v[52:55], a[16:19], v[96:99]\n"
    "s_nop 0\n"
    "ds_read_b64_tr_b16 v[44:45], v7 offset:12288\n"
    "v_cvt_pk_bf16_f32 v64, v64, v65\n"
    "v_mfma_f32_16x16x32_bf16 v[100:103], v[52:55], a[20:23], v[100:103]\n"
    "s_add_u32 s84, s4, 688128\n"
    "ds_read_b64_tr_b16 v[46:47], v7 offset:14336\n"
    "v_cvt_pk_bf16_f32 v65, v66, v67\n"
    "v_mfma_f32_16x16x32_bf16 v[152:155], v[56:59], a[16:19], v[152:155]\n"
    "s_add_u32 m0, s36, 77824\n"
    "ds_read_b64_tr_b16 v[48:49], v6 offset:12544\n"
    "ds_read_b64_tr_b16 v[50:51], v6 offset:14592\n"
    "v_mfma_f32_16x16x32_bf16 v[156:159], v[56:59], a[20:23], v[156:159]\n"
    "s_waitcnt vmcnt(11) lgkmcnt(6)\n"
    "buffer_load_dwordx4 v15, s[8:11], s84 offen lds\n"
    "v_mfma_f32_16x16x32_bf16 v[104:107], v[32:35], a[16:19], v[104:107]\n"
    "s_barrier\n"
    "ds_read_b64_tr_b16 v[52:53], v7 offset:12544\n"
    "v_cvt_pk_bf16_f32 v66, v120, v121\n"
    "v_cvt_pk_bf16_f32 v67, v122, v123\n"
    "v_mfma_f32_16x16x32_bf16 v[108:111], v[32:35], a[20:23], v[108:111]\n"
    "s_add_u32 s84, s5, 0\n"
    "ds_read_b64_tr_b16 v[54:55], v7 offset:14592\n"
    "v_pk_max_i16 v64, v64, 0\n"
    "v_mfma_f32_16x16x32_bf16 v[160:163], v[36:39], a[16:19], v[160:163]\n"
    "s_add_u32 m0, s36, 81920\n"
    "ds_read_b64_tr_b16 v[56:57], v6 offset:16384\n"
    "v_pk_max_i16 v65, v65, 0\n"
    "v_mfma_f32_16x16x32_bf16 v[164:167], v[36:39], a[20:23], v[164:167]\n"
    "s_waitcnt lgkmcnt(5)\n"
    "ds_read_b64_tr_b16 v[58:59], v6 offset:18432\n"
    "ds_read_b64_tr_b16 v[32:33], v7 offset:16384\n"
    "v_mfma_f32_16x16x32_bf16 v[96:99], v[40:43], a[24:27], v[96:99]\n"
    "buffer_load_dwordx4 v15, s[8:11], s84 offen lds\n"
    "v_mfma_f32_16x16x32_bf16 v[100:103], v[40:43], a[28:31], v[100:103]\n"
    "ds_read_b64_tr_b16 v[34:35], v7 offset:18432\n"
    "v_pk_max_i16 v66, v66, 0\n"
    "v_mfma_f32_16x16x32_bf16 v[152:155], v[44:47], a[24:27], v[152:155]\n"
    "s_add_u32 s86, s68, 0\n"
    "ds_read_b64_tr_b16 v[36:37], v6 offset:16640\n"
    "v_pk_max_i16 v67, v67, 0\n"
    "v_mfma_f32_16x16x32_bf16 v[156:159], v[44:47], a[28:31], v[156:159]\n"
    "s_waitcnt lgkmcnt(5)\n"
    "ds_read_b64_tr_b16 v[38:39], v6 offset:18688\n"
    "ds_read_b64_tr_b16 v[40:41], v7 offset:16640\n"
    "v_mfma_f32_16x16x32_bf16 v[104:107], v[48:51], a[24:27], v[104:107]\n"
    "buffer_store_dwordx4 v[64:67], v20, s[16:19], s86 offen offset:0 sc1\n"
    "v_mfma_f32_16x16x32_bf16 v[108:111], v[48:51], a[28:31], v[108:111]\n"
    "ds_read_b64_tr_b16 v[42:43], v7 offset:18688\n"
    "v_mul_f32_e64 v68, v68, |v68|\n"
    "v_mul_f32_e64 v69, v69, |v69|\n"
    "v_mfma_f32_16x16x32_bf16 v[160:163], v[52:55], a[24:27], v[160:163]\n"
    "ds_read_b64_tr_b16 v[44:45], v6 offset:20480\n"
    "v_mul_f32_e64 v70, v70, |v70|\n"
    "v_mfma_f32_16x16x32_bf16 v[164:167], v[52:55], a[28:31], v[164:167]\n"
    "s_waitcnt lgkmcnt(5)\n"
    "ds_read_b64_tr_b16 v[46:47], v6 offset:22528\n"
    "v_mul_f32_e64 v71, v71, |v71|\n"
    "v_mfma_f32_16x16x32_bf16 v[96:99], v[56:59], a[32:35], v[96:99]\n"
    "s_nop 0\n"
    "ds_read_b64_tr_b16 v[48:49], v7 offset:20480\n"
    "v_mul_f32_e64 v124, v124, |v124|\n"
    "v_mfma_f32_16x16x32_bf16 v[100:103], v[56:59], a[36:39], v[100:103]\n"
    "s_add_u32 s84, s5, 229376\n"
    "ds_read_b64_tr_b16 v[50:51], v7 offset:22528\n"
    "v_mul_f32_e64 v125, v125, |v125|\n"
    "v_mfma_f32_16x16x32_bf16 v[152:155], v[32:35], a[32:35], v[152:155]\n"
    "s_add_u32 m0, s36, 86016\n"
    "ds_read_b64_tr_b16 v[52:53], v6 offset:20736\n"
    "ds_read_b64_tr_b16 v[54:55], v6 offset:22784\n"
    "v_mul_f32_e64 v126, v126, |v126|\n"
    "v_mfma_f32_16x16x32_bf16 v[156:159], v[32:35], a[36:39], v[156:159]\n"
    "s_waitcnt lgkmcnt(6)\n"
    "buffer_load_dwordx4 v15, s[8:11], s84 offen lds\n"
    "v_mfma_f32_16x16x32_bf16 v[104:107], v[36:39], a[32:35], v[104:107]\n"
    "s_nop 0\n"
    "ds_read_b64_tr_b16 v[56:57], v7 offset:20736\n"
    "v_mul_f32_e64 v127, v127, |v127|\n"
    "v_mfma_f32_16x16x32_bf16 v[108:111], v[36:39], a[36:39], v[108:111]\n"
    "s_add_u32 s84, s5, 458752\n"
    "ds_read_b64_tr_b16 v[58:59], v7 offset:22784\n"
    "v_cvt_pk_bf16_f32 v68, v68, v69\n"
    "v_mfma_f32_16x16x32_bf16 v[160:163], v[40:43], a[32:35], v[160:163]\n"
    "s_add_u32 m0, s36, 90112\n"
    "ds_read_b64_tr_b16 v[32:33], v6 offset:24576\n"
    "v_cvt_pk_bf16_f32 v69, v70, v71\n"
    "v_mfma_f32_16x16x32_bf16 v[164:167], v[40:43], a[36:39], v[164:167]\n"
    "s_waitcnt lgkmcnt(5)\n"
    "ds_read_b64_tr_b16 v[34:35], v6 offset:26624\n"
    "ds_read_b64_tr_b16 v[36:37], v7 offset:24576\n"
    "v_mfma_f32_16x16x32_bf16 v[96:99], v[44:47], a[40:43], v[96:99]\n"
    "s_nop 0\n"
    "buffer_load_dwordx4 v15, s[8:11], s84 offen lds\n"
    "v_mfma_f32_16x16x32_bf16 v[100:103], v[44:47], a[44:47], v[100:103]\n"
    "s_add_u32 s84, s5, 688128\n"
    "ds_read_b64_tr_b16 v[38:39], v7 offset:26624\n"
    "v_cvt_pk_bf16_f32 v70, v124, v125\n"
    "v_mfma_f32_16x16x32_bf16 v[152:155], v[48:51], a[40:43], v[152:155]\n"
    "s_add_u32 m0, s36, 94208\n"
    "ds_read_b64_tr_b16 v[40:41], v6 offset:24832\n"
    "v_cvt_pk_bf16_f32 v71, v126, v127\n"
    "v_pk_max_i16 v68, v68, 0\n"
    "v_mfma_f32_16x16x32_bf16 v[156:159], v[48:51], a[44:47], v[156:159]\n"
    "s_waitcnt lgkmcnt(5)\n"
    "ds_read_b64_tr_b16 v[42:43], v6 offset:26880\n"
    "ds_read_b64_tr_b16 v[44:45], v7 offset:24832\n"
    "v_mfma_f32_16x16x32_bf16 v[104:107], v[52:55], a[40:43], v[104:107]\n"
    "s_nop 0\n"
    "buffer_load_dwordx4 v15, s[8:11], s84 offen lds\n"
    "v_mfma_f32_16x16x32_bf16 v[108:111], v[52:55], a[44:47], v[108:111]\n"
    "ds_read_b64_tr_b16 v[46:47], v7 offset:26880\n"
    "v_pk_max_i16 v69, v69, 0\n"
    "v_mfma_f32_16x16x32_bf16 v[160:163], v[56:59], a[40:43], v[160:163]\n"
    "ds_read_b64_tr_b16 v[48:49], v6 offset:28672\n"
    "v_pk_max_i16 v70, v70, 0\n"
    "v_mfma_f32_16x16x32_bf16 v[164:167], v[56:59], a[44:47], v[164:167]\n"
    "s_waitcnt lgkmcnt(5)\n"
    "ds_read_b64_tr_b16 v[50:51], v6 offset:30720\n"
    "v_pk_max_i16 v71, v71, 0\n"
    "v_mfma_f32_16x16x32_bf16 v[96:99], v[32:35], a[48:51], v[96:99]\n"
    "s_nop 0\n"
    "ds_read_b64_tr_b16 v[52:53], v7 offset:28672\n"
    "ds_read_b64_tr_b16 v[54:55], v7 offset:30720\n"
    "v_mfma_f32_16x16x32_bf16 v[100:103], v[32:35], a[52:55], v[100:103]\n"
    "buffer_store_dwordx4 v[68:71], v20, s[16:19], s86 offen offset:1024 sc1\n"
    "v_mfma_f32_16x16x32_bf16 v[152:155], v[36:39], a[48:51], v[152:155]\n"
    "ds_read_b64_tr_b16 v[56:57], v6 offset:28928\n"
    "v_mul_f32_e64 v72, v72, |v72|\n"
    "v_mul_f32_e64 v73, v73, |v73|\n"
    "v_mfma_f32_16x16x32_bf16 v[156:159], v[36:39], a[52:55], v[156:159]\n"
    "s_waitcnt vmcnt(14) lgkmcnt(5)\n"
    "ds_read_b64_tr_b16 v[58:59], v6 offset:30976\n"
    "v_mul_f32_e64 v74, v74, |v74|\n"
    "v_mfma_f32_16x16x32_bf16 v[104:107], v[40:43], a[48:51], v[104:107]\n"
    "s_barrier\n"
    "ds_read_b64_tr_b16 v[32:33], v7 offset:28928\n"
    "v_mul_f32_e64 v75, v75, |v75|\n"
    "v_mfma_f32_16x16x32_bf16 v[108:111], v[40:43], a[52:55], v[108:111]\n"
    "s_add_u32 s84, s6, 0\n"
    "ds_read_b64_tr_b16 v[34:35], v7 offset:30976\n"
    "v_mul_f32_e64 v128, v128, |v128|\n"
    "v_mfma_f32_16x16x32_bf16 v[160:163], v[44:47], a[48:51], v[160:163]\n"
    "s_add_u32 m0, s36, 98304\n"
    "ds_read_b64_tr_b16 v[36:37], v6 offset:32768\n"
    "v_mul_f32_e64 v129, v129, |v129|\n"
    "v_mfma_f32_16x16x32_bf16 v[164:167], v[44:47], a[52:55], v[164:167]\n"
    "s_waitcnt lgkmcnt(5)\n"
    "ds_read_b64_tr_b16 v[38:39], v6 offset:34816\n"
    "ds_read_b64_tr_b16 v[40:41], v7 offset:32768\n"
    "v_mfma_f32_16x16x32_bf16 v[96:99], v[48:51], a[56:59], v[96:99]\n"
    "buffer_load_dwordx4 v15, s[8:11], s84 offen lds\n"
    "v_mfma_f32_16x16x32_bf16 v[100:103], v[48:51], a[60:63], v[100:103]\n"
    "s_add_u32 s84, s6, 229376\n"
    "ds_read_b64_tr_b16 v[42:43], v7 offset:34816\n"
    "v_mul_f32_e64 v130, v130, |v130|\n"
    "v_mfma_f32_16x16x32_bf16 v[152:155], v[52:55], a[56:59], v[152:155]\n"
    "s_add_u32 m0, s36, 102400\n"
    "ds_read_b64_tr_b16 v[44:45], v6 offset:33024\n"
    "v_mul_f32_e64 v131, v131, |v131|\n"
    "v_mfma_f32_16x16x32_bf16 v[156:159], v[52:55], a[60:63], v[156:159]\n"
    "s_waitcnt lgkmcnt(5)\n"
    "ds_read_b64_tr_b16 v[46:47], v6 offset:35072\n"
    "ds_read_b64_tr_b16 v[48:49], v7 offset:33024\n"
    "v_mfma_f32_16x16x32_bf16 v[104:107], v[56:59], a[56:59], v[104:107]\n"
    "s_nop 0\n"
    "buffer_load_dwordx4 v15, s[8:11], s84 offen lds\n"
    "v_mfma_f32_16x16x32_bf16 v[108:111], v[56:59], a[60:63], v[108:111]\n"
    "ds_read_b64_tr_b16 v[50:51], v7 offset:35072\n"
    "v_cvt_pk_bf16_f32 v72, v72, v73\n"
    "v_mfma_f32_16x16x32_bf16 v[160:163], v[32:35], a[56:59], v[160:163]\n"
    "ds_read_b64_tr_b16 v[52:53], v6 offset:36864\n"
    "v_cvt_pk_bf16_f32 v73, v74, v75\n"
    "v_mfma_f32_16x16x32_bf16 v[164:167], v[32:35], a[60:63], v[164:167]\n"
    "s_waitcnt lgkmcnt(5)\n"
    "ds_read_b64_tr_b16 v[54:55], v6 offset:38912\n"
    "v_cvt_pk_bf16_f32 v74, v128, v129\n"
    "v_mfma_f32_16x16x32_bf16 v[96:99], v[36:39], a[64:67], v[96:99]\n"
    "s_nop 0\n"
    "ds_read_b64_tr_b16 v[56:57], v7 offset:36864\n"
    "ds_read_b64_tr_b16 v[58:59], v7 offset:38912\n"
    "v_mfma_f32_16x16x32_bf16 v[100:103], v[36:39], a[68:71], v[100:103]\n"
    "buffer_load_dwordx2 v[28:29], v28, s[32:35], s46 offen\n"
    "v_mfma_f32_16x16x32_bf16 v[152:155], v[40:43], a[64:67], v[152:155]\n"
    "s_add_u32 s84, s6, 458752\n"
    "ds_read_b64_tr_b16 v[32:33], v6 offset:37120\n"
    "v_cvt_pk_bf16_f32 v75, v130, v131\n"
    "v_mfma_f32_16x16x32_bf16 v[156:159], v[40:43], a[68:71], v[156:159]\n"
    "s_waitcnt lgkmcnt(5)\n"
    "ds_read_b64_tr_b16 v[34:35], v6 offset:39168\n"
    "v_pk_max_i16 v72, v72, 0\n"
    "v_mfma_f32_16x16x32_bf16 v[104:107], v[44:47], a[64:67], v[104:107]\n"
    "s_add_u32 m0, s36, 106496\n"
    "ds_read_b64_tr_b16 v[36:37], v7 offset:37120\n"
    "ds_read_b64_tr_b16 v[38:39], v7 offset:39168\n"
    "v_mfma_f32_16x16x32_bf16 v[108:111], v[44:47], a[68:71], v[108:111]\n"
    "s_nop 0\n"
    "buffer_load_dwordx4 v15, s[8:11], s84 offen lds\n"
    "v_mfma_f32_16x16x32_bf16 v[160:163], v[48:51], a[64:67], v[160:163]\n"
    "ds_read_b64_tr_b16 v[40:41], v6 offset:40960\n"
    "v_pk_max_i16 v73, v73, 0\n"
    "v_mfma_f32_16x16x32_bf16 v[164:167], v[48:51], a[68:71], v[164:167]\n"
    "s_waitcnt lgkmcnt(5)\n"
    "ds_read_b64_tr_b16 v[42:43], v6 offset:43008\n"
    "v_pk_max_i16 v74, v74, 0\n"
    "v_mfma_f32_16x16x32_bf16 v[96:99], v[52:55], a[72:75], v[96:99]\n"
    "s_nop 0\n"
    "ds_read_b64_tr_b16 v[44:45], v7 offset:40960\n"
    "v_pk_max_i16 v75, v75, 0\n"
    "v_mfma_f32_16x16x32_bf16 v[100:103], v[52:55], a[76:79], v[100:103]\n"
    "s_add_u32 s86, s68, 8192\n"
    "ds_read_b64_tr_b16 v[46:47], v7 offset:43008\n"
    "ds_read_b64_tr_b16 v[48:49], v6 offset:41216\n"
    "v_mfma_f32_16x16x32_bf16 v[152:155], v[56:59], a[72:75], v[152:155]\n"
    "buffer_store_dwordx4 v[72:75], v20, s[16:19], s86 offen offset:0 sc1\n"
    "v_mfma_f32_16x16x32_bf16 v[156:159], v[56:59], a[76:79], v[156:159]\n"
    "s_waitcnt lgkmcnt(5)\n"
    "ds_read_b64_tr_b16 v[50:51], v6 offset:43264\n"
    "v_mul_f32_e64 v76, v76, |v76|\n"
    "v_mul_f32_e64 v77, v77, |v77|\n"
    "v_mfma_f32_16x16x32_bf16 v[104:107], v[32:35], a[72:75], v[104:107]\n"
    "s_nop 0\n"
    "ds_read_b64_tr_b16 v[52:53], v7 offset:41216\n"
    "v_mul_f32_e64 v78, v78, |v78|\n"
    "v_mfma_f32_16x16x32_bf16 v[108:111], v[32:35], a[76:79], v[108:111]\n"
    "ds_read_b64_tr_b16 v[54:55], v7 offset:43264\n"
    "v_mul_f32_e64 v79, v79, |v79|\n"
    "v_mfma_f32_16x16x32_bf16 v[160:163], v[36:39], a[72:75], v[160:163]\n"
    "s_add_u32 s84, s6, 688128\n"
    "ds_read_b64_tr_b16 v[56:57], v6 offset:45056\n"
    "v_mul_f32_e64 v132, v132, |v132|\n"
    "v_mfma_f32_16x16x32_bf16 v[164:167], v[36:39], a[76:79], v[164:167]\n"
    "s_waitcnt lgkmcnt(5)\n"
    "ds_read_b64_tr_b16 v[58:59], v6 offset:47104\n"
    "v_mul_f32_e64 v133, v133, |v133|\n"
    "v_mul_f32_e64 v134, v134, |v134|\n"
    "v_mfma_f32_16x16x32_bf16 v[96:99], v[40:43], a[80:83], v[96:99]\n"
    "s_add_u32 m0, s36, 110592\n"
    "ds_read_b64_tr_b16 v[32:33], v7 offset:45056\n"
    "ds_read_b64_tr_b16 v[34:35], v7 offset:47104\n"
    "v_mfma_f32_16x16x32_bf16 v[100:103], v[40:43], a[84:87], v[100:103]\n"
    "s_nop 0\n"
    "buffer_load_dwordx4 v15, s[8:11], s84 offen lds\n"
    "v_mfma_f32_16x16x32_bf16 v[152:155], v[44:47], a[80:83], v[152:155]\n"
    "s_add_u32 m0, s36, 114688\n"
    "ds_read_b64_tr_b16 v[36:37], v6 offset:45312\n"
    "v_mul_f32_e64 v135, v135, |v135|\n"
    "v_mfma_f32_16x16x32_bf16 v[156:159], v[44:47], a[84:87], v[156:159]\n"
    "s_waitcnt vmcnt(16) lgkmcnt(5)\n"
    "ds_read_b64_tr_b16 v[38:39], v6 offset:47360\n"
    "v_cvt_pk_bf16_f32 v76, v76, v77\n"
    "v_mfma_f32_16x16x32_bf16 v[104:107], v[48:51], a[80:83], v[104:107]\n"
    "s_barrier\n"
    "ds_read_b64_tr_b16 v[40:41], v7 offset:45312\n"
    "ds_read_b64_tr_b16 v[42:43], v7 offset:47360\n"
    "v_cvt_pk_bf16_f32 v77, v78, v79\n"
    "v_mfma_f32_16x16x32_bf16 v[108:111], v[48:51], a[84:87], v[108:111]\n"
    "s_add_u32 s84, s7, 0\n"
    "buffer_load_dwordx4 v15, s[8:11], s84 offen lds\n"
    "v_mfma_f32_16x16x32_bf16 v[160:163], v[52:55], a[80:83], v[160:163]\n"
    "s_nop 0\n"
    "ds_read_b64_tr_b16 v[44:45], v6 offset:49152\n"
    "v_cvt_pk_bf16_f32 v78, v132, v133\n"
    "v_mfma_f32_16x16x32_bf16 v[164:167], v[52:55], a[84:87], v[164:167]\n"
    "s_waitcnt lgkmcnt(5)\n"
    "ds_read_b64_tr_b16 v[46:47], v6 offset:51200\n"
    "v_cvt_pk_bf16_f32 v79, v134, v135\n"
    "v_mfma_f32_16x16x32_bf16 v[96:99], v[56:59], a[88:91], v[96:99]\n"
    "s_add_u32 s84, s7, 229376\n"
    "ds_read_b64_tr_b16 v[48:49], v7 offset:49152\n"
    "v_pk_max_i16 v76, v76, 0\n"
    "v_mfma_f32_16x16x32_bf16 v[100:103], v[56:59], a[92:95], v[100:103]\n"
    "s_add_u32 m0, s36, 118784\n"
    "ds_read_b64_tr_b16 v[50:51], v7 offset:51200\n"
    "ds_read_b64_tr_b16 v[52:53], v6 offset:49408\n"
    "v_pk_max_i16 v77, v77, 0\n"
    "v_mfma_f32_16x16x32_bf16 v[152:155], v[32:35], a[88:91], v[152:155]\n"
    "s_nop 0\n"
    "buffer_load_dwordx4 v15, s[8:11], s84 offen lds\n"
    "v_mfma_f32_16x16x32_bf16 v[156:159], v[32:35], a[92:95], v[156:159]\n"
    "s_waitcnt lgkmcnt(5)\n"
    "ds_read_b64_tr_b16 v[54:55], v6 offset:51456\n"
    "v_pk_max_i16 v78, v78, 0\n"
    "v_mfma_f32_16x16x32_bf16 v[104:107], v[36:39], a[88:91], v[104:107]\n"
    "s_nop 0\n"
    "ds_read_b64_tr_b16 v[56:57], v7 offset:49408\n"
    "v_pk_max_i16 v79, v79, 0\n"
    "v_mfma_f32_16x16x32_bf16 v[108:111], v[36:39], a[92:95], v[108:111]\n"
    "ds_read_b64_tr_b16 v[58:59], v7 offset:51456\n"
    "ds_read_b64_tr_b16 v[32:33], v6 offset:53248\n"
    "v_mfma_f32_16x16x32_bf16 v[160:163], v[40:43], a[88:91], v[160:163]\n"
    "buffer_store_dwordx4 v[76:79], v20, s[16:19], s86 offen offset:1024 sc1\n"
    "v_mfma_f32_16x16x32_bf16 v[164:167], v[40:43], a[92:95], v[164:167]\n"
    "s_waitcnt lgkmcnt(5)\n"
    "ds_read_b64_tr_b16 v[34:35], v6 offset:55296\n"
    "v_mul_f32_e64 v80, v80, |v80|\n"
    "v_mul_f32_e64 v81, v81, |v81|\n"
    "v_mfma_f32_16x16x32_bf16 v[96:99], v[44:47], a[96:99], v[96:99]\n"
    "s_nop 0\n"
    "ds_read_b64_tr_b16 v[36:37], v7 offset:53248\n"
    "v_mul_f32_e64 v82, v82, |v82|\n"
    "v_mfma_f32_16x16x32_bf16 v[100:103], v[44:47], a[100:103], v[100:103]\n"
    "ds_read_b64_tr_b16 v[38:39], v7 offset:55296\n"
    "v_mul_f32_e64 v83, v83, |v83|\n"
    "v_mfma_f32_16x16x32_bf16 v[152:155], v[48:51], a[96:99], v[152:155]\n"
    "s_add_u32 s84, s7, 458752\n"
    "ds_read_b64_tr_b16 v[40:41], v6 offset:53504\n"
    "v_mul_f32_e64 v136, v136, |v136|\n"
    "v_mfma_f32_16x16x32_bf16 v[156:159], v[48:51], a[100:103], v[156:159]\n"
    "s_waitcnt lgkmcnt(5)\n"
    "ds_read_b64_tr_b16 v[42:43], v6 offset:55552\n"
    "v_mul_f32_e64 v137, v137, |v137|\n"
    "v_mfma_f32_16x16x32_bf16 v[104:107], v[52:55], a[96:99], v[104:107]\n"
    "s_add_u32 m0, s36, 122880\n"
    "ds_read_b64_tr_b16 v[44:45], v7 offset:53504\n"
    "ds_read_b64_tr_b16 v[46:47], v7 offset:55552\n"
    "v_mul_f32_e64 v138, v138, |v138|\n"
    "v_mfma_f32_16x16x32_bf16 v[108:111], v[52:55], a[100:103], v[108:111]\n"
    "s_nop 0\n"
    "buffer_load_dwordx4 v15, s[8:11], s84 offen lds\n"
    "v_mfma_f32_16x16x32_bf16 v[160:163], v[56:59], a[96:99], v[160:163]\n"
    "ds_read_b64_tr_b16 v[48:49], v6 offset:57344\n"
    "v_mul_f32_e64 v139, v139, |v139|\n"
    "v_mfma_f32_16x16x32_bf16 v[164:167], v[56:59], a[100:103], v[164:167]\n"
    "s_waitcnt lgkmcnt(5)\n"
    "ds_read_b64_tr_b16 v[50:51], v6 offset:59392\n"
    "v_cvt_pk_bf16_f32 v80, v80, v81\n"
    "v_mfma_f32_16x16x32_bf16 v[96:99], v[32:35], a[104:107], v[96:99]\n"
    "s_add_u32 s84, s7, 688128\n"
    "ds_read_b64_tr_b16 v[52:53], v7 offset:57344\n"
    "v_cvt_pk_bf16_f32 v81, v82, v83\n"
    "v_mfma_f32_16x16x32_bf16 v[100:103], v[32:35], a[108:111], v[100:103]\n"
    "s_add_u32 m0, s36, 126976\n"
    "ds_read_b64_tr_b16 v[54:55], v7 offset:59392\n"
    "ds_read_b64_tr_b16 v[56:57], v6 offset:57600\n"
    "v_mfma_f32_16x16x32_bf16 v[152:155], v[36:39], a[104:107], v[152:155]\n"
    "s_nop 0\n"
    "buffer_load_dwordx4 v15, s[8:11], s84 offen lds\n"
    "v_mfma_f32_16x16x32_bf16 v[156:159], v[36:39], a[108:111], v[156:159]\n"
    "s_waitcnt lgkmcnt(5)\n"
    "ds_read_b64_tr_b16 v[58:59], v6 offset:59648\n"
    "v_cvt_pk_bf16_f32 v82, v136, v137\n"
    "v_mfma_f32_16x16x32_bf16 v[104:107], v[40:43], a[104:107], v[104:107]\n"
    "s_add_u32 s84, s64, 0\n"
    "ds_read_b64_tr_b16 v[32:33], v7 offset:57600\n"
    "v_cvt_pk_bf16_f32 v83, v138, v139\n"
    "v_pk_max_i16 v80, v80, 0\n"
    "v_mfma_f32_16x16x32_bf16 v[108:111], v[40:43], a[108:111], v[108:111]\n"
    "s_add_u32 m0, s36, 0\n"
    "ds_read_b64_tr_b16 v[34:35], v7 offset:59648\n"
    "ds_read_b64_tr_b16 v[36:37], v6 offset:61440\n"
    "v_mfma_f32_16x16x32_bf16 v[160:163], v[44:47], a[104:107], v[160:163]\n"
    "s_nop 0\n"
    "buffer_load_dwordx4 v16, s[16:19], s84 offen sc1 lds\n"
    "v_mfma_f32_16x16x32_bf16 v[164:167], v[44:47], a[108:111], v[164:167]\n"
    "s_waitcnt lgkmcnt(5)\n"
    "ds_read_b64_tr_b16 v[38:39], v6 offset:63488\n"
    "v_pk_max_i16 v81, v81, 0\n"
    "v_mfma_f32_16x16x32_bf16 v[96:99], v[48:51], a[112:115], v[96:99]\n"
    "s_nop 0\n"
    "ds_read_b64_tr_b16 v[40:41], v7 offset:61440\n"
    "v_pk_max_i16 v82, v82, 0\n"
    "v_mfma_f32_16x16x32_bf16 v[100:103], v[48:51], a[116:119], v[100:103]\n"
    "ds_read_b64_tr_b16 v[42:43], v7 offset:63488\n"
    "v_pk_max_i16 v83, v83, 0\n"
    "v_mfma_f32_16x16x32_bf16 v[152:155], v[52:55], a[112:115], v[152:155]\n"
    "s_add_u32 s86, s68, 16384\n"
    "ds_read_b64_tr_b16 v[44:45], v6 offset:61696\n"
    "ds_read_b64_tr_b16 v[46:47], v6 offset:63744\n"
    "v_mfma_f32_16x16x32_bf16 v[156:159], v[52:55], a[116:119], v[156:159]\n"
    "s_waitcnt vmcnt(18) lgkmcnt(6)\n"
    "buffer_store_dwordx4 v[80:83], v20, s[16:19], s86 offen offset:0 sc1\n"
    "v_mfma_f32_16x16x32_bf16 v[104:107], v[56:59], a[112:115], v[104:107]\n"
    "s_barrier\n"
    "ds_read_b64_tr_b16 v[48:49], v7 offset:61696\n"
    "v_mul_f32_e64 v84, v84, |v84|\n"
    "v_mul_f32_e64 v85, v85, |v85|\n"
    "v_mfma_f32_16x16x32_bf16 v[108:111], v[56:59], a[116:119], v[108:111]\n"
    "ds_read_b64_tr_b16 v[50:51], v7 offset:63744\n"
    "v_mul_f32_e64 v86, v86, |v86|\n"
    "v_mfma_f32_16x16x32_bf16 v[160:163], v[32:35], a[112:115], v[160:163]\n"
    "ds_read_b64_tr_b16 v[52:53], v8 offset:0\n"
    "v_mul_f32_e64 v87, v87, |v87|\n"
    "v_mfma_f32_16x16x32_bf16 v[164:167], v[32:35], a[116:119], v[164:167]\n"
    "s_waitcnt lgkmcnt(5)\n"
    "ds_read_b64_tr_b16 v[54:55], v8 offset:2048\n"
    "v_mul_f32_e64 v140, v140, |v140|\n"
    "v_mfma_f32_16x16x32_bf16 v[96:99], v[36:39], a[120:123], v[96:99]\n"
    "s_add_u32 s84, s64, 4096\n"
    "ds_read_b64_tr_b16 v[56:57], v9 offset:0\n"
    "v_mul_f32_e64 v141, v141, |v141|\n"
    "v_mfma_f32_16x16x32_bf16 v[100:103], v[36:39], a[124:127], v[100:103]\n"
    "s_add_u32 m0, s36, 4096\n"
    "ds_read_b64_tr_b16 v[58:59], v9 offset:2048\n"
    "ds_read_b64_tr_b16 v[32:33], v8 offset:256\n"
    "v_mfma_f32_16x16x32_bf16 v[152:155], v[40:43], a[120:123], v[152:155]\n"
    "s_nop 0\n"
    "buffer_load_dwordx4 v16, s[16:19], s84 offen sc1 lds\n"
    "v_mfma_f32_16x16x32_bf16 v[156:159], v[40:43], a[124:127], v[156:159]\n"
    "s_waitcnt lgkmcnt(5)\n"
    "ds_read_b64_tr_b16 v[34:35], v8 offset:2304\n"
    "v_mul_f32_e64 v142, v142, |v142|\n"
    "v_mfma_f32_16x16x32_bf16 v[104:107], v[44:47], a[120:123], v[104:107]\n"
    "s_add_u32 s84, s65, 0\n"
    "ds_read_b64_tr_b16 v[36:37], v9 offset:256\n"
    "v_mul_f32_e64 v143, v143, |v143|\n"
    "v_mfma_f32_16x16x32_bf16 v[108:111], v[44:47], a[124:127], v[108:111]\n"
    "s_add_u32 m0, s36, 8192\n"
    "ds_read_b64_tr_b16 v[38:39], v9 offset:2304\n"
    "ds_read_b64_tr_b16 v[40:41], v8 offset:4096\n"
    "v_mfma_f32_16x16x32_bf16 v[160:163], v[48:51], a[120:123], v[160:163]\n"
    "buffer_load_dwordx4 v16, s[16:19], s84 offen sc1 lds\n"
    "v_mfma_f32_16x16x32_bf16 v[164:167], v[48:51], a[124:127], v[164:167]\n"
    "s_waitcnt lgkmcnt(5)\n"
    "ds_read_b64_tr_b16 v[42:43], v8 offset:6144\n"
    "v_cvt_pk_bf16_f32 v84, v84, v85\n"
    "v_mfma_f32_16x16x32_bf16 v[96:99], v[52:55], a[128:131], v[96:99]\n"
    "s_nop 0\n"
    "ds_read_b64_tr_b16 v[44:45], v9 offset:4096\n"
    "v_cvt_pk_bf16_f32 v85, v86, v87\n"
    "v_mfma_f32_16x16x32_bf16 v[100:103], v[52:55], a[132:135], v[100:103]\n"
    "s_add_u32 s84, s65, 4096\n"
    "ds_read_b64_tr_b16 v[46:47], v9 offset:6144\n"
    "v_cvt_pk_bf16_f32 v86, v140, v141\n"
    "v_mfma_f32_16x16x32_bf16 v[152:155], v[56:59], a[128:131], v[152:155]\n"
    "s_add_u32 m0, s36, 12288\n"
    "ds_read_b64_tr_b16 v[48:49], v8 offset:4352\n"
    "ds_read_b64_tr_b16 v[50:51], v8 offset:6400\n"
    "v_mfma_f32_16x16x32_bf16 v[156:159], v[56:59], a[132:135], v[156:159]\n"
    "s_waitcnt lgkmcnt(6)\n"
    "buffer_load_dwordx4 v16, s[16:19], s84 offen sc1 lds\n"
    "v_mfma_f32_16x16x32_bf16 v[104:107], v[32:35], a[128:131], v[104:107]\n"
    "s_nop 0\n"
    "ds_read_b64_tr_b16 v[52:53], v9 offset:4352\n"
    "v_cvt_pk_bf16_f32 v87, v142, v143\n"
    "v_mfma_f32_16x16x32_bf16 v[108:111], v[32:35], a[132:135], v[108:111]\n"
    "s_add_u32 s84, s66, 0\n"
    "ds_read_b64_tr_b16 v[54:55], v9 offset:6400\n"
    "v_pk_max_i16 v84, v84, 0\n"
    "v_mfma_f32_16x16x32_bf16 v[160:163], v[36:39], a[128:131], v[160:163]\n"
    "s_add_u32 m0, s36, 16384\n"
    "ds_read_b64_tr_b16 v[56:57], v8 offset:8192\n"
    "ds_read_b64_tr_b16 v[58:59], v8 offset:10240\n"
    "v_mfma_f32_16x16x32_bf16 v[164:167], v[36:39], a[132:135], v[164:167]\n"
    "s_waitcnt lgkmcnt(6)\n"
    "buffer_load_dwordx4 v16, s[16:19], s84 offen sc1 lds\n"
    "v_mfma_f32_16x16x32_bf16 v[96:99], v[40:43], a[136:139], v[96:99]\n"
    "ds_read_b64_tr_b16 v[32:33], v9 offset:8192\n"
    "v_pk_max_i16 v85, v85, 0\n"
    "v_mfma_f32_16x16x32_bf16 v[100:103], v[40:43], a[140:143], v[100:103]\n"
    "ds_read_b64_tr_b16 v[34:35], v9 offset:10240\n"
    "v_pk_max_i16 v86, v86, 0\n"
    "v_mfma_f32_16x16x32_bf16 v[152:155], v[44:47], a[136:139], v[152:155]\n"
    "ds_read_b64_tr_b16 v[36:37], v8 offset:8448\n"
    "v_pk_max_i16 v87, v87, 0\n"
    "v_mfma_f32_16x16x32_bf16 v[156:159], v[44:47], a[140:143], v[156:159]\n"
    "s_waitcnt lgkmcnt(5)\n"
    "ds_read_b64_tr_b16 v[38:39], v8 offset:10496\n"
    "ds_read_b64_tr_b16 v[40:41], v9 offset:8448\n"
    "v_mfma_f32_16x16x32_bf16 v[104:107], v[48:51], a[136:139], v[104:107]\n"
    "s_nop 0\n"
    "buffer_store_dwordx4 v[84:87], v20, s[16:19], s86 offen offset:1024 sc1\n"
    "v_mfma_f32_16x16x32_bf16 v[108:111], v[48:51], a[140:143], v[108:111]\n"
    "ds_read_b64_tr_b16 v[42:43], v9 offset:10496\n"
    "v_mul_f32_e64 v88, v88, |v88|\n"
    "v_mul_f32_e64 v89, v89, |v89|\n"
    "v_mfma_f32_16x16x32_bf16 v[160:163], v[52:55], a[136:139], v[160:163]\n"
    "ds_read_b64_tr_b16 v[44:45], v8 offset:12288\n"
    "v_mul_f32_e64 v90, v90, |v90|\n"
    "v_mfma_f32_16x16x32_bf16 v[164:167], v[52:55], a[140:143], v[164:167]\n"
    "s_waitcnt lgkmcnt(5)\n"
    "ds_read_b64_tr_b16 v[46:47], v8 offset:14336\n"
    "v_mul_f32_e64 v91, v91, |v91|\n"
    "v_mfma_f32_16x16x32_bf16 v[96:99], v[56:59], a[144:147], v[96:99]\n"
    "s_nop 0\n"
    "ds_read_b64_tr_b16 v[48:49], v9 offset:12288\n"
    "v_mul_f32_e64 v144, v144, |v144|\n"
    "v_mfma_f32_16x16x32_bf16 v[100:103], v[56:59], a[148:151], v[100:103]\n"
    "s_add_u32 s84, s66, 4096\n"
    "ds_read_b64_tr_b16 v[50:51], v9 offset:14336\n"
    "v_mul_f32_e64 v145, v145, |v145|\n"
    "v_mul_f32_e64 v146, v146, |v146|\n"
    "v_mfma_f32_16x16x32_bf16 v[152:155], v[32:35], a[144:147], v[152:155]\n"
    "s_add_u32 m0, s36, 20480\n"
    "ds_read_b64_tr_b16 v[52:53], v8 offset:12544\n"
    "ds_read_b64_tr_b16 v[54:55], v8 offset:14592\n"
    "v_mfma_f32_16x16x32_bf16 v[156:159], v[32:35], a[148:151], v[156:159]\n"
    "s_waitcnt vmcnt(19) lgkmcnt(6)\n"
    "buffer_load_dwordx4 v16, s[16:19], s84 offen sc1 lds\n"
    "v_mfma_f32_16x16x32_bf16 v[104:107], v[36:39], a[144:147], v[104:107]\n"
    "s_barrier\n"
    "ds_read_b64_tr_b16 v[56:57], v9 offset:12544\n"
    "v_mul_f32_e64 v147, v147, |v147|\n"
    "v_mfma_f32_16x16x32_bf16 v[108:111], v[36:39], a[148:151], v[108:111]\n"
    "s_add_u32 s84, s67, 0\n"
    "ds_read_b64_tr_b16 v[58:59], v9 offset:14592\n"
    "v_cvt_pk_bf16_f32 v88, v88, v89\n"
    "v_mfma_f32_16x16x32_bf16 v[160:163], v[40:43], a[144:147], v[160:163]\n"
    "s_add_u32 m0, s36, 24576\n"
    "ds_read_b64_tr_b16 v[32:33], v8 offset:16384\n"
    "ds_read_b64_tr_b16 v[34:35], v8 offset:18432\n"
    "v_cvt_pk_bf16_f32 v89, v90, v91\n"
    "v_mfma_f32_16x16x32_bf16 v[164:167], v[40:43], a[148:151], v[164:167]\n"
    "s_waitcnt lgkmcnt(6)\n"
    "buffer_load_dwordx4 v16, s[16:19], s84 offen sc1 lds\n"
    "v_mfma_f32_16x16x32_bf16 v[96:99], v[44:47], a[152:155], v[96:99]\n"
    "LPAD5_%=:\n"
    "s_cbranch_execz LPADR_%=\n"
    "ds_read_b64_tr_b16 v[36:37], v9 offset:16384\n"
    "v_cvt_pk_bf16_f32 v90, v144, v145\n"
    "v_mfma_f32_16x16x32_bf16 v[100:103], v[44:47], a[156:159], v[100:103]\n"
    "s_add_u32 s84, s67, 4096\n"
    "ds_read_b64_tr_b16 v[38:39], v9 offset:18432\n"
    "v_cvt_pk_bf16_f32 v91, v146, v147\n"
    "v_mfma_f32_16x16x32_bf16 v[152:155], v[48:51], a[152:155], v[152:155]\n"
    "s_add_u32 m0, s36, 28672\n"
    "ds_read_b64_tr_b16 v[40:41], v8 offset:16640\n"
    "v_pk_max_i16 v88, v88, 0\n"
    "v_mfma_f32_16x16x32_bf16 v[156:159], v[48:51], a[156:159], v[156:159]\n"
    "s_waitcnt lgkmcnt(5)\n"
    "ds_read_b64_tr_b16 v[42:43], v8 offset:18688\n"
    "ds_read_b64_tr_b16 v[44:45], v9 offset:16640\n"
    "v_pk_max_i16 v89, v89, 0\n"
    "v_mfma_f32_16x16x32_bf16 v[104:107], v[52:55], a[152:155], v[104:107]\n"
    "buffer_load_dwordx4 v16, s[16:19], s84 offen sc1 lds\n"
    "v_mfma_f32_16x16x32_bf16 v[108:111], v[52:55], a[156:159], v[108:111]\n"
    "ds_read_b64_tr_b16 v[46:47], v9 offset:18688\n"
    "v_pk_max_i16 v90, v90, 0\n"
    "v_mfma_f32_16x16x32_bf16 v[160:163], v[56:59], a[152:155], v[160:163]\n"
    "s_add_u32 s86, s68, 24576\n"
    "ds_read_b64_tr_b16 v[48:49], v8 offset:20480\n"
    "v_pk_max_i16 v91, v91, 0\n"
    "v_mfma_f32_16x16x32_bf16 v[164:167], v[56:59], a[156:159], v[164:167]\n"
    "s_waitcnt lgkmcnt(5)\n"
    "ds_read_b64_tr_b16 v[50:51], v8 offset:22528\n"
    "ds_read_b64_tr_b16 v[52:53], v9 offset:20480\n"
    "v_mfma_f32_16x16x32_bf16 v[96:99], v[32:35], a[160:163], v[96:99]\n"
    "s_nop 0\n"
    "buffer_store_dwordx4 v[88:91], v20, s[16:19], s86 offen offset:0 sc1\n"
    "v_mfma_f32_16x16x32_bf16 v[100:103], v[32:35], a[164:167], v[100:103]\n"
    "ds_read_b64_tr_b16 v[54:55], v9 offset:22528\n"
    "v_mul_f32_e64 v92, v92, |v92|\n"
    "v_mul_f32_e64 v93, v93, |v93|\n"
    "v_mfma_f32_16x16x32_bf16 v[152:155], v[36:39], a[160:163], v[152:155]\n"
    "ds_read_b64_tr_b16 v[56:57], v8 offset:20736\n"
    "v_mul_f32_e64 v94, v94, |v94|\n"
    "v_mfma_f32_16x16x32_bf16 v[156:159], v[36:39], a[164:167], v[156:159]\n"
    "s_waitcnt lgkmcnt(5)\n"
    "ds_read_b64_tr_b16 v[58:59], v8 offset:22784\n"
    "v_mul_f32_e64 v95, v95, |v95|\n"
    "v_mfma_f32_16x16x32_bf16 v[104:107], v[40:43], a[160:163], v[104:107]\n"
    "s_nop 0\n"
    "ds_read_b64_tr_b16 v[32:33], v9 offset:20736\n"
    "v_mul_f32_e64 v148, v148, |v148|\n"
    "v_mfma_f32_16x16x32_bf16 v[108:111], v[40:43], a[164:167], v[108:111]\n"
    "s_add_u32 s84, s80, 0\n"
    "ds_read_b64_tr_b16 v[34:35], v9 offset:22784\n"
    "v_mul_f32_e64 v149, v149, |v149|\n"
    "v_mfma_f32_16x16x32_bf16 v[160:163], v[44:47], a[160:163], v[160:163]\n"
    "s_add_u32 m0, s36, 32768\n"
    "ds_read_b64_tr_b16 v[36:37], v8 offset:24576\n"
    "ds_read_b64_tr_b16 v[38:39], v8 offset:26624\n"
    "v_mul_f32_e64 v150, v150, |v150|\n"
    "v_mfma_f32_16x16x32_bf16 v[164:167], v[44:47], a[164:167], v[164:167]\n"
    "s_waitcnt lgkmcnt(6)\n"
    "buffer_load_dwordx4 v16, s[16:19], s84 offen sc1 lds\n"
    "v_mfma_f32_16x16x32_bf16 v[96:99], v[48:51], a[168:171], v[96:99]\n"
    "ds_read_b64_tr_b16 v[40:41], v9 offset:24576\n"
    "v_mul_f32_e64 v151, v151, |v151|\n"
    "v_mfma_f32_16x16x32_bf16 v[100:103], v[48:51], a[172:175], v[100:103]\n"
    "s_add_u32 s84, s80, 4096\n"
    "ds_read_b64_tr_b16 v[42:43], v9 offset:26624\n"
    "v_cvt_pk_bf16_f32 v92, v92, v93\n"
    "v_mfma_f32_16x16x32_bf16 v[152:155], v[52:55], a[168:171], v[152:155]\n"
    "s_add_u32 m0, s36, 36864\n"
    "ds_read_b64_tr_b16 v[44:45], v8 offset:24832\n"
    "v_cvt_pk_bf16_f32 v93, v94, v95\n"
    "v_mfma_f32_16x16x32_bf16 v[156:159], v[52:55], a[172:175], v[156:159]\n"
    "s_waitcnt lgkmcnt(5)\n"
    "ds_read_b64_tr_b16 v[46:47], v8 offset:26880\n"
    "ds_read_b64_tr_b16 v[48:49], v9 offset:24832\n"
    "v_mfma_f32_16x16x32_bf16 v[104:107], v[56:59], a[168:171], v[104:107]\n"
    "s_nop 0\n"
    "buffer_load_dwordx4 v16, s[16:19], s84 offen sc1 lds\n"
    "v_mfma_f32_16x16x32_bf16 v[108:111], v[56:59], a[172:175], v[108:111]\n"
    "s_add_u32 s84, s81, 0\n"
    "ds_read_b64_tr_b16 v[50:51], v9 offset:26880\n"
    "v_cvt_pk_bf16_f32 v94, v148, v149\n"
    "v_mfma_f32_16x16x32_bf16 v[160:163], v[32:35], a[168:171], v[160:163]\n"
    "s_add_u32 m0, s36, 40960\n"
    "ds_read_b64_tr_b16 v[52:53], v8 offset:28672\n"
    "v_cvt_pk_bf16_f32 v95, v150, v151\n"
    "v_pk_max_i16 v92, v92, 0\n"
    "v_mfma_f32_16x16x32_bf16 v[164:167], v[32:35], a[172:175], v[164:167]\n"
    "s_waitcnt lgkmcnt(5)\n"
    "ds_read_b64_tr_b16 v[54:55], v8 offset:30720\n"
    "ds_read_b64_tr_b16 v[56:57], v9 offset:28672\n"
    "v_mfma_f32_16x16x32_bf16 v[96:99], v[36:39], a[176:179], v[96:99]\n"
    "buffer_load_dwordx4 v16, s[16:19], s84 offen sc1 lds\n"
    "v_mfma_f32_16x16x32_bf16 v[100:103], v[36:39], a[180:183], v[100:103]\n"
    "ds_read_b64_tr_b16 v[58:59], v9 offset:30720\n"
    "v_pk_max_i16 v93, v93, 0\n"
    "v_mfma_f32_16x16x32_bf16 v[152:155], v[40:43], a[176:179], v[152:155]\n"
    "ds_read_b64_tr_b16 v[32:33], v8 offset:28928\n"
    "v_pk_max_i16 v94, v94, 0\n"
    "v_mfma_f32_16x16x32_bf16 v[156:159], v[40:43], a[180:183], v[156:159]\n"
    "s_waitcnt vmcnt(19) lgkmcnt(5)\n"
    "ds_read_b64_tr_b16 v[34:35], v8 offset:30976\n"
    "v_pk_max_i16 v95, v95, 0\n"
    "v_mfma_f32_16x16x32_bf16 v[104:107], v[44:47], a[176:179], v[104:107]\n"
    "s_barrier\n"
    "ds_read_b64_tr_b16 v[36:37], v9 offset:28928\n"
    "ds_read_b64_tr_b16 v[38:39], v9 offset:30976\n"
    "v_mfma_f32_16x16x32_bf16 v[108:111], v[44:47], a[180:183], v[108:111]\n"
    "buffer_store_dwordx4 v[92:95], v20, s[16:19], s86 offen offset:1024 sc1\n"
    "v_mfma_f32_16x16x32_bf16 v[160:163], v[48:51], a[176:179], v[160:163]\n"
    "ds_read_b64_tr_b16 v[40:41], v8 offset:32768\n"
    "v_mfma_f32_16x16x32_bf16 v[164:167], v[48:51], a[180:183], v[164:167]\n"
    "s_waitcnt lgkmcnt(5)\n"
    "ds_read_b64_tr_b16 v[42:43], v8 offset:34816\n"
    "ds_read_b128 v[208:211], v218 offset:10240\n"
    "v_mfma_f32_16x16x32_bf16 v[96:99], v[52:55], a[184:187], v[96:99]\n"
    "s_nop 0\n"
    "ds_read_b64_tr_b16 v[44:45], v9 offset:32768\n"
    "ds_read_b128 v[212:215], v218 offset:11264\n"
    "v_mfma_f32_16x16x32_bf16 v[100:103], v[52:55], a[188:191], v[100:103]\n"
    "s_add_u32 s84, s81, 4096\n"
    "ds_read_b64_tr_b16 v[46:47], v9 offset:34816\n"
    "ds_read_b128 v[120:123], v218 offset:0\n"
    "v_mfma_f32_16x16x32_bf16 v[152:155], v[56:59], a[184:187], v[152:155]\n"
    "s_add_u32 m0, s36, 45056\n"
    "ds_read_b64_tr_b16 v[48:49], v8 offset:33024\n"
    "ds_read_b128 v[124:127], v218 offset:1024\n"
    "v_mfma_f32_16x16x32_bf16 v[156:159], v[56:59], a[188:191], v[156:159]\n"
    "s_waitcnt lgkmcnt(9)\n"
    "ds_read_b64_tr_b16 v[50:51], v8 offset:35072\n"
    "ds_read_b64_tr_b16 v[52:53], v9 offset:33024\n"
    "v_mfma_f32_16x16x32_bf16 v[104:107], v[32:35], a[184:187], v[104:107]\n"
    "s_nop 0\n"
    "buffer_load_dwordx4 v16, s[16:19], s84 offen sc1 lds\n"
    "v_mfma_f32_16x16x32_bf16 v[108:111], v[32:35], a[188:191], v[108:111]\n"
    "s_add_u32 s97, s92, 4194304\n"
    "ds_read_b64_tr_b16 v[54:55], v9 offset:35072\n"
    "ds_read_b128 v[128:131], v218 offset:2048\n"
    "v_mfma_f32_16x16x32_bf16 v[160:163], v[36:39], a[184:187], v[160:163]\n"
    "s_add_u32 m0, s36, 147456\n"
    "ds_read_b64_tr_b16 v[56:57], v8 offset:36864\n"
    "ds_read_b128 v[132:135], v218 offset:3072\n"
    "v_mfma_f32_16x16x32_bf16 v[164:167], v[36:39], a[188:191], v[164:167]\n"
    "s_waitcnt lgkmcnt(9)\n"
    "ds_read_b64_tr_b16 v[58:59], v8 offset:38912\n"
    "ds_read_b64_tr_b16 v[32:33], v9 offset:36864\n"
    "v_mfma_f32_16x16x32_bf16 v[96:99], v[40:43], a[192:195], v[96:99]\n"
    "s_nop 0\n"
    "buffer_load_dwordx4 v31, s[16:19], s97 offen sc1 lds\n"
    "v_mfma_f32_16x16x32_bf16 v[100:103], v[40:43], a[196:199], v[100:103]\n"
    "ds_read_b64_tr_b16 v[34:35], v9 offset:38912\n"
    "ds_read_b128 v[136:139], v218 offset:4096\n"
    "v_mfma_f32_16x16x32_bf16 v[152:155], v[44:47], a[192:195], v[152:155]\n"
    "ds_read_b64_tr_b16 v[36:37], v8 offset:37120\n"
    "ds_read_b128 v[140:143], v218 offset:5120\n"
    "v_mfma_f32_16x16x32_bf16 v[156:159], v[44:47], a[196:199], v[156:159]\n"
    "s_waitcnt lgkmcnt(9)\n"
    "ds_read_b64_tr_b16 v[38:39], v8 offset:39168\n"
    "ds_read_b128 v[144:147], v218 offset:6144\n"
    "v_mfma_f32_16x16x32_bf16 v[104:107], v[48:51], a[192:195], v[104:107]\n"
    "s_nop 0\n"
    "ds_read_b64_tr_b16 v[40:41], v9 offset:37120\n"
    "ds_read_b128 v[148:151], v218 offset:7168\n"
    "v_mfma_f32_16x16x32_bf16 v[108:111], v[48:51], a[196:199], v[108:111]\n"
    "s_add_u32 s97, s92, 4198400\n"
    "ds_read_b64_tr_b16 v[42:43], v9 offset:39168\n"
    "v_mfma_f32_16x16x32_bf16 v[160:163], v[52:55], a[192:195], v[160:163]\n"
    "s_add_u32 m0, s36, 151552\n"
    "ds_read_b64_tr_b16 v[44:45], v8 offset:40960\n"
    "ds_read_b64_tr_b16 v[46:47], v8 offset:43008\n"
    "v_mfma_f32_16x16x32_bf16 v[164:167], v[52:55], a[196:199], v[164:167]\n"
    "s_waitcnt lgkmcnt(10)\n"
    "buffer_load_dwordx4 v31, s[16:19], s97 offen sc1 lds\n"
    "v_mfma_f32_16x16x32_bf16 v[96:99], v[56:59], a[200:203], v[96:99]\n"
    "s_nop 0\n"
    "ds_read_b64_tr_b16 v[48:49], v9 offset:40960\n"
    "v_mfma_f32_16x16x32_bf16 v[100:103], v[56:59], a[204:207], v[100:103]\n"
    "ds_read_b64_tr_b16 v[50:51], v9 offset:43008\n"
    "v_mfma_f32_16x16x32_bf16 v[152:155], v[32:35], a[200:203], v[152:155]\n"
    "ds_read_b64_tr_b16 v[52:53], v8 offset:41216\n"
    "v_mfma_f32_16x16x32_bf16 v[156:159], v[32:35], a[204:207], v[156:159]\n"
    "s_waitcnt lgkmcnt(5)\n"
    "ds_read_b64_tr_b16 v[54:55], v8 offset:43264\n"
    "v_mfma_f32_16x16x32_bf16 v[104:107], v[36:39], a[200:203], v[104:107]\n"
    "s_add_u32 s97, s92, 4202496\n"
    "ds_read_b64_tr_b16 v[56:57], v9 offset:41216\n"
    "v_mfma_f32_16x16x32_bf16 v[108:111], v[36:39], a[204:207], v[108:111]\n"
    "s_add_u32 m0, s36, 155648\n"
    "ds_read_b64_tr_b16 v[58:59], v9 offset:43264\n"
    "ds_read_b64_tr_b16 v[32:33], v8 offset:45056\n"
    "v_mfma_f32_16x16x32_bf16 v[160:163], v[40:43], a[200:203], v[160:163]\n"
    "s_nop 0\n"
    "buffer_load_dwordx4 v31, s[16:19], s97 offen sc1 lds\n"
    "v_mfma_f32_16x16x32_bf16 v[164:167], v[40:43], a[204:207], v[164:167]\n"
    "s_waitcnt lgkmcnt(5)\n"
    "ds_read_b64_tr_b16 v[34:35], v8 offset:47104\n"
    "v_xor_b32 v208, 0x80008000, v208\n"
    "v_mfma_f32_16x16x32_bf16 v[96:99], v[44:47], a[208:211], v[96:99]\n"
    "s_nop 0\n"
    "ds_read_b64_tr_b16 v[36:37], v9 offset:45056\n"
    "v_xor_b32 v209, 0x80008000, v209\n"
    "v_mfma_f32_16x16x32_bf16 v[100:103], v[44:47], a[212:215], v[100:103]\n"
    "s_add_u32 s97, s92, 4206592\n"
    "ds_read_b64_tr_b16 v[38:39], v9 offset:47104\n"
    "v_xor_b32 v210, 0x80008000, v210\n"
    "v_mfma_f32_16x16x32_bf16 v[152:155], v[48:51], a[208:211], v[152:155]\n"
    "s_add_u32 m0, s36, 159744\n"
    "ds_read_b64_tr_b16 v[40:41], v8 offset:45312\n"
    "v_xor_b32 v211, 0x80008000, v211\n"
    "v_mfma_f32_16x16x32_bf16 v[156:159], v[48:51], a[212:215], v[156:159]\n"
    "s_waitcnt vmcnt(19) lgkmcnt(5)\n"
    "ds_read_b64_tr_b16 v[42:43], v8 offset:47360\n"
    "v_xor_b32 v212, 0x80008000, v212\n"
    "v_mfma_f32_16x16x32_bf16 v[104:107], v[52:55], a[208:211], v[104:107]\n"
    "s_barrier\n"
    "ds_read_b64_tr_b16 v[44:45], v9 offset:45312\n"
    "ds_read_b64_tr_b16 v[46:47], v9 offset:47360\n"
    "v_mfma_f32_16x16x32_bf16 v[108:111], v[52:55], a[212:215], v[108:111]\n"
    "buffer_load_dwordx4 v31, s[16:19], s97 offen sc1 lds\n"
    "v_mfma_f32_16x16x32_bf16 v[160:163], v[56:59], a[208:211], v[160:163]\n"
    "ds_read_b64_tr_b16 v[48:49], v8 offset:49152\n"
    "v_xor_b32 v213, 0x80008000, v213\n"
    "v_mfma_f32_16x16x32_bf16 v[164:167], v[56:59], a[212:215], v[164:167]\n"
    "s_waitcnt lgkmcnt(5)\n"
    "ds_read_b64_tr_b16 v[50:51], v8 offset:51200\n"
    "v_xor_b32 v214, 0x80008000, v214\n"
    "v_mfma_f32_16x16x32_bf16 v[96:99], v[32:35], a[216:219], v[96:99]\n"
    "s_nop 0\n"
    "ds_read_b64_tr_b16 v[52:53], v9 offset:49152\n"
    "v_xor_b32 v215, 0x80008000, v215\n"
    "v_mfma_f32_16x16x32_bf16 v[100:103], v[32:35], a[220:223], v[100:103]\n"
    "s_add_u32 s84, s82, 0\n"
    "ds_read_b64_tr_b16 v[54:55], v9 offset:51200\n"
    "v_mfma_f32_16x16x32_bf16 v[152:155], v[36:39], a[216:219], v[152:155]\n"
    "s_add_u32 m0, s36, 49152\n"
    "ds_read_b64_tr_b16 v[56:57], v8 offset:49408\n"
    "v_mfma_f32_16x16x32_bf16 v[156:159], v[36:39], a[220:223], v[156:159]\n"
    "s_waitcnt lgkmcnt(5)\n"
    "ds_read_b64_tr_b16 v[58:59], v8 offset:51456\n"
    "ds_read_b64_tr_b16 v[32:33], v9 offset:49408\n"
    "v_mfma_f32_16x16x32_bf16 v[104:107], v[40:43], a[216:219], v[104:107]\n"
    "buffer_load_dwordx4 v16, s[16:19], s84 offen sc1 lds\n"
    "v_mfma_f32_16x16x32_bf16 v[108:111], v[40:43], a[220:223], v[108:111]\n"
    "ds_read_b64_tr_b16 v[34:35], v9 offset:51456\n"
    "v_mfma_f32_16x16x32_bf16 v[160:163], v[44:47], a[216:219], v[160:163]\n"
    "s_add_u32 s84, s82, 4096\n"
    "ds_read_b64_tr_b16 v[36:37], v8 offset:53248\n"
    "v_mfma_f32_16x16x32_bf16 v[164:167], v[44:47], a[220:223], v[164:167]\n"
    "s_waitcnt lgkmcnt(5)\n"
    "ds_read_b64_tr_b16 v[38:39], v8 offset:55296\n"
    "v_mfma_f32_16x16x32_bf16 v[96:99], v[48:51], a[224:227], v[96:99]\n"
    "s_add_u32 m0, s36, 53248\n"
    "ds_read_b64_tr_b16 v[40:41], v9 offset:53248\n"
    "ds_read_b64_tr_b16 v[42:43], v9 offset:55296\n"
    "v_mfma_f32_16x16x32_bf16 v[100:103], v[48:51], a[228:231], v[100:103]\n"
    "s_nop 0\n"
    "buffer_load_dwordx4 v16, s[16:19], s84 offen sc1 lds\n"
    "v_mfma_f32_16x16x32_bf16 v[152:155], v[52:55], a[224:227], v[152:155]\n"
    "s_add_u32 s84, s37, 0\n"
    "ds_read_b64_tr_b16 v[44:45], v8 offset:53504\n"
    "v_mfma_f32_16x16x32_bf16 v[156:159], v[52:55], a[228:231], v[156:159]\n"
    "s_waitcnt lgkmcnt(5)\n"
    "ds_read_b64_tr_b16 v[46:47], v8 offset:55552\n"
    "v_mfma_f32_16x16x32_bf16 v[104:107], v[56:59], a[224:227], v[104:107]\n"
    "s_add_u32 m0, s36, 57344\n"
    "ds_read_b64_tr_b16 v[48:49], v9 offset:53504\n"
    "ds_read_b64_tr_b16 v[50:51], v9 offset:55552\n"
    "v_mfma_f32_16x16x32_bf16 v[108:111], v[56:59], a[228:231], v[108:111]\n"
    "buffer_load_dwordx4 v16, s[16:19], s84 offen sc1 lds\n"
    "v_mfma_f32_16x16x32_bf16 v[160:163], v[32:35], a[224:227], v[160:163]\n"
    "ds_read_b64_tr_b16 v[52:53], v8 offset:57344\n"
    "v_mfma_f32_16x16x32_bf16 v[164:167], v[32:35], a[228:231], v[164:167]\n"
    "s_waitcnt lgkmcnt(5)\n"
    "ds_read_b64_tr_b16 v[54:55], v8 offset:59392\n"
    "v_mfma_f32_16x16x32_bf16 v[96:99], v[36:39], a[232:235], v[96:99]\n"
    "s_add_u32 s84, s37, 4096\n"
    "ds_read_b64_tr_b16 v[56:57], v9 offset:57344\n"
    "v_mfma_f32_16x16x32_bf16 v[100:103], v[36:39], a[236:239], v[100:103]\n"
    "s_add_u32 m0, s36, 61440\n"
    "ds_read_b64_tr_b16 v[58:59], v9 offset:59392\n"
    "ds_read_b64_tr_b16 v[32:33], v8 offset:57600\n"
    "v_mfma_f32_16x16x32_bf16 v[152:155], v[40:43], a[232:235], v[152:155]\n"
    "s_nop 0\n"
    "buffer_load_dwordx4 v16, s[16:19], s84 offen sc1 lds\n"
    "v_mfma_f32_16x16x32_bf16 v[156:159], v[40:43], a[236:239], v[156:159]\n"
    "s_waitcnt lgkmcnt(5)\n"
    "ds_read_b64_tr_b16 v[34:35], v8 offset:59648\n"
    "v_mfma_f32_16x16x32_bf16 v[104:107], v[44:47], a[232:235], v[104:107]\n"
    "s_nop 0\n"
    "ds_read_b64_tr_b16 v[36:37], v9 offset:57600\n"
    "v_mfma_f32_16x16x32_bf16 v[108:111], v[44:47], a[236:239], v[108:111]\n"
    "ds_read_b64_tr_b16 v[38:39], v9 offset:59648\n"
    "ds_read_b64_tr_b16 v[40:41], v8 offset:61440\n"
    "s_cmp_eq_u32 s101, 0\n"
    "s_cbranch_scc1 LSPW_%=\n"
    "LSPWr_%=:\n"
    "v_mfma_f32_16x16x32_bf16 v[160:163], v[48:51], a[232:235], v[160:163]\n"
    "buffer_load_dwordx4 v[240:243], v31, s[16:19], s93 offen offset:0 sc1\n"
    "v_mfma_f32_16x16x32_bf16 v[164:167], v[48:51], a[236:239], v[164:167]\n"
    "s_waitcnt lgkmcnt(5)\n"
    "ds_read_b64_tr_b16 v[42:43], v8 offset:63488\n"
    "v_mfma_f32_16x16x32_bf16 v[96:99], v[52:55], a[240:243], v[96:99]\n"
    "s_nop 0\n"
    "ds_read_b64_tr_b16 v[44:45], v9 offset:61440\n"
    "v_mfma_f32_16x16x32_bf16 v[100:103], v[52:55], a[244:247], v[100:103]\n"
    "ds_read_b64_tr_b16 v[46:47], v9 offset:63488\n"
    "v_mfma_f32_16x16x32_bf16 v[152:155], v[56:59], a[240:243], v[152:155]\n"
    "s_mov_b32 s83, 12713984\n"
    "ds_read_b64_tr_b16 v[48:49], v8 offset:61696\n"
    "v_mfma_f32_16x16x32_bf16 v[156:159], v[56:59], a[244:247], v[156:159]\n"
    "s_waitcnt vmcnt(10) lgkmcnt(5)\n"
    "ds_read_b64_tr_b16 v[50:51], v8 offset:63744\n"
    "v_mfma_f32_16x16x32_bf16 v[104:107], v[32:35], a[240:243], v[104:107]\n"
    "s_barrier\n"
    "ds_read_b64_tr_b16 v[52:53], v9 offset:61696\n"
    "ds_read_b64_tr_b16 v[54:55], v9 offset:63744\n"
    "v_mfma_f32_16x16x32_bf16 v[108:111], v[32:35], a[244:247], v[108:111]\n"
    "buffer_store_dwordx2 v[60:61], v23, s[16:19], s83 offen sc1\n"
    "v_mfma_f32_16x16x32_bf16 v[160:163], v[36:39], a[240:243], v[160:163]\n"
    "ds_read_b64_tr_b16 v[56:57], v10 offset:0\n"
    "v_mfma_f32_16x16x32_bf16 v[164:167], v[36:39], a[244:247], v[164:167]\n"
    "s_waitcnt lgkmcnt(5)\n"
    "ds_read_b64_tr_b16 v[58:59], v10 offset:1024\n"
    "v_mfma_f32_16x16x32_bf16 v[96:99], v[40:43], a[248:251], v[96:99]\n"
    "s_nop 0\n"
    "ds_read_b64_tr_b16 v[32:33], v11 offset:0\n"
    "v_mfma_f32_16x16x32_bf16 v[100:103], v[40:43], a[252:255], v[100:103]\n"
    "ds_read_b64_tr_b16 v[34:35], v11 offset:1024\n"
    "ds_read_b64_tr_b16 v[36:37], v10 offset:2048\n"
    "v_mfma_f32_16x16x32_bf16 v[152:155], v[44:47], a[248:251], v[152:155]\n"
    "buffer_load_dwordx4 v[244:247], v31, s[16:19], s93 offen offset:1024 sc1\n"
    "v_mfma_f32_16x16x32_bf16 v[156:159], v[44:47], a[252:255], v[156:159]\n"
    "s_waitcnt lgkmcnt(5)\n"
    "ds_read_b64_tr_b16 v[38:39], v10 offset:3072\n"
    "v_mfma_f32_16x16x32_bf16 v[104:107], v[48:51], a[248:251], v[104:107]\n"
    "s_nop 0\n"
    "ds_read_b64_tr_b16 v[40:41], v11 offset:2048\n"
    "v_mfma_f32_16x16x32_bf16 v[108:111], v[48:51], a[252:255], v[108:111]\n"
    "ds_read_b64_tr_b16 v[42:43], v11 offset:3072\n"
    "v_mfma_f32_16x16x32_bf16 v[160:163], v[52:55], a[248:251], v[160:163]\n"
    "s_add_u32 s84, s52, 0\n"
    "ds_read_b64_tr_b16 v[44:45], v10 offset:4096\n"
    "v_mfma_f32_16x16x32_bf16 v[164:167], v[52:55], a[252:255], v[164:167]\n"
    "s_waitcnt lgkmcnt(5)\n"
    "ds_read_b64_tr_b16 v[46:47], v10 offset:5120\n"
    "v_mfma_f32_16x16x32_bf16 v[116:119], v[56:59], a[4:7], v[112:115]\n"
    "s_add_u32 m0, s36, 65536\n"
    "ds_read_b64_tr_b16 v[48:49], v11 offset:4096\n"
    "ds_read_b64_tr_b16 v[50:51], v11 offset:5120\n"
    "v_mfma_f32_16x16x32_bf16 v[112:115], v[56:59], a[0:3], v[112:115]\n"
    "buffer_load_dwordx4 v17, s[12:15], s84 offen lds\n"
    "v_mfma_f32_16x16x32_bf16 v[172:175], v[32:35], a[4:7], v[168:171]\n"
    "s_add_u32 s84, s52, 32768\n"
    "ds_read_b64_tr_b16 v[52:53], v10 offset:6144\n"
    "v_mfma_f32_16x16x32_bf16 v[168:171], v[32:35], a[0:3], v[168:171]\n"
    "s_waitcnt lgkmcnt(5)\n"
    "ds_read_b64_tr_b16 v[54:55], v10 offset:7168\n"
    "v_mfma_f32_16x16x32_bf16 v[112:115], v[36:39], a[8:11], v[112:115]\n"
    "s_add_u32 m0, s36, 69632\n"
    "ds_read_b64_tr_b16 v[56:57], v11 offset:6144\n"
    "ds_read_b64_tr_b16 v[58:59], v11 offset:7168\n"
    "v_mul_f32_e64 v96, v96, |v96|\n"
    "v_mfma_f32_16x16x32_bf16 v[116:119], v[36:39], a[12:15], v[116:119]\n"
    "s_nop 0\n"
    "buffer_load_dwordx4 v17, s[12:15], s84 offen lds\n"
    "v_mfma_f32_16x16x32_bf16 v[168:171], v[40:43], a[8:11], v[168:171]\n"
    "ds_read_b64_tr_b16 v[32:33], v10 offset:8192\n"
    "v_mul_f32_e64 v97, v97, |v97|\n"
    "v_mfma_f32_16x16x32_bf16 v[172:175], v[40:43], a[12:15], v[172:175]\n"
    "s_waitcnt lgkmcnt(5)\n"
    "ds_read_b64_tr_b16 v[34:35], v10 offset:9216\n"
    "v_mul_f32_e64 v98, v98, |v98|\n"
    "v_mfma_f32_16x16x32_bf16 v[112:115], v[44:47], a[16:19], v[112:115]\n"
    "s_add_u32 s84, s52, 65536\n"
    "ds_read_b64_tr_b16 v[36:37], v11 offset:8192\n"
    "v_mul_f32_e64 v99, v99, |v99|\n"
    "v_mfma_f32_16x16x32_bf16 v[116:119], v[44:47], a[20:23], v[116:119]\n"
    "s_add_u32 m0, s36, 73728\n"
    "ds_read_b64_tr_b16 v[38:39], v11 offset:9216\n"
    "ds_read_b64_tr_b16 v[40:41], v10 offset:10240\n"
    "v_mfma_f32_16x16x32_bf16 v[168:171], v[48:51], a[16:19], v[168:171]\n"
    "s_nop 0\n"
    "buffer_load_dwordx4 v17, s[12:15], s84 offen lds\n"
    "v_mfma_f32_16x16x32_bf16 v[172:175], v[48:51], a[20:23], v[172:175]\n"
    "s_waitcnt lgkmcnt(5)\n"
    "ds_read_b64_tr_b16 v[42:43], v10 offset:11264\n"
    "v_mul_f32_e64 v152, v152, |v152|\n"
    "v_mfma_f32_16x16x32_bf16 v[112:115], v[52:55], a[24:27], v[112:115]\n"
    "s_add_u32 s84, s52, 98304\n"
    "ds_read_b64_tr_b16 v[44:45], v11 offset:10240\n"
    "v_mul_f32_e64 v153, v153, |v153|\n"
    "v_mfma_f32_16x16x32_bf16 v[116:119], v[52:55], a[28:31], v[116:119]\n"
    "s_add_u32 m0, s36, 77824\n"
    "ds_read_b64_tr_b16 v[46:47], v11 offset:11264\n"
    "ds_read_b64_tr_b16 v[48:49], v10 offset:12288\n"
    "v_mfma_f32_16x16x32_bf16 v[168:171], v[56:59], a[24:27], v[168:171]\n"
    "s_nop 0\n"
    "buffer_load_dwordx4 v17, s[12:15], s84 offen lds\n"
    "v_mfma_f32_16x16x32_bf16 v[172:175], v[56:59], a[28:31], v[172:175]\n"
    "s_waitcnt lgkmcnt(5)\n"
    "ds_read_b64_tr_b16 v[50:51], v10 offset:13312\n"
    "v_mul_f32_e64 v154, v154, |v154|\n"
    "v_mfma_f32_16x16x32_bf16 v[112:115], v[32:35], a[32:35], v[112:115]\n"
    "s_nop 0\n"
    "ds_read_b64_tr_b16 v[52:53], v11 offset:12288\n"
    "v_mul_f32_e64 v155, v155, |v155|\n"
    "v_mfma_f32_16x16x32_bf16 v[116:119], v[32:35], a[36:39], v[116:119]\n"
    "s_add_u32 s84, s52, 131072\n"
    "ds_read_b64_tr_b16 v[54:55], v11 offset:13312\n"
    "v_cvt_pk_bf16_f32 v96, v96, v97\n"
    "v_mfma_f32_16x16x32_bf16 v[168:171], v[36:39], a[32:35], v[168:171]\n"
    "s_add_u32 m0, s36, 81920\n"
    "ds_read_b64_tr_b16 v[56:57], v10 offset:14336\n"
    "ds_read_b64_tr_b16 v[58:59], v10 offset:15360\n"
    "v_mfma_f32_16x16x32_bf16 v[172:175], v[36:39], a[36:39], v[172:175]\n"
    "s_waitcnt lgkmcnt(6)\n"
    "buffer_load_dwordx4 v17, s[12:15], s84 offen lds\n"
    "v_mfma_f32_16x16x32_bf16 v[112:115], v[40:43], a[40:43], v[112:115]\n"
    "s_barrier\n"
    "ds_read_b64_tr_b16 v[32:33], v11 offset:14336\n"
    "v_cvt_pk_bf16_f32 v97, v98, v99\n"
    "v_mfma_f32_16x16x32_bf16 v[116:119], v[40:43], a[44:47], v[116:119]\n"
    "s_add_u32 s84, s52, 163840\n"
    "ds_read_b64_tr_b16 v[34:35], v11 offset:15360\n"
    "v_cvt_pk_bf16_f32 v98, v152, v153\n"
    "v_mfma_f32_16x16x32_bf16 v[168:171], v[44:47], a[40:43], v[168:171]\n"
    "s_add_u32 m0, s36, 86016\n"
    "ds_read_b64_tr_b16 v[36:37], v10 offset:16384\n"
    "ds_read_b64_tr_b16 v[38:39], v10 offset:17408\n"
    "v_mfma_f32_16x16x32_bf16 v[172:175], v[44:47], a[44:47], v[172:175]\n"
    "s_waitcnt lgkmcnt(6)\n"
    "buffer_load_dwordx4 v17, s[12:15], s84 offen lds\n"
    "v_mfma_f32_16x16x32_bf16 v[112:115], v[48:51], a[48:51], v[112:115]\n"
    "s_nop 0\n"
    "ds_read_b64_tr_b16 v[40:41], v11 offset:16384\n"
    "v_cvt_pk_bf16_f32 v99, v154, v155\n"
    "v_mfma_f32_16x16x32_bf16 v[116:119], v[48:51], a[52:55], v[116:119]\n"
    "s_add_u32 s84, s52, 196608\n"
    "ds_read_b64_tr_b16 v[42:43], v11 offset:17408\n"
    "v_pk_max_i16 v96, v96, 0\n"
    "v_mfma_f32_16x16x32_bf16 v[168:171], v[52:55], a[48:51], v[168:171]\n"
    "s_add_u32 m0, s36, 90112\n"
    "ds_read_b64_tr_b16 v[44:45], v10 offset:18432\n"
    "v_pk_max_i16 v97, v97, 0\n"
    "v_mfma_f32_16x16x32_bf16 v[172:175], v[52:55], a[52:55], v[172:175]\n"
    "s_waitcnt lgkmcnt(5)\n"
    "ds_read_b64_tr_b16 v[46:47], v10 offset:19456\n"
    "ds_read_b64_tr_b16 v[48:49], v11 offset:18432\n"
    "v_mfma_f32_16x16x32_bf16 v[112:115], v[56:59], a[56:59], v[112:115]\n"
    "s_nop 0\n"
    "buffer_load_dwordx4 v17, s[12:15], s84 offen lds\n"
    "v_mfma_f32_16x16x32_bf16 v[116:119], v[56:59], a[60:63], v[116:119]\n"
    "ds_read_b64_tr_b16 v[50:51], v11 offset:19456\n"
    "v_pk_max_i16 v98, v98, 0\n"
    "v_mfma_f32_16x16x32_bf16 v[168:171], v[32:35], a[56:59], v[168:171]\n"
    "s_add_u32 s86, s69, 0\n"
    "ds_read_b64_tr_b16 v[52:53], v10 offset:20480\n"
    "v_pk_max_i16 v99, v99, 0\n"
    "v_mfma_f32_16x16x32_bf16 v[172:175], v[32:35], a[60:63], v[172:175]\n"
    "s_waitcnt lgkmcnt(5)\n"
    "ds_read_b64_tr_b16 v[54:55], v10 offset:21504\n"
    "ds_read_b64_tr_b16 v[56:57], v11 offset:20480\n"
    "v_mfma_f32_16x16x32_bf16 v[112:115], v[36:39], a[64:67], v[112:115]\n"
    "buffer_store_dwordx4 v[96:99], v20, s[16:19], s86 offen offset:0 sc1\n"
    "v_mfma_f32_16x16x32_bf16 v[116:119], v[36:39], a[68:71], v[116:119]\n"
    "ds_read_b64_tr_b16 v[58:59], v11 offset:21504\n"
    "v_mul_f32_e64 v100, v100, |v100|\n"
    "v_mfma_f32_16x16x32_bf16 v[168:171], v[40:43], a[64:67], v[168:171]\n"
    "ds_read_b64_tr_b16 v[32:33], v10 offset:22528\n"
    "v_mul_f32_e64 v101, v101, |v101|\n"
    "v_mfma_f32_16x16x32_bf16 v[172:175], v[40:43], a[68:71], v[172:175]\n"
    "s_waitcnt lgkmcnt(5)\n"
    "ds_read_b64_tr_b16 v[34:35], v10 offset:23552\n"
    "v_mul_f32_e64 v102, v102, |v102|\n"
    "v_mfma_f32_16x16x32_bf16 v[112:115], v[44:47], a[72:75], v[112:115]\n"
    "s_nop 0\n"
    "ds_read_b64_tr_b16 v[36:37], v11 offset:22528\n"
    "v_mul_f32_e64 v103, v103, |v103|\n"
    "v_mfma_f32_16x16x32_bf16 v[116:119], v[44:47], a[76:79], v[116:119]\n"
    "s_add_u32 s84, s52, 229376\n"
    "ds_read_b64_tr_b16 v[38:39], v11 offset:23552\n"
    "v_mul_f32_e64 v156, v156, |v156|\n"
    "v_mfma_f32_16x16x32_bf16 v[168:171], v[48:51], a[72:75], v[168:171]\n"
    "s_add_u32 m0, s36, 94208\n"
    "ds_read_b64_tr_b16 v[40:41], v10 offset:24576\n"
    "ds_read_b64_tr_b16 v[42:43], v10 offset:25600\n"
    "v_mfma_f32_16x16x32_bf16 v[172:175], v[48:51], a[76:79], v[172:175]\n"
    "s_waitcnt lgkmcnt(6)\n"
    "buffer_load_dwordx4 v17, s[12:15], s84 offen lds\n"
    "v_mfma_f32_16x16x32_bf16 v[112:115], v[52:55], a[80:83], v[112:115]\n"
    "s_nop 0\n"
    "ds_read_b64_tr_b16 v[44:45], v11 offset:24576\n"
    "v_mul_f32_e64 v157, v157, |v157|\n"
    "v_mfma_f32_16x16x32_bf16 v[116:119], v[52:55], a[84:87], v[116:119]\n"
    "ds_read_b64_tr_b16 v[46:47], v11 offset:25600\n"
    "v_mul_f32_e64 v158, v158, |v158|\n"
    "v_mfma_f32_16x16x32_bf16 v[168:171], v[56:59], a[80:83], v[168:171]\n"
    "ds_read_b64_tr_b16 v[48:49], v10 offset:26624\n"
    "v_mul_f32_e64 v159, v159, |v159|\n"
    "v_mfma_f32_16x16x32_bf16 v[172:175], v[56:59], a[84:87], v[172:175]\n"
    "s_waitcnt lgkmcnt(5)\n"
    "ds_read_b64_tr_b16 v[50:51], v10 offset:27648\n"
    "ds_read_b64_tr_b16 v[52:53], v11 offset:26624\n"
    "v_mfma_f32_16x16x32_bf16 v[112:115], v[32:35], a[88:91], v[112:115]\n"
    "s_nop 0\n"
    "buffer_load_dwordx4 v[248:251], v31, s[16:19], s93 offen offset:2048 sc1\n"
    "v_mfma_f32_16x16x32_bf16 v[116:119], v[32:35], a[92:95], v[116:119]\n"
    "ds_read_b64_tr_b16 v[54:55], v11 offset:27648\n"
    "v_mfma_f32_16x16x32_bf16 v[168:171], v[36:39], a[88:91], v[168:171]\n"
    "ds_read_b64_tr_b16 v[56:57], v10 offset:28672\n"
    "v_cvt_pk_bf16_f32 v100, v100, v101\n"
    "v_mfma_f32_16x16x32_bf16 v[172:175], v[36:39], a[92:95], v[172:175]\n"
    "s_waitcnt lgkmcnt(5)\n"
    "ds_read_b64_tr_b16 v[58:59], v10 offset:29696\n"
    "v_cvt_pk_bf16_f32 v101, v102, v103\n"
    "v_mfma_f32_16x16x32_bf16 v[112:115], v[40:43], a[96:99], v[112:115]\n"
    "s_nop 0\n"
    "ds_read_b64_tr_b16 v[32:33], v11 offset:28672\n"
    "v_cvt_pk_bf16_f32 v102, v156, v157\n"
    "v_mfma_f32_16x16x32_bf16 v[116:119], v[40:43], a[100:103], v[116:119]\n"
    "ds_read_b64_tr_b16 v[34:35], v11 offset:29696\n"
    "v_cvt_pk_bf16_f32 v103, v158, v159\n"
    "v_mfma_f32_16x16x32_bf16 v[168:171], v[44:47], a[96:99], v[168:171]\n"
    "ds_read_b64_tr_b16 v[36:37], v10 offset:30720\n"
    "ds_read_b64_tr_b16 v[38:39], v10 offset:31744\n"
    "v_mfma_f32_16x16x32_bf16 v[172:175], v[44:47], a[100:103], v[172:175]\n"
    "s_waitcnt vmcnt(21) lgkmcnt(6)\n"
    "buffer_load_dwordx4 v[252:255], v31, s[16:19], s93 offen offset:3072 sc1\n"
    "v_mfma_f32_16x16x32_bf16 v[112:115], v[48:51], a[104:107], v[112:115]\n"
    "s_barrier\n"
    "ds_read_b64_tr_b16 v[40:41], v11 offset:30720\n"
    "v_pk_max_i16 v100, v100, 0\n"
    "v_mfma_f32_16x16x32_bf16 v[116:119], v[48:51], a[108:111], v[116:119]\n"
    "ds_read_b64_tr_b16 v[42:43], v11 offset:31744\n"
    "v_pk_max_i16 v101, v101, 0\n"
    "v_mfma_f32_16x16x32_bf16 v[168:171], v[52:55], a[104:107], v[168:171]\n"
    "ds_read_b64_tr_b16 v[44:45], v10 offset:32768\n"
    "v_pk_max_i16 v102, v102, 0\n"
    "v_mfma_f32_16x16x32_bf16 v[172:175], v[52:55], a[108:111], v[172:175]\n"
    "s_waitcnt lgkmcnt(5)\n"
    "ds_read_b64_tr_b16 v[46:47], v10 offset:33792\n"
    "v_pk_max_i16 v103, v103, 0\n"
    "v_mfma_f32_16x16x32_bf16 v[112:115], v[56:59], a[112:115], v[112:115]\n"
    "s_nop 0\n"
    "ds_read_b64_tr_b16 v[48:49], v11 offset:32768\n"
    "v_mfma_f32_16x16x32_bf16 v[116:119], v[56:59], a[116:119], v[116:119]\n"
    "ds_read_b64_tr_b16 v[50:51], v11 offset:33792\n"
    "ds_read_b64_tr_b16 v[52:53], v10 offset:34816\n"
    "v_mfma_f32_16x16x32_bf16 v[168:171], v[32:35], a[112:115], v[168:171]\n"
    "buffer_store_dwordx4 v[100:103], v20, s[16:19], s86 offen offset:1024 sc1\n"
    "v_mfma_f32_16x16x32_bf16 v[172:175], v[32:35], a[116:119], v[172:175]\n"
    "s_waitcnt lgkmcnt(5)\n"
    "ds_read_b64_tr_b16 v[54:55], v10 offset:35840\n"
    "v_mul_f32_e64 v104, v104, |v104|\n"
    "v_mul_f32_e64 v105, v105, |v105|\n"
    "v_mfma_f32_16x16x32_bf16 v[112:115], v[36:39], a[120:123], v[112:115]\n"
    "LPAD6_%=:\n"
    "s_cbranch_execz LPADR_%=\n"
    "ds_read_b64_tr_b16 v[56:57], v11 offset:34816\n"
    "v_mul_f32_e64 v106, v106, |v106|\n"
    "v_mfma_f32_16x16x32_bf16 v[116:119], v[36:39], a[124:127], v[116:119]\n"
    "ds_read_b64_tr_b16 v[58:59], v11 offset:35840\n"
    "v_mul_f32_e64 v107, v107, |v107|\n"
    "v_mfma_f32_16x16x32_bf16 v[168:171], v[40:43], a[120:123], v[168:171]\n"
    "s_add_u32 s84, s53, 0\n"
    "ds_read_b64_tr_b16 v[32:33], v10 offset:36864\n"
    "v_mul_f32_e64 v160, v160, |v160|\n"
    "v_mfma_f32_16x16x32_bf16 v[172:175], v[40:43], a[124:127], v[172:175]\n"
    "s_waitcnt lgkmcnt(5)\n"
    "ds_read_b64_tr_b16 v[34:35], v10 offset:37888\n"
    "v_mul_f32_e64 v161, v161, |v161|\n"
    "v_mfma_f32_16x16x32_bf16 v[112:115], v[44:47], a[128:131], v[112:115]\n"
    "s_add_u32 m0, s36, 98304\n"
    "ds_read_b64_tr_b16 v[36:37], v11 offset:36864\n"
    "ds_read_b64_tr_b16 v[38:39], v11 offset:37888\n"
    "v_mfma_f32_16x16x32_bf16 v[116:119], v[44:47], a[132:135], v[116:119]\n"
    "buffer_load_dwordx4 v17, s[12:15], s84 offen lds\n"
    "v_mfma_f32_16x16x32_bf16 v[168:171], v[48:51], a[128:131], v[168:171]\n"
    "s_add_u32 s84, s53, 32768\n"
    "ds_read_b64_tr_b16 v[40:41], v10 offset:38912\n"
    "v_mul_f32_e64 v162, v162, |v162|\n"
    "v_mfma_f32_16x16x32_bf16 v[172:175], v[48:51], a[132:135], v[172:175]\n"
    "s_waitcnt lgkmcnt(5)\n"
    "ds_read_b64_tr_b16 v[42:43], v10 offset:39936\n"
    "v_mul_f32_e64 v163, v163, |v163|\n"
    "v_mfma_f32_16x16x32_bf16 v[112:115], v[52:55], a[136:139], v[112:115]\n"
    "s_add_u32 m0, s36, 102400\n"
    "ds_read_b64_tr_b16 v[44:45], v11 offset:38912\n"
    "ds_read_b64_tr_b16 v[46:47], v11 offset:39936\n"
    "v_mfma_f32_16x16x32_bf16 v[116:119], v[52:55], a[140:143], v[116:119]\n"
    "s_nop 0\n"
    "buffer_load_dwordx4 v17, s[12:15], s84 offen lds\n"
    "v_mfma_f32_16x16x32_bf16 v[168:171], v[56:59], a[136:139], v[168:171]\n"
    "ds_read_b64_tr_b16 v[48:49], v10 offset:40960\n"
    "v_cvt_pk_bf16_f32 v104, v104, v105\n"
    "v_mfma_f32_16x16x32_bf16 v[172:175], v[56:59], a[140:143], v[172:175]\n"
    "s_waitcnt lgkmcnt(5)\n"
    "ds_read_b64_tr_b16 v[50:51], v10 offset:41984\n"
    "v_cvt_pk_bf16_f32 v105, v106, v107\n"
    "v_mfma_f32_16x16x32_bf16 v[112:115], v[32:35], a[144:147], v[112:115]\n"
    "s_add_u32 s84, s53, 65536\n"
    "ds_read_b64_tr_b16 v[52:53], v11 offset:40960\n"
    "v_cvt_pk_bf16_f32 v106, v160, v161\n"
    "v_mfma_f32_16x16x32_bf16 v[116:119], v[32:35], a[148:151], v[116:119]\n"
    "s_add_u32 m0, s36, 106496\n"
    "ds_read_b64_tr_b16 v[54:55], v11 offset:41984\n"
    "ds_read_b64_tr_b16 v[56:57], v10 offset:43008\n"
    "v_mfma_f32_16x16x32_bf16 v[168:171], v[36:39], a[144:147], v[168:171]\n"
    "s_nop 0\n"
    "buffer_load_dwordx4 v17, s[12:15], s84 offen lds\n"
    "v_mfma_f32_16x16x32_bf16 v[172:175], v[36:39], a[148:151], v[172:175]\n"
    "s_waitcnt lgkmcnt(5)\n"
    "ds_read_b64_tr_b16 v[58:59], v10 offset:44032\n"
    "v_cvt_pk_bf16_f32 v107, v162, v163\n"
    "v_mfma_f32_16x16x32_bf16 v[112:115], v[40:43], a[152:155], v[112:115]\n"
    "s_add_u32 s84, s53, 98304\n"
    "ds_read_b64_tr_b16 v[32:33], v11 offset:43008\n"
    "v_pk_max_i16 v104, v104, 0\n"
    "v_mfma_f32_16x16x32_bf16 v[116:119], v[40:43], a[156:159], v[116:119]\n"
    "s_add_u32 m0, s36, 110592\n"
    "ds_read_b64_tr_b16 v[34:35], v11 offset:44032\n"
    "ds_read_b64_tr_b16 v[36:37], v10 offset:45056\n"
    "v_mfma_f32_16x16x32_bf16 v[168:171], v[44:47], a[152:155], v[168:171]\n"
    "s_nop 0\n"
    "buffer_load_dwordx4 v17, s[12:15], s84 offen lds\n"
    "v_mfma_f32_16x16x32_bf16 v[172:175], v[44:47], a[156:159], v[172:175]\n"
    "s_waitcnt lgkmcnt(5)\n"
    "ds_read_b64_tr_b16 v[38:39], v10 offset:46080\n"
    "v_pk_max_i16 v105, v105, 0\n"
    "v_mfma_f32_16x16x32_bf16 v[112:115], v[48:51], a[160:163], v[112:115]\n"
    "s_nop 0\n"
    "ds_read_b64_tr_b16 v[40:41], v11 offset:45056\n"
    "v_pk_max_i16 v106, v106, 0\n"
    "v_mfma_f32_16x16x32_bf16 v[116:119], v[48:51], a[164:167], v[116:119]\n"
    "ds_read_b64_tr_b16 v[42:43], v11 offset:46080\n"
    "v_pk_max_i16 v107, v107, 0\n"
    "v_mfma_f32_16x16x32_bf16 v[168:171], v[52:55], a[160:163], v[168:171]\n"
    "s_add_u32 s86, s69, 8192\n"
    "ds_read_b64_tr_b16 v[44:45], v10 offset:47104\n"
    "ds_read_b64_tr_b16 v[46:47], v10 offset:48128\n"
    "v_mfma_f32_16x16x32_bf16 v[172:175], v[52:55], a[164:167], v[172:175]\n"
    "s_waitcnt vmcnt(19) lgkmcnt(6)\n"
    "buffer_store_dwordx4 v[104:107], v20, s[16:19], s86 offen offset:0 sc1\n"
    "v_mfma_f32_16x16x32_bf16 v[112:115], v[56:59], a[168:171], v[112:115]\n"
    "s_barrier\n"
    "ds_read_b64_tr_b16 v[48:49], v11 offset:47104\n"
    "v_mul_f32_e64 v108, v108, |v108|\n"
    "v_mul_f32_e64 v109, v109, |v109|\n"
    "v_mfma_f32_16x16x32_bf16 v[116:119], v[56:59], a[172:175], v[116:119]\n"
    "ds_read_b64_tr_b16 v[50:51], v11 offset:48128\n"
    "v_mul_f32_e64 v110, v110, |v110|\n"
    "v_mfma_f32_16x16x32_bf16 v[168:171], v[32:35], a[168:171], v[168:171]\n"
    "ds_read_b64_tr_b16 v[52:53], v10 offset:49152\n"
    "v_mul_f32_e64 v111, v111, |v111|\n"
    "v_mfma_f32_16x16x32_bf16 v[172:175], v[32:35], a[172:175], v[172:175]\n"
    "s_waitcnt lgkmcnt(5)\n"
    "ds_read_b64_tr_b16 v[54:55], v10 offset:50176\n"
    "v_mul_f32_e64 v164, v164, |v164|\n"
    "v_mfma_f32_16x16x32_bf16 v[112:115], v[36:39], a[176:179], v[112:115]\n"
    "s_nop 0\n"
    "ds_read_b64_tr_b16 v[56:57], v11 offset:49152\n"
    "v_mul_f32_e64 v165, v165, |v165|\n"
    "v_mfma_f32_16x16x32_bf16 v[116:119], v[36:39], a[180:183], v[116:119]\n"
    "s_mov_b32 s87, 12713984\n"
    "ds_read_b64_tr_b16 v[58:59], v11 offset:50176\n"
    "ds_read_b64_tr_b16 v[32:33], v10 offset:51200\n"
    "v_mfma_f32_16x16x32_bf16 v[168:171], v[40:43], a[176:179], v[168:171]\n"
    "buffer_load_dwordx2 v[62:63], v24, s[16:19], s87 offen sc1\n"
    "v_mfma_f32_16x16x32_bf16 v[172:175], v[40:43], a[180:183], v[172:175]\n"
    "s_waitcnt lgkmcnt(5)\n"
    "ds_read_b64_tr_b16 v[34:35], v10 offset:52224\n"
    "v_mul_f32_e64 v166, v166, |v166|\n"
    "v_mfma_f32_16x16x32_bf16 v[112:115], v[44:47], a[184:187], v[112:115]\n"
    "s_add_u32 s84, s54, 0\n"
    "ds_read_b64_tr_b16 v[36:37], v11 offset:51200\n"
    "v_mul_f32_e64 v167, v167, |v167|\n"
    "v_mfma_f32_16x16x32_bf16 v[116:119], v[44:47], a[188:191], v[116:119]\n"
    "s_add_u32 m0, s36, 114688\n"
    "ds_read_b64_tr_b16 v[38:39], v11 offset:52224\n"
    "ds_read_b64_tr_b16 v[40:41], v10 offset:53248\n"
    "v_mfma_f32_16x16x32_bf16 v[168:171], v[48:51], a[184:187], v[168:171]\n"
    "buffer_load_dwordx4 v17, s[12:15], s84 offen lds\n"
    "v_mfma_f32_16x16x32_bf16 v[172:175], v[48:51], a[188:191], v[172:175]\n"
    "s_waitcnt lgkmcnt(5)\n"
    "ds_read_b64_tr_b16 v[42:43], v10 offset:54272\n"
    "v_cvt_pk_bf16_f32 v108, v108, v109\n"
    "v_mfma_f32_16x16x32_bf16 v[112:115], v[52:55], a[192:195], v[112:115]\n"
    "s_nop 0\n"
    "ds_read_b64_tr_b16 v[44:45], v11 offset:53248\n"
    "v_cvt_pk_bf16_f32 v109, v110, v111\n"
    "v_mfma_f32_16x16x32_bf16 v[116:119], v[52:55], a[196:199], v[116:119]\n"
    "s_add_u32 s84, s54, 32768\n"
    "ds_read_b64_tr_b16 v[46:47], v11 offset:54272\n"
    "v_cvt_pk_bf16_f32 v110, v164, v165\n"
    "v_mfma_f32_16x16x32_bf16 v[168:171], v[56:59], a[192:195], v[168:171]\n"
    "s_add_u32 m0, s36, 118784\n"
    "ds_read_b64_tr_b16 v[48:49], v10 offset:55296\n"
    "ds_read_b64_tr_b16 v[50:51], v10 offset:56320\n"
    "v_mfma_f32_16x16x32_bf16 v[172:175], v[56:59], a[196:199], v[172:175]\n"
    "s_waitcnt lgkmcnt(6)\n"
    "buffer_load_dwordx4 v17, s[12:15], s84 offen lds\n"
    "v_mfma_f32_16x16x32_bf16 v[112:115], v[32:35], a[200:203], v[112:115]\n"
    "s_nop 0\n"
    "ds_read_b64_tr_b16 v[52:53], v11 offset:55296\n"
    "ds_read_b128 a[168:171], v218 offset:8192\n"
    "v_cvt_pk_bf16_f32 v111, v166, v167\n"
    "v_mfma_f32_16x16x32_bf16 v[116:119], v[32:35], a[204:207], v[116:119]\n"
    "s_add_u32 s84, s55, 0\n"
    "ds_read_b64_tr_b16 v[54:55], v11 offset:56320\n"
    "ds_read_b128 a[172:175], v218 offset:9216\n"
    "v_pk_max_i16 v108, v108, 0\n"
    "v_mfma_f32_16x16x32_bf16 v[168:171], v[36:39], a[200:203], v[168:171]\n"
    "s_add_u32 m0, s36, 122880\n"
    "ds_read_b64_tr_b16 v[56:57], v10 offset:57344\n"
    "ds_read_b64_tr_b16 v[58:59], v10 offset:58368\n"
    "v_mfma_f32_16x16x32_bf16 v[172:175], v[36:39], a[204:207], v[172:175]\n"
    "s_waitcnt lgkmcnt(8)\n"
    "buffer_load_dwordx4 v17, s[12:15], s84 offen lds\n"
    "v_mfma_f32_16x16x32_bf16 v[112:115], v[40:43], a[208:211], v[112:115]\n"
    "ds_read_b64_tr_b16 v[32:33], v11 offset:57344\n"
    "ds_read_b128 a[176:179], v218 offset:10240\n"
    "v_pk_max_i16 v109, v109, 0\n"
    "v_mfma_f32_16x16x32_bf16 v[116:119], v[40:43], a[212:215], v[116:119]\n"
    "ds_read_b64_tr_b16 v[34:35], v11 offset:58368\n"
    "ds_read_b128 a[180:183], v218 offset:11264\n"
    "v_pk_max_i16 v110, v110, 0\n"
    "v_mfma_f32_16x16x32_bf16 v[168:171], v[44:47], a[208:211], v[168:171]\n"
    "ds_read_b64_tr_b16 v[36:37], v10 offset:59392\n"
    "ds_read_b64 a[184:185], v216 offset:0\n"
    "v_pk_max_i16 v111, v111, 0\n"
    "v_mfma_f32_16x16x32_bf16 v[172:175], v[44:47], a[212:215], v[172:175]\n"
    "s_waitcnt lgkmcnt(9)\n"
    "ds_read_b64_tr_b16 v[38:39], v10 offset:60416\n"
    "ds_read_b64_tr_b16 v[40:41], v11 offset:59392\n"
    "v_mfma_f32_16x16x32_bf16 v[112:115], v[48:51], a[216:219], v[112:115]\n"
    "s_nop 0\n"
    "buffer_store_dwordx4 v[108:111], v20, s[16:19], s86 offen offset:1024 sc1\n"
    "v_mfma_f32_16x16x32_bf16 v[116:119], v[48:51], a[220:223], v[116:119]\n"
    "ds_read_b64_tr_b16 v[42:43], v11 offset:60416\n"
    "ds_read_b64 a[186:187], v216 offset:4096\n"
    "v_mfma_f32_16x16x32_bf16 v[168:171], v[52:55], a[216:219], v[168:171]\n"
    "ds_read_b64_tr_b16 v[44:45], v10 offset:61440\n"
    "ds_read_b64 a[188:189], v216 offset:8192\n"
    "v_mfma_f32_16x16x32_bf16 v[172:175], v[52:55], a[220:223], v[172:175]\n"
    "s_waitcnt lgkmcnt(9)\n"
    "ds_read_b64_tr_b16 v[46:47], v10 offset:62464\n"
    "ds_read_b64 a[190:191], v216 offset:12288\n"
    "v_mfma_f32_16x16x32_bf16 v[112:115], v[56:59], a[224:227], v[112:115]\n"
    "s_nop 0\n"
    "ds_read_b64_tr_b16 v[48:49], v11 offset:61440\n"
    "ds_read_b64 a[192:193], v217 offset:0\n"
    "v_mfma_f32_16x16x32_bf16 v[116:119], v[56:59], a[228:231], v[116:119]\n"
    "s_add_u32 s84, s55, 32768\n"
    "ds_read_b64_tr_b16 v[50:51], v11 offset:62464\n"
    "ds_read_b64 a[194:195], v217 offset:4096\n"
    "v_mfma_f32_16x16x32_bf16 v[168:171], v[32:35], a[224:227], v[168:171]\n"
    "s_add_u32 m0, s36, 126976\n"
    "ds_read_b64_tr_b16 v[52:53], v10 offset:63488\n"
    "ds_read_b64_tr_b16 v[54:55], v10 offset:64512\n"
    "v_mfma_f32_16x16x32_bf16 v[172:175], v[32:35], a[228:231], v[172:175]\n"
    "s_waitcnt vmcnt(13) lgkmcnt(11)\n"
    "buffer_load_dwordx4 v17, s[12:15], s84 offen lds\n"
    "v_mfma_f32_16x16x32_bf16 v[112:115], v[36:39], a[232:235], v[112:115]\n"
    "s_barrier\n"
    "ds_read_b64_tr_b16 v[56:57], v11 offset:63488\n"
    "ds_read_b64 a[196:197], v217 offset:8192\n"
    "v_mfma_f32_16x16x32_bf16 v[116:119], v[36:39], a[236:239], v[116:119]\n"
    "s_add_u32 s84, s58, 0\n"
    "ds_read_b64_tr_b16 v[58:59], v11 offset:64512\n"
    "ds_read_b64 a[198:199], v217 offset:12288\n"
    "v_mfma_f32_16x16x32_bf16 v[168:171], v[40:43], a[232:235], v[168:171]\n"
    "s_add_u32 m0, s36, 0\n"
    "ds_read_b64_tr_b16 v[32:33], v4 offset:0\n"
    "ds_read_b64_tr_b16 v[34:35], v4 offset:4096\n"
    "v_mfma_f32_16x16x32_bf16 v[172:175], v[40:43], a[236:239], v[172:175]\n"
    "s_waitcnt lgkmcnt(9)\n"
    "buffer_load_dwordx4 v17, s[12:15], s84 offen lds\n"
    "v_mfma_f32_16x16x32_bf16 v[112:115], v[44:47], a[240:243], v[112:115]\n"
    "s_nop 0\n"
    "ds_read_b64_tr_b16 v[36:37], v5 offset:0\n"
    "v_mfma_f32_16x16x32_bf16 v[116:119], v[44:47], a[244:247], v[116:119]\n"
    "s_add_u32 s84, s58, 32768\n"
    "ds_read_b64_tr_b16 v[38:39], v5 offset:4096\n"
    "v_mfma_f32_16x16x32_bf16 v[168:171], v[48:51], a[240:243], v[168:171]\n"
    "s_add_u32 m0, s36, 4096\n"
    "ds_read_b64_tr_b16 v[40:41], v4 offset:256\n"
    "v_mfma_f32_16x16x32_bf16 v[172:175], v[48:51], a[244:247], v[172:175]\n"
    "s_waitcnt lgkmcnt(5)\n"
    "ds_read_b64_tr_b16 v[42:43], v4 offset:4352\n"
    "ds_read_b64_tr_b16 v[44:45], v5 offset:256\n"
    "v_mfma_f32_16x16x32_bf16 v[112:115], v[52:55], a[248:251], v[112:115]\n"
    "s_barrier\n"
    "buffer_load_dwordx4 v17, s[12:15], s84 offen lds\n"
    "v_mfma_f32_16x16x32_bf16 v[116:119], v[52:55], a[252:255], v[116:119]\n"
    "s_add_u32 s84, s61, 0\n"
    "ds_read_b64_tr_b16 v[46:47], v5 offset:4352\n"
    "v_mfma_f32_16x16x32_bf16 v[168:171], v[56:59], a[248:251], v[168:171]\n"
    "s_add_u32 m0, s36, 8192\n"
    "ds_read_b64_tr_b16 v[48:49], v4 offset:512\n"
    "v_mfma_f32_16x16x32_bf16 v[172:175], v[56:59], a[252:255], v[172:175]\n"
    "s_waitcnt lgkmcnt(5)\n"
    "ds_read_b64_tr_b16 v[50:51], v4 offset:4608\n"
    "v_mfma_f32_32x32x16_bf16 v[176:191], a[184:187], a[168:171], 0\n"
    "buffer_load_dwordx4 v17, s[12:15], s84 offen lds\n"
    "v_mfma_f32_32x32x16_bf16 v[192:207], a[192:195], a[168:171], 0\n"
    "v_mul_f32_e64 v112, v112, |v112|\n"
    "v_mul_f32_e64 v113, v113, |v113|\n"
    "v_mul_f32_e64 v114, v114, |v114|\n"
    "v_mul_f32_e64 v115, v115, |v115|\n"
    "v_mul_f32_e64 v168, v168, |v168|\n"
    "s_add_u32 s84, s61, 32768\n"
    "v_mfma_f32_32x32x16_bf16 v[176:191], a[192:195], v[208:211], v[176:191]\n"
    "v_mul_f32_e64 v169, v169, |v169|\n"
    "v_mul_f32_e64 v170, v170, |v170|\n"
    "s_add_u32 m0, s36, 12288\n"
    "v_mul_f32_e64 v171, v171, |v171|\n"
    "v_cvt_pk_bf16_f32 v112, v112, v113\n"
    "v_cvt_pk_bf16_f32 v113, v114, v115\n"
    "buffer_load_dwordx4 v17, s[12:15], s84 offen lds\n"
    "v_mfma_f32_32x32x16_bf16 v[192:207], a[184:187], a[176:179], v[192:207]\n"
    "v_cvt_pk_bf16_f32 v114, v168, v169\n"
    "v_cvt_pk_bf16_f32 v115, v170, v171\n"
    "s_add_u32 s84, s55, 65536\n"
    "v_pk_max_i16 v112, v112, 0\n"
    "v_pk_max_i16 v113, v113, 0\n"
    "v_pk_max_i16 v114, v114, 0\n"
    "s_waitcnt vmcnt(9)\n"
    "v_cmp_ne_u64 vcc, s[98:99], v[62:63]\n"
    "v_mfma_f32_32x32x16_bf16 v[176:191], a[188:191], a[172:175], v[176:191]\n"
    "v_pk_max_i16 v115, v115, 0\n"
    "v_mul_f32_e64 v116, v116, |v116|\n"
    "s_add_u32 m0, s36, 16384\n"
    "v_mul_f32_e64 v117, v117, |v117|\n"
    "v_mul_f32_e64 v118, v118, |v118|\n"
    "v_mul_f32_e64 v119, v119, |v119|\n"
    "buffer_load_dwordx4 v17, s[12:15], s84 offen lds\n"
    "s_cbranch_vccnz LPLH1_%=s\n"
    "s_nop 0\n"
    "LPLH1_%=:\n"
    "v_mfma_f32_32x32x16_bf16 v[192:207], a[196:199], a[172:175], v[192:207]\n"
    "v_mul_f32_e64 v172, v172, |v172|\n"
    "v_mul_f32_e64 v173, v173, |v173|\n"
    "v_mul_f32_e64 v174, v174, |v174|\n"
    "v_mul_f32_e64 v175, v175, |v175|\n"
    "v_cvt_pk_bf16_f32 v116, v116, v117\n"
    "v_mfma_f32_32x32x16_bf16 v[176:191], a[196:199], v[212:215], v[176:191]\n"
    "v_cvt_pk_bf16_f32 v117, v118, v119\n"
    "v_cvt_pk_bf16_f32 v118, v172, v173\n"
    "v_cvt_pk_bf16_f32 v119, v174, v175\n"
    "v_pk_max_i16 v116, v116, 0\n"
    "v_pk_max_i16 v117, v117, 0\n"
    "s_add_u32 s85, s71, 0\n"
    "v_mfma_f32_32x32x16_bf16 v[192:207], a[188:191], a[180:183], v[192:207]\n"
    "v_pk_max_i16 v118, v118, 0\n"
    "v_pk_max_i16 v119, v119, 0\n"
    "buffer_load_dwordx4 a[0:3], v20, s[16:19], s85 offen offset:0 sc1\n"
    "s_nop 1\n"
    "v_pk_mul_f32 v[152:153], v[176:177], v[136:137]\n"
    "v_pk_mul_f32 v[176:177], v[176:177], v[120:121]\n"
    "s_nop 4\n"
    "v_pk_fma_f32 v[176:177], v[192:193], v[136:137], v[176:177]\n"
    "v_pk_fma_f32 v[192:193], v[192:193], v[120:121], v[152:153] neg_lo:[0,0,1] neg_hi:[0,0,1]\n"
    "v_pk_mul_f32 v[154:155], v[178:179], v[138:139]\n"
    "v_pk_mul_f32 v[178:179], v[178:179], v[122:123]\n"
    "v_pk_fma_f32 v[178:179], v[194:195], v[138:139], v[178:179]\n"
    "v_pk_fma_f32 v[194:195], v[194:195], v[122:123], v[154:155] neg_lo:[0,0,1] neg_hi:[0,0,1]\n"
    "v_pk_mul_f32 v[152:153], v[180:181], v[140:141]\n"
    "s_add_u32 s86, s70, 0\n"
    "v_pk_mul_f32 v[180:181], v[180:181], v[124:125]\n"
    "v_pk_fma_f32 v[180:181], v[196:197], v[140:141], v[180:181]\n"
    "v_pk_fma_f32 v[196:197], v[196:197], v[124:125], v[152:153] neg_lo:[0,0,1] neg_hi:[0,0,1]\n"
    "v_pk_mul_f32 v[154:155], v[182:183], v[142:143]\n"
    "buffer_store_dwordx4 v[112:115], v20, s[16:19], s86 offen offset:0 sc1\n"
    "v_pk_mul_f32 v[182:183], v[182:183], v[126:127]\n"
    "v_pk_fma_f32 v[182:183], v[198:199], v[142:143], v[182:183]\n"
    "v_pk_fma_f32 v[198:199], v[198:199], v[126:127], v[154:155] neg_lo:[0,0,1] neg_hi:[0,0,1]\n"
    "v_pk_mul_f32 v[152:153], v[184:185], v[144:145]\n"
    "v_pk_mul_f32 v[184:185], v[184:185], v[128:129]\n"
    "v_pk_fma_f32 v[184:185], v[200:201], v[144:145], v[184:185]\n"
    "v_pk_fma_f32 v[200:201], v[200:201], v[128:129], v[152:153] neg_lo:[0,0,1] neg_hi:[0,0,1]\n"
    "v_pk_mul_f32 v[154:155], v[186:187], v[146:147]\n"
    "v_pk_mul_f32 v[186:187], v[186:187], v[130:131]\n"
    "v_pk_fma_f32 v[186:187], v[202:203], v[146:147], v[186:187]\n"
    "v_pk_fma_f32 v[202:203], v[202:203], v[130:131], v[154:155] neg_lo:[0,0,1] neg_hi:[0,0,1]\n"
    "v_pk_mul_f32 v[152:153], v[188:189], v[148:149]\n"
    "v_pk_mul_f32 v[188:189], v[188:189], v[132:133]\n"
    "v_pk_fma_f32 v[188:189], v[204:205], v[148:149], v[188:189]\n"
    "v_pk_fma_f32 v[204:205], v[204:205], v[132:133], v[152:153] neg_lo:[0,0,1] neg_hi:[0,0,1]\n"
    "v_pk_mul_f32 v[154:155], v[190:191], v[150:151]\n"
    "v_pk_mul_f32 v[190:191], v[190:191], v[134:135]\n"
    "v_pk_fma_f32 v[190:191], v[206:207], v[150:151], v[190:191]\n"
    "v_pk_fma_f32 v[206:207], v[206:207], v[134:135], v[154:155] neg_lo:[0,0,1] neg_hi:[0,0,1]\n"
    "v_cvt_pk_bf16_f32 v152, v176, v177\n"
    "v_cvt_pk_bf16_f32 v153, v178, v179\n"
    "v_cvt_pk_bf16_f32 v154, v180, v181\n"
    "v_cvt_pk_bf16_f32 v155, v182, v183\n"
    "v_cvt_pk_bf16_f32 v156, v184, v185\n"
    "v_cvt_pk_bf16_f32 v157, v186, v187\n"
    "v_cvt_pk_bf16_f32 v158, v188, v189\n"
    "v_cvt_pk_bf16_f32 v159, v190, v191\n"
    "v_cvt_pk_bf16_f32 v160, v192, v193\n"
    "buffer_store_dwordx4 v[116:119], v20, s[16:19], s86 offen offset:1024 sc1\n"
    "v_cvt_pk_bf16_f32 v161, v194, v195\n"
    "v_cvt_pk_bf16_f32 v162, v196, v197\n"
    "v_cvt_pk_bf16_f32 v163, v198, v199\n"
    "v_cvt_pk_bf16_f32 v164, v200, v201\n"
    "v_cvt_pk_bf16_f32 v165, v202, v203\n"
    "v_cvt_pk_bf16_f32 v166, v204, v205\n"
    "v_cvt_pk_bf16_f32 v167, v206, v207\n"
    "v_mfma_f32_32x32x16_bf16 v[176:191], a[168:171], v[152:155], 0\n"
    "v_lshlrev_b32_e64 v120, 16, v240\n"
    "v_and_b32 v121, 0xffff0000, v240\n"
    "v_lshlrev_b32_e64 v122, 16, v241\n"
    "v_and_b32 v123, 0xffff0000, v241\n"
    "v_mfma_f32_32x32x16_bf16 v[192:207], a[168:171], v[160:163], 0\n"
    "v_lshlrev_b32_e64 v136, 16, v242\n"
    "v_and_b32 v137, 0xffff0000, v242\n"
    "v_lshlrev_b32_e64 v138, 16, v243\n"
    "v_and_b32 v139, 0xffff0000, v243\n"
    "v_mfma_f32_32x32x16_bf16 v[176:191], v[208:211], v[160:163], v[176:191]\n"
    "v_lshlrev_b32_e64 v124, 16, v244\n"
    "v_and_b32 v125, 0xffff0000, v244\n"
    "v_lshlrev_b32_e64 v126, 16, v245\n"
    "buffer_load_dwordx4 a[4:7], v20, s[16:19], s85 offen offset:1024 sc1\n"
    "v_and_b32 v127, 0xffff0000, v245\n"
    "v_mfma_f32_32x32x16_bf16 v[192:207], a[176:179], v[152:155], v[192:207]\n"
    "v_lshlrev_b32_e64 v140, 16, v246\n"
    "v_and_b32 v141, 0xffff0000, v246\n"
    "v_lshlrev_b32_e64 v142, 16, v247\n"
    "v_and_b32 v143, 0xffff0000, v247\n"
    "v_mfma_f32_32x32x16_bf16 v[176:191], a[172:175], v[156:159], v[176:191]\n"
    "s_add_u32 s84, s55, 98304\n"
    "v_lshlrev_b32_e64 v128, 16, v248\n"
    "v_and_b32 v129, 0xffff0000, v248\n"
    "v_lshlrev_b32_e64 v130, 16, v249\n"
    "s_add_u32 m0, s36, 20480\n"
    "v_and_b32 v131, 0xffff0000, v249\n"
    "v_mfma_f32_32x32x16_bf16 v[192:207], a[172:175], v[164:167], v[192:207]\n"
    "buffer_load_dwordx4 v17, s[12:15], s84 offen lds\n"
    "v_lshlrev_b32_e64 v144, 16, v250\n"
    "v_and_b32 v145, 0xffff0000, v250\n"
    "v_lshlrev_b32_e64 v146, 16, v251\n"
    "v_and_b32 v147, 0xffff0000, v251\n"
    "v_mfma_f32_32x32x16_bf16 v[176:191], v[212:215], v[164:167], v[176:191]\n"
    "s_add_u32 s84, s58, 65536\n"
    "v_lshlrev_b32_e64 v132, 16, v252\n"
    "v_and_b32 v133, 0xffff0000, v252\n"
    "v_lshlrev_b32_e64 v134, 16, v253\n"
    "s_add_u32 m0, s36, 24576\n"
    "v_and_b32 v135, 0xffff0000, v253\n"
    "v_mfma_f32_32x32x16_bf16 v[192:207], a[180:183], v[156:159], v[192:207]\n"
    "buffer_load_dwordx4 v17, s[12:15], s84 offen lds\n"
    "v_lshlrev_b32_e64 v148, 16, v254\n"
    "v_and_b32 v149, 0xffff0000, v254\n"
    "v_lshlrev_b32_e64 v150, 16, v255\n"
    "v_and_b32 v151, 0xffff0000, v255\n"
    "s_cmp_eq_u32 s101, 0\n"
    "s_cbranch_scc1 LSPA_%=\n"
    "LSPAr_%=:\n"
    "v_pk_mul_f32 v[152:153], v[176:177], v[136:137]\n"
    "v_pk_mul_f32 v[176:177], v[176:177], v[120:121]\n"
    "s_add_u32 s84, s58, 98304\n"
    "s_nop 1\n"
    "s_nop 0\n"
    "v_pk_fma_f32 v[176:177], v[192:193], v[136:137], v[176:177] neg_lo:[0,1,0] neg_hi:[0,1,0]\n"
    "v_pk_fma_f32 v[192:193], v[192:193], v[120:121], v[152:153]\n"
    "v_pk_mul_f32 v[154:155], v[178:179], v[138:139]\n"
    "v_pk_mul_f32 v[178:179], v[178:179], v[122:123]\n"
    "s_add_u32 m0, s36, 28672\n"
    "v_pk_fma_f32 v[178:179], v[194:195], v[138:139], v[178:179] neg_lo:[0,1,0] neg_hi:[0,1,0]\n"
    "v_pk_fma_f32 v[194:195], v[194:195], v[122:123], v[154:155]\n"
    "v_pk_mul_f32 v[152:153], v[180:181], v[140:141]\n"
    "v_pk_mul_f32 v[180:181], v[180:181], v[124:125]\n"
    "buffer_load_dwordx4 v17, s[12:15], s84 offen lds\n"
    "v_pk_fma_f32 v[180:181], v[196:197], v[140:141], v[180:181] neg_lo:[0,1,0] neg_hi:[0,1,0]\n"
    "v_pk_fma_f32 v[196:197], v[196:197], v[124:125], v[152:153]\n"
    "v_pk_mul_f32 v[154:155], v[182:183], v[142:143]\n"
    "v_pk_mul_f32 v[182:183], v[182:183], v[126:127]\n"
    "v_pk_fma_f32 v[182:183], v[198:199], v[142:143], v[182:183] neg_lo:[0,1,0] neg_hi:[0,1,0]\n"
    "v_pk_fma_f32 v[198:199], v[198:199], v[126:127], v[154:155]\n"
    "v_pk_mul_f32 v[152:153], v[184:185], v[144:145]\n"
    "v_pk_mul_f32 v[184:185], v[184:185], v[128:129]\n"
    "v_pk_fma_f32 v[184:185], v[200:201], v[144:145], v[184:185] neg_lo:[0,1,0] neg_hi:[0,1,0]\n"
    "v_pk_fma_f32 v[200:201], v[200:201], v[128:129], v[152:153]\n"
    "v_pk_mul_f32 v[154:155], v[186:187], v[146:147]\n"
    "v_pk_mul_f32 v[186:187], v[186:187], v[130:131]\n"
    "s_add_u32 s85, s74, 0\n"
    "v_pk_fma_f32 v[186:187], v[202:203], v[146:147], v[186:187] neg_lo:[0,1,0] neg_hi:[0,1,0]\n"
    "v_pk_fma_f32 v[202:203], v[202:203], v[130:131], v[154:155]\n"
    "v_pk_mul_f32 v[152:153], v[188:189], v[148:149]\n"
    "v_pk_mul_f32 v[188:189], v[188:189], v[132:133]\n"
    "buffer_load_dwordx4 a[8:11], v20, s[16:19], s85 offen offset:0 sc1\n"
    "v_pk_fma_f32 v[188:189], v[204:205], v[148:149], v[188:189] neg_lo:[0,1,0] neg_hi:[0,1,0]\n"
    "v_pk_fma_f32 v[204:205], v[204:205], v[132:133], v[152:153]\n"
    "v_pk_mul_f32 v[154:155], v[190:191], v[150:151]\n"
    "v_pk_mul_f32 v[190:191], v[190:191], v[134:135]\n"
    "v_pk_fma_f32 v[190:191], v[206:207], v[150:151], v[190:191] neg_lo:[0,1,0] neg_hi:[0,1,0]\n"
    "v_pk_fma_f32 v[206:207], v[206:207], v[134:135], v[154:155]\n"
    "s_cmp_eq_u32 s101, 0\n"
    "s_cbranch_scc1 LSPB_%=\n"
    "s_nop 0\n"
    "LSPBr_%=:\n"
    "v_cvt_pk_bf16_f32 v152, v176, v177\n"
    "v_cvt_pk_bf16_f32 v153, v178, v179\n"
    "v_cvt_pk_bf16_f32 v154, v180, v181\n"
    "v_cvt_pk_bf16_f32 v155, v182, v183\n"
    "v_cvt_pk_bf16_f32 v156, v184, v185\n"
    "v_cvt_pk_bf16_f32 v157, v186, v187\n"
    "v_cvt_pk_bf16_f32 v158, v188, v189\n"
    "v_cvt_pk_bf16_f32 v159, v190, v191\n"
    "v_cvt_pk_bf16_f32 v160, v192, v193\n"
    "v_cvt_pk_bf16_f32 v161, v194, v195\n"
    "v_cvt_pk_bf16_f32 v162, v196, v197\n"
    "v_cvt_pk_bf16_f32 v163, v198, v199\n"
    "buffer_load_dwordx4 a[12:15], v20, s[16:19], s85 offen offset:1024 sc1\n"
    "v_cvt_pk_bf16_f32 v164, v200, v201\n"
    "v_cvt_pk_bf16_f32 v165, v202, v203\n"
    "v_cvt_pk_bf16_f32 v166, v204, v205\n"
    "v_cvt_pk_bf16_f32 v167, v206, v207\n"
    "v_mfma_f32_32x32x16_bf16 v[176:191], v[152:155], a[168:171], 0\n"
    "ds_read_b128 v[120:123], v218 offset:0\n"
    "v_mfma_f32_32x32x16_bf16 v[192:207], v[160:163], a[168:171], 0\n"
    "ds_read_b128 v[124:127], v218 offset:1024\n"
    "s_add_u32 s85, s77, 0\n"
    "s_nop 0\n"
    "buffer_load_dwordx4 a[16:19], v20, s[16:19], s85 offen offset:0 sc1\n"
    "v_mfma_f32_32x32x16_bf16 v[176:191], v[160:163], a[176:179], v[176:191]\n"
    "ds_read_b128 v[128:131], v218 offset:2048\n"
    "v_mfma_f32_32x32x16_bf16 v[192:207], v[152:155], v[208:211], v[192:207]\n"
    "ds_read_b128 v[132:135], v218 offset:3072\n"
    "buffer_load_dwordx4 a[20:23], v20, s[16:19], s85 offen offset:1024 sc1\n"
    "v_mfma_f32_32x32x16_bf16 v[176:191], v[156:159], a[172:175], v[176:191]\n"
    "ds_read_b128 v[136:139], v218 offset:4096\n"
    "v_mfma_f32_32x32x16_bf16 v[192:207], v[164:167], a[172:175], v[192:207]\n"
    "ds_read_b128 v[140:143], v218 offset:5120\n"
    "s_add_u32 s85, s71, 8192\n"
    "v_mfma_f32_32x32x16_bf16 v[176:191], v[164:167], a[180:183], v[176:191]\n"
    "ds_read_b128 v[144:147], v218 offset:6144\n"
    "buffer_load_dwordx4 a[24:27], v20, s[16:19], s85 offen offset:0 sc1\n"
    "v_mfma_f32_32x32x16_bf16 v[192:207], v[156:159], v[212:215], v[192:207]\n"
    "ds_read_b128 v[148:151], v218 offset:7168\n"
    "s_waitcnt lgkmcnt(3)\n"
    "s_nop 6\n"
    "v_pk_mul_f32 v[152:153], v[176:177], v[136:137]\n"
    "v_pk_mul_f32 v[176:177], v[176:177], v[120:121]\n"
    "s_nop 0\n"
    "v_pk_fma_f32 v[176:177], v[192:193], v[136:137], v[176:177] neg_lo:[0,1,0] neg_hi:[0,1,0]\n"
    "v_pk_fma_f32 v[192:193], v[192:193], v[120:121], v[152:153]\n"
    "v_pk_mul_f32 v[154:155], v[178:179], v[138:139]\n"
    "buffer_load_dwordx4 a[28:31], v20, s[16:19], s85 offen offset:1024 sc1\n"
    "v_pk_mul_f32 v[178:179], v[178:179], v[122:123]\n"
    "v_pk_fma_f32 v[178:179], v[194:195], v[138:139], v[178:179] neg_lo:[0,1,0] neg_hi:[0,1,0]\n"
    "v_pk_fma_f32 v[194:195], v[194:195], v[122:123], v[154:155]\n"
    "s_waitcnt lgkmcnt(2)\n"
    "v_pk_mul_f32 v[152:153], v[180:181], v[140:141]\n"
    "v_pk_mul_f32 v[180:181], v[180:181], v[124:125]\n"
    "v_pk_fma_f32 v[180:181], v[196:197], v[140:141], v[180:181] neg_lo:[0,1,0] neg_hi:[0,1,0]\n"
    "v_pk_fma_f32 v[196:197], v[196:197], v[124:125], v[152:153]\n"
    "v_pk_mul_f32 v[154:155], v[182:183], v[142:143]\n"
    "v_pk_mul_f32 v[182:183], v[182:183], v[126:127]\n"
    "v_pk_fma_f32 v[182:183], v[198:199], v[142:143], v[182:183] neg_lo:[0,1,0] neg_hi:[0,1,0]\n"
    "v_pk_fma_f32 v[198:199], v[198:199], v[126:127], v[154:155]\n"
    "s_waitcnt lgkmcnt(1)\n"
    "v_pk_mul_f32 v[152:153], v[184:185], v[144:145]\n"
    "v_pk_mul_f32 v[184:185], v[184:185], v[128:129]\n"
    "v_pk_fma_f32 v[184:185], v[200:201], v[144:145], v[184:185] neg_lo:[0,1,0] neg_hi:[0,1,0]\n"
    "v_pk_fma_f32 v[200:201], v[200:201], v[128:129], v[152:153]\n"
    "v_pk_mul_f32 v[154:155], v[186:187], v[146:147]\n"
    "s_add_u32 s85, s74, 8192\n"
    "v_pk_mul_f32 v[186:187], v[186:187], v[130:131]\n"
    "v_pk_fma_f32 v[186:187], v[202:203], v[146:147], v[186:187] neg_lo:[0,1,0] neg_hi:[0,1,0]\n"
    "v_pk_fma_f32 v[202:203], v[202:203], v[130:131], v[154:155]\n"
    "s_waitcnt lgkmcnt(0)\n"
    "v_pk_mul_f32 v[152:153], v[188:189], v[148:149]\n"
    "buffer_load_dwordx4 a[32:35], v20, s[16:19], s85 offen offset:0 sc1\n"
    "v_pk_mul_f32 v[188:189], v[188:189], v[132:133]\n"
    "v_pk_fma_f32 v[188:189], v[204:205], v[148:149], v[188:189] neg_lo:[0,1,0] neg_hi:[0,1,0]\n"
    "v_pk_fma_f32 v[204:205], v[204:205], v[132:133], v[152:153]\n"
    "v_pk_mul_f32 v[154:155], v[190:191], v[150:151]\n"
    "v_pk_mul_f32 v[190:191], v[190:191], v[134:135]\n"
    "v_pk_fma_f32 v[190:191], v[206:207], v[150:151], v[190:191] neg_lo:[0,1,0] neg_hi:[0,1,0]\n"
    "v_pk_fma_f32 v[206:207], v[206:207], v[134:135], v[154:155]\n"
    "v_cvt_pk_bf16_f32 v152, v176, v177\n"
    "v_cvt_pk_bf16_f32 v153, v178, v179\n"
    "v_cvt_pk_bf16_f32 v154, v180, v181\n"
    "v_cvt_pk_bf16_f32 v155, v182, v183\n"
    "v_cvt_pk_bf16_f32 v156, v184, v185\n"
    "v_cvt_pk_bf16_f32 v157, v186, v187\n"
    "v_cvt_pk_bf16_f32 v158, v188, v189\n"
    "v_cvt_pk_bf16_f32 v159, v190, v191\n"
    "v_cvt_pk_bf16_f32 v160, v192, v193\n"
    "v_cvt_pk_bf16_f32 v161, v194, v195\n"
    "v_cvt_pk_bf16_f32 v162, v196, v197\n"
    "v_cvt_pk_bf16_f32 v163, v198, v199\n"
    "v_cvt_pk_bf16_f32 v164, v200, v201\n"
    "buffer_load_dwordx4 a[36:39], v20, s[16:19], s85 offen offset:1024 sc1\n"
    "v_cvt_pk_bf16_f32 v165, v202, v203\n"
    "v_cvt_pk_bf16_f32 v166, v204, v205\n"
    "v_cvt_pk_bf16_f32 v167, v206, v207\n"
    "v_mfma_f32_32x32x16_bf16 v[176:191], a[168:171], v[152:155], 0\n"
    "s_waitcnt vmcnt(12)\n"
    "s_nop 0\n"
    "v_mfma_f32_32x32x16_bf16 v[192:207], a[168:171], v[160:163], 0\n"
    "s_mov_b32 s83, 12976128\n"
    "LPAD7_%=:\n"
    "s_cbranch_execz LPADR_%=\n"
    "buffer_store_dwordx2 v[60:61], v23, s[16:19], s83 offen sc1\n"
    "s_nop 0\n"
    "v_mfma_f32_32x32x16_bf16 v[176:191], a[176:179], v[160:163], v[176:191]\n"
    "v_mfma_f32_32x32x16_bf16 v[192:207], v[208:211], v[152:155], v[192:207]\n"
    "s_add_u32 s84, s61, 65536\n"
    "s_add_u32 m0, s36, 32768\n"
    "v_mfma_f32_32x32x16_bf16 v[176:191], a[172:175], v[156:159], v[176:191]\n"
    "buffer_load_dwordx4 v17, s[12:15], s84 offen lds\n"
    "v_mfma_f32_32x32x16_bf16 v[192:207], a[172:175], v[164:167], v[192:207]\n"
    "s_add_u32 s84, s61, 98304\n"
    "s_add_u32 m0, s36, 36864\n"
    "v_mfma_f32_32x32x16_bf16 v[176:191], a[180:183], v[164:167], v[176:191]\n"
    "buffer_load_dwordx4 v17, s[12:15], s84 offen lds\n"
    "v_mfma_f32_32x32x16_bf16 v[192:207], v[212:215], v[156:159], v[192:207]\n"
    "s_barrier\n"
    "s_add_u32 s84, s55, 131072\n"
    "s_add_u32 m0, s36, 40960\n"
    "s_nop 0\n"
    "buffer_load_dwordx4 v17, s[12:15], s84 offen lds\n"
    "s_nop 6\n"
    "s_nop 0\n"
    "v_cvt_pk_bf16_f32 v240, v176, v192\n"
    "ds_write_b32 v219, v240 offset:0\n"
    "v_cvt_pk_bf16_f32 v241, v177, v193\n"
    "ds_write_b32 v220, v241 offset:4096\n"
    "v_cvt_pk_bf16_f32 v242, v178, v194\n"
    "ds_write_b32 v221, v242 offset:8192\n"
    "s_add_u32 s84, s55, 163840\n"
    "v_cvt_pk_bf16_f32 v243, v179, v195\n"
    "ds_write_b32 v222, v243 offset:12288\n"
    "s_add_u32 m0, s36, 45056\n"
    "v_cvt_pk_bf16_f32 v244, v180, v196\n"
    "ds_write_b32 v219, v244 offset:256\n"
    "v_cvt_pk_bf16_f32 v245, v181, v197\n"
    "buffer_load_dwordx4 v17, s[12:15], s84 offen lds\n"
    "ds_write_b32 v220, v245 offset:4352\n"
    "v_cvt_pk_bf16_f32 v246, v182, v198\n"
    "ds_write_b32 v221, v246 offset:8448\n"
    "v_cvt_pk_bf16_f32 v247, v183, v199\n"
    "ds_write_b32 v222, v247 offset:12544\n"
    "v_cvt_pk_bf16_f32 v248, v184, v200\n"
    "ds_write_b32 v219, v248 offset:512\n"
    "v_cvt_pk_bf16_f32 v249, v185, v201\n"
    "ds_write_b32 v220, v249 offset:4608\n"
    "v_cvt_pk_bf16_f32 v250, v186, v202\n"
    "ds_write_b32 v221, v250 offset:8704\n"
    "s_add_u32 s85, s77, 8192\n"
    "v_cvt_pk_bf16_f32 v251, v187, v203\n"
    "ds_write_b32 v222, v251 offset:12800\n"
    "buffer_load_dwordx4 a[40:43], v20, s[16:19], s85 offen offset:0 sc1\n"
    "v_cvt_pk_bf16_f32 v252, v188, v204\n"
    "ds_write_b32 v219, v252 offset:768\n"
    "v_cvt_pk_bf16_f32 v253, v189, v205\n"
    "ds_write_b32 v220, v253 offset:4864\n"
    "v_cvt_pk_bf16_f32 v254, v190, v206\n"
    "ds_write_b32 v221, v254 offset:8960\n"
    "v_cvt_pk_bf16_f32 v255, v191, v207\n"
    "ds_write_b32 v222, v255 offset:13056\n"
    "s_waitcnt lgkmcnt(0)\n"
    "s_barrier\n"
    "buffer_load_dwordx4 a[44:47], v20, s[16:19], s85 offen offset:1024 sc1\n"
    "ds_read2st64_b32 v[240:241], v223 offset0:0 offset1:4\n"
    "ds_read2st64_b32 v[242:243], v223 offset0:8 offset1:12\n"
    "ds_read2st64_b32 v[244:245], v223 offset0:1 offset1:5\n"
    "ds_read2st64_b32 v[246:247], v223 offset0:9 offset1:13\n"
    "ds_read2st64_b32 v[248:249], v223 offset0:2 offset1:6\n"
    "ds_read2st64_b32 v[250:251], v223 offset0:10 offset1:14\n"
    "ds_read2st64_b32 v[252:253], v223 offset0:3 offset1:7\n"
    "ds_read2st64_b32 v[254:255], v223 offset0:11 offset1:15\n"
    "s_add_u32 s85, s71, 16384\n"
    "buffer_load_dwordx4 a[48:51], v20, s[16:19], s85 offen offset:0 sc1\n"
    "v_mfma_f32_16x16x32_bf16 v[176:179], v[32:35], v[64:67], 0\n"
    "ds_read_b64_tr_b16 v[52:53], v5 offset:512\n"
    "v_mfma_f32_16x16x32_bf16 v[180:183], v[32:35], v[68:71], 0\n"
    "ds_read_b64_tr_b16 v[54:55], v5 offset:4608\n"
    "ds_read_b64_tr_b16 v[56:57], v4 offset:768\n"
    "v_mfma_f32_16x16x32_bf16 v[208:211], v[36:39], v[64:67], 0\n"
    "buffer_load_dwordx4 a[52:55], v20, s[16:19], s85 offen offset:1024 sc1\n"
    "v_mfma_f32_16x16x32_bf16 v[212:215], v[36:39], v[68:71], 0\n"
    "s_waitcnt lgkmcnt(3)\n"
    "ds_read_b64_tr_b16 v[58:59], v4 offset:4864\n"
    "v_lshlrev_b32 v14, 4, v30\n"
    "v_mfma_f32_16x16x32_bf16 v[184:187], v[40:43], v[64:67], 0\n"
    "ds_read_b64_tr_b16 v[32:33], v5 offset:768\n"
    "v_add_u32 v14, 0x20000, v14\n"
    "v_mfma_f32_16x16x32_bf16 v[188:191], v[40:43], v[68:71], 0\n"
    "s_lshl_b32 s101, s48, 8\n"
    "ds_read_b64_tr_b16 v[34:35], v5 offset:4864\n"
    "s_and_b32 s89, s100, 1\n"
    "v_mfma_f32_16x16x32_bf16 v[216:219], v[44:47], v[64:67], 0\n"
    "s_add_u32 s97, s29, 0\n"
    "ds_read_b64_tr_b16 v[36:37], v4 offset:8192\n"
    "ds_read_b64_tr_b16 v[38:39], v4 offset:12288\n"
    "v_mfma_f32_16x16x32_bf16 v[220:223], v[44:47], v[68:71], 0\n"
    "s_waitcnt lgkmcnt(6)\n"
    "buffer_store_dwordx4 v[240:243], v26, s[16:19], s97 offen sc1\n"
    "v_mfma_f32_16x16x32_bf16 v[192:195], v[48:51], v[64:67], 0\n"
    "s_lshl_b32 s89, s89, 7\n"
    "ds_read_b64_tr_b16 v[40:41], v5 offset:8192\n"
    "v_mfma_f32_16x16x32_bf16 v[196:199], v[48:51], v[68:71], 0\n"
    "s_add_u32 s101, s101, s89\n"
    "ds_read_b64_tr_b16 v[42:43], v5 offset:12288\n"
    "v_mfma_f32_16x16x32_bf16 v[224:227], v[52:55], v[64:67], 0\n"
    "s_bfe_u32 s89, s100, 0x30003\n"
    "ds_read_b64_tr_b16 v[44:45], v4 offset:8448\n"
    "s_add_u32 s101, s101, s89\n"
    "v_mfma_f32_16x16x32_bf16 v[228:231], v[52:55], v[68:71], 0\n"
    "s_waitcnt lgkmcnt(5)\n"
    "ds_read_b64_tr_b16 v[46:47], v4 offset:12544\n"
    "v_mfma_f32_16x16x32_bf16 v[200:203], v[56:59], v[64:67], 0\n"
    "s_lshl_b32 s101, s101, 11\n"
    "ds_read_b64_tr_b16 v[48:49], v5 offset:8448\n"
    "s_add_u32 s101, s101, s46\n"
    "v_mfma_f32_16x16x32_bf16 v[204:207], v[56:59], v[68:71], 0\n"
    "s_add_u32 s97, s29, 1048576\n"
    "ds_read_b64_tr_b16 v[50:51], v5 offset:12544\n"
    "ds_read_b64_tr_b16 v[52:53], v4 offset:8704\n"
    "v_mfma_f32_16x16x32_bf16 v[232:235], v[32:35], v[64:67], 0\n"
    "s_add_u32 s93, s36, 0x20000\n"
    "buffer_store_dwordx4 v[244:247], v26, s[16:19], s97 offen sc1\n"
    "v_mfma_f32_16x16x32_bf16 v[236:239], v[32:35], v[68:71], 0\n"
    "s_waitcnt lgkmcnt(5)\n"
    "ds_read_b64_tr_b16 v[54:55], v4 offset:12800\n"
    "v_bfe_u32 v21, v30, 3, 2\n"
    "v_mfma_f32_16x16x32_bf16 v[176:179], v[36:39], v[72:75], v[176:179]\n"
    "s_nop 0\n"
    "ds_read_b64_tr_b16 v[56:57], v5 offset:8704\n"
    "v_lshlrev_b32_e64 v21, 5, v21\n"
    "v_mfma_f32_16x16x32_bf16 v[180:183], v[36:39], v[76:79], v[180:183]\n"
    "ds_read_b64_tr_b16 v[58:59], v5 offset:12800\n"
    "v_lshrrev_b32_e64 v161, 5, v30\n"
    "v_mfma_f32_16x16x32_bf16 v[208:211], v[40:43], v[72:75], v[208:211]\n"
    "ds_read_b64_tr_b16 v[32:33], v4 offset:8960\n"
    "v_lshl_add_u32 v21, v161, 10, v21\n"
    "v_mfma_f32_16x16x32_bf16 v[212:215], v[40:43], v[76:79], v[212:215]\n"
    "s_waitcnt lgkmcnt(5)\n"
    "ds_read_b64_tr_b16 v[34:35], v4 offset:13056\n"
    "v_lshl_add_u32 v21, v21, 11, s101\n"
    "v_mfma_f32_16x16x32_bf16 v[184:187], v[44:47], v[72:75], v[184:187]\n"
    "s_add_u32 s97, s29, 2097152\n"
    "ds_read_b64_tr_b16 v[36:37], v5 offset:8960\n"
    "ds_read_b64_tr_b16 v[38:39], v5 offset:13056\n"
    "v_mfma_f32_16x16x32_bf16 v[188:191], v[44:47], v[76:79], v[188:191]\n"
    "s_nop 0\n"
    "buffer_store_dwordx4 v[248:251], v26, s[16:19], s97 offen sc1\n"
    "v_mfma_f32_16x16x32_bf16 v[216:219], v[48:51], v[72:75], v[216:219]\n"
    "ds_read_b64_tr_b16 v[40:41], v4 offset:16384\n"
    "v_lshrrev_b32_e64 v161, 3, v30\n"
    "v_mfma_f32_16x16x32_bf16 v[220:223], v[48:51], v[76:79], v[220:223]\n"
    "s_waitcnt lgkmcnt(5)\n"
    "ds_read_b64_tr_b16 v[42:43], v4 offset:20480\n"
    "v_lshrrev_b32 v161, 1, v161\n"
    "v_mfma_f32_16x16x32_bf16 v[192:195], v[52:55], v[72:75], v[192:195]\n"
    "ds_read_b64_tr_b16 v[44:45], v5 offset:16384\n"
    "v_and_b32_e64 v160, 7, v30\n"
    "v_mfma_f32_16x16x32_bf16 v[196:199], v[52:55], v[76:79], v[196:199]\n"
    "ds_read_b64_tr_b16 v[46:47], v5 offset:20480\n"
    "v_xor_b32_e64 v161, v160, v161\n"
    "v_mfma_f32_16x16x32_bf16 v[224:227], v[56:59], v[72:75], v[224:227]\n"
    "s_add_u32 s85, s74, 16384\n"
    "ds_read_b64_tr_b16 v[48:49], v4 offset:16640\n"
    "v_lshl_add_u32 v21, v161, 4, v21\n"
    "v_mfma_f32_16x16x32_bf16 v[228:231], v[56:59], v[76:79], v[228:231]\n"
    "s_waitcnt lgkmcnt(5)\n"
    "ds_read_b64_tr_b16 v[50:51], v4 offset:20736\n"
    "ds_read_b64_tr_b16 v[52:53], v5 offset:16640\n"
    "v_mfma_f32_16x16x32_bf16 v[200:203], v[32:35], v[72:75], v[200:203]\n"
    "s_nop 0\n"
    "buffer_load_dwordx4 a[56:59], v20, s[16:19], s85 offen offset:0 sc1\n"
    "v_mfma_f32_16x16x32_bf16 v[204:207], v[32:35], v[76:79], v[204:207]\n"
    "ds_read_b64_tr_b16 v[54:55], v5 offset:20736\n"
    "v_lshrrev_b32_e64 v15, 5, v30\n"
    "v_mfma_f32_16x16x32_bf16 v[232:235], v[36:39], v[72:75], v[232:235]\n"
    "ds_read_b64_tr_b16 v[56:57], v4 offset:16896\n"
    "v_lshlrev_b32_e64 v15, 11, v15\n"
    "v_mfma_f32_16x16x32_bf16 v[236:239], v[36:39], v[76:79], v[236:239]\n"
    "s_waitcnt lgkmcnt(5)\n"
    "ds_read_b64_tr_b16 v[58:59], v4 offset:20992\n"
    "v_and_b32 v161, 31, v30\n"
    "v_mfma_f32_16x16x32_bf16 v[176:179], v[40:43], v[80:83], v[176:179]\n"
    "s_add_u32 s97, s29, 3145728\n"
    "ds_read_b64_tr_b16 v[32:33], v5 offset:16896\n"
    "ds_read_b64_tr_b16 v[34:35], v5 offset:20992\n"
    "v_mfma_f32_16x16x32_bf16 v[180:183], v[40:43], v[84:87], v[180:183]\n"
    "buffer_store_dwordx4 v[252:255], v26, s[16:19], s97 offen sc1\n"
    "v_mfma_f32_16x16x32_bf16 v[208:211], v[44:47], v[80:83], v[208:211]\n"
    "ds_read_b64_tr_b16 v[36:37], v4 offset:17152\n"
    "v_lshl_add_u32 v15, v161, 6, v15\n"
    "v_mfma_f32_16x16x32_bf16 v[212:215], v[44:47], v[84:87], v[212:215]\n"
    "s_waitcnt lgkmcnt(5)\n"
    "ds_read_b64_tr_b16 v[38:39], v4 offset:21248\n"
    "v_bfe_u32 v161, v30, 2, 2\n"
    "v_mfma_f32_16x16x32_bf16 v[184:187], v[48:51], v[80:83], v[184:187]\n"
    "s_nop 0\n"
    "ds_read_b64_tr_b16 v[40:41], v5 offset:17152\n"
    "v_xor_b32_e64 v161, s48, v161\n"
    "v_mfma_f32_16x16x32_bf16 v[188:191], v[48:51], v[84:87], v[188:191]\n"
    "ds_read_b64_tr_b16 v[42:43], v5 offset:21248\n"
    "v_lshl_add_u32 v15, v161, 4, v15\n"
    "v_mfma_f32_16x16x32_bf16 v[216:219], v[52:55], v[80:83], v[216:219]\n"
    "ds_read_b64_tr_b16 v[44:45], v4 offset:24576\n"
    "v_add_u32 v15, 0x24000, v15\n"
    "v_mfma_f32_16x16x32_bf16 v[220:223], v[52:55], v[84:87], v[220:223]\n"
    "s_waitcnt lgkmcnt(5)\n"
    "ds_read_b64_tr_b16 v[46:47], v4 offset:28672\n"
    "ds_read_b64_tr_b16 v[48:49], v5 offset:24576\n"
    "v_mfma_f32_16x16x32_bf16 v[192:195], v[56:59], v[80:83], v[192:195]\n"
    "s_nop 0\n"
    "buffer_load_dwordx4 a[60:63], v20, s[16:19], s85 offen offset:1024 sc1\n"
    "v_mfma_f32_16x16x32_bf16 v[196:199], v[56:59], v[84:87], v[196:199]\n"
    "ds_read_b64_tr_b16 v[50:51], v5 offset:28672\n"
    "v_lshrrev_b32_e64 v16, 5, v30\n"
    "v_mfma_f32_16x16x32_bf16 v[224:227], v[32:35], v[80:83], v[224:227]\n"
    "ds_read_b64_tr_b16 v[52:53], v4 offset:24832\n"
    "v_add_u32_e64 v16, -1, v16\n"
    "v_mfma_f32_16x16x32_bf16 v[228:231], v[32:35], v[84:87], v[228:231]\n"
    "s_waitcnt lgkmcnt(5)\n"
    "ds_read_b64_tr_b16 v[54:55], v4 offset:28928\n"
    "v_lshlrev_b32 v161, 31, v30\n"
    "v_mfma_f32_16x16x32_bf16 v[200:203], v[36:39], v[80:83], v[200:203]\n"
    "s_add_u32 s85, s77, 16384\n"
    "ds_read_b64_tr_b16 v[56:57], v5 offset:24832\n"
    "ds_read_b64_tr_b16 v[58:59], v5 offset:28928\n"
    "v_mfma_f32_16x16x32_bf16 v[204:207], v[36:39], v[84:87], v[204:207]\n"
    "buffer_load_dwordx4 a[64:67], v20, s[16:19], s85 offen offset:0 sc1\n"
    "v_mfma_f32_16x16x32_bf16 v[232:235], v[40:43], v[80:83], v[232:235]\n"
    "ds_read_b64_tr_b16 v[32:33], v4 offset:25088\n"
    "v_or_b32_e64 v161, 0x3f800000, v161\n"
    "v_mfma_f32_16x16x32_bf16 v[236:239], v[40:43], v[84:87], v[236:239]\n"
    "s_waitcnt lgkmcnt(5)\n"
    "ds_read_b64_tr_b16 v[34:35], v4 offset:29184\n"
    "v_and_b32 v16, v161, v16\n"
    "v_mfma_f32_16x16x32_bf16 v[176:179], v[44:47], v[88:91], v[176:179]\n"
    "s_lshr_b32 s94, s36, 6\n"
    "ds_read_b64_tr_b16 v[36:37], v5 offset:25088\n"
    "v_mfma_f32_16x16x32_bf16 v[180:183], v[44:47], v[92:95], v[180:183]\n"
    "s_add_u32 s94, s94, 0x24000\n"
    "ds_read_b64_tr_b16 v[38:39], v5 offset:29184\n"
    "ds_read_b64_tr_b16 v[40:41], v4 offset:25344\n"
    "v_mfma_f32_16x16x32_bf16 v[208:211], v[48:51], v[88:91], v[208:211]\n"
    "s_add_u32 s84, s58, 131072\n"
    "buffer_load_dwordx4 a[68:71], v20, s[16:19], s85 offen offset:1024 sc1\n"
    "v_mfma_f32_16x16x32_bf16 v[212:215], v[48:51], v[92:95], v[212:215]\n"
    "s_waitcnt lgkmcnt(5)\n"
    "ds_read_b64_tr_b16 v[42:43], v4 offset:29440\n"
    "v_mov_b32_e64 v172, s94\n"
    "v_mfma_f32_16x16x32_bf16 v[184:187], v[52:55], v[88:91], v[184:187]\n"
    "s_barrier\n"
    "ds_read_b64_tr_b16 v[44:45], v5 offset:25344\n"
    "v_lshlrev_b32 v11, 7, v30\n"
    "v_mfma_f32_16x16x32_bf16 v[188:191], v[52:55], v[92:95], v[188:191]\n"
    "s_add_u32 m0, s36, 49152\n"
    "ds_read_b64_tr_b16 v[46:47], v5 offset:29440\n"
    "ds_read_b64_tr_b16 v[48:49], v4 offset:32768\n"
    "v_mfma_f32_16x16x32_bf16 v[216:219], v[56:59], v[88:91], v[216:219]\n"
    "buffer_load_dwordx4 v17, s[12:15], s84 offen lds\n"
    "v_mfma_f32_16x16x32_bf16 v[220:223], v[56:59], v[92:95], v[220:223]\n"
    "s_waitcnt lgkmcnt(5)\n"
    "ds_read_b64_tr_b16 v[50:51], v4 offset:36864\n"
    "v_and_b32 v161, 7, v30\n"
    "v_mfma_f32_16x16x32_bf16 v[192:195], v[32:35], v[88:91], v[192:195]\n"
    "ds_read_b64_tr_b16 v[52:53], v5 offset:32768\n"
    "v_xor_b32_e64 v161, s48, v161\n"
    "v_mfma_f32_16x16x32_bf16 v[196:199], v[32:35], v[92:95], v[196:199]\n"
    "ds_read_b64_tr_b16 v[54:55], v5 offset:36864\n"
    "v_lshl_add_u32 v11, v161, 4, v11\n"
    "v_mfma_f32_16x16x32_bf16 v[224:227], v[36:39], v[88:91], v[224:227]\n"
    "s_mov_b32 s87, 12976128\n"
    "ds_read_b64_tr_b16 v[56:57], v4 offset:33024\n"
    "ds_read_b64_tr_b16 v[58:59], v4 offset:37120\n"
    "v_mfma_f32_16x16x32_bf16 v[228:231], v[36:39], v[92:95], v[228:231]\n"
    "s_waitcnt lgkmcnt(6)\n"
    "buffer_load_dwordx2 v[62:63], v24, s[16:19], s87 offen sc1\n"
    "v_mfma_f32_16x16x32_bf16 v[200:203], v[40:43], v[88:91], v[200:203]\n"
    "s_nop 0\n"
    "ds_read_b64_tr_b16 v[32:33], v5 offset:33024\n"
    "v_add_u32 v11, 0x24000, v11\n"
    "v_mfma_f32_16x16x32_bf16 v[204:207], v[40:43], v[92:95], v[204:207]\n"
    "s_add_u32 s84, s58, 163840\n"
    "ds_read_b64_tr_b16 v[34:35], v5 offset:37120\n"
    "v_xor_b32_e64 v12, 64, v11\n"
    "v_mfma_f32_16x16x32_bf16 v[232:235], v[44:47], v[88:91], v[232:235]\n"
    "s_add_u32 m0, s36, 53248\n"
    "ds_read_b64_tr_b16 v[36:37], v4 offset:33280\n"
    "ds_read_b64_tr_b16 v[38:39], v4 offset:37376\n"
    "v_mfma_f32_16x16x32_bf16 v[236:239], v[44:47], v[92:95], v[236:239]\n"
    "s_waitcnt lgkmcnt(6)\n"
    "buffer_load_dwordx4 v17, s[12:15], s84 offen lds\n"
    "v_mfma_f32_16x16x32_bf16 v[176:179], v[48:51], v[96:99], v[176:179]\n"
    "s_nop 0\n"
    "ds_read_b64_tr_b16 v[40:41], v5 offset:33280\n"
    "ds_read_b128 a[216:219], v14 offset:12288\n"
    "v_mfma_f32_16x16x32_bf16 v[180:183], v[48:51], v[100:103], v[180:183]\n"
    "s_add_u32 s84, s61, 131072\n"
    "ds_read_b64_tr_b16 v[42:43], v5 offset:37376\n"
    "ds_read_b128 a[220:223], v14 offset:13312\n"
    "v_mfma_f32_16x16x32_bf16 v[208:211], v[52:55], v[96:99], v[208:211]\n"
    "s_add_u32 m0, s36, 57344\n"
    "ds_read_b64_tr_b16 v[44:45], v4 offset:33536\n"
    "ds_read_b128 a[224:227], v14 offset:14336\n"
    "v_mfma_f32_16x16x32_bf16 v[212:215], v[52:55], v[100:103], v[212:215]\n"
    "s_waitcnt lgkmcnt(8)\n"
    "ds_read_b64_tr_b16 v[46:47], v4 offset:37632\n"
    "ds_read_b64_tr_b16 v[48:49], v5 offset:33536\n"
    "v_mfma_f32_16x16x32_bf16 v[184:187], v[56:59], v[96:99], v[184:187]\n"
    "s_nop 0\n"
    "buffer_load_dwordx4 v17, s[12:15], s84 offen lds\n"
    "v_mfma_f32_16x16x32_bf16 v[188:191], v[56:59], v[100:103], v[188:191]\n"
    "s_add_u32 s84, s61, 163840\n"
    "ds_read_b64_tr_b16 v[50:51], v5 offset:37632\n"
    "ds_read_b128 a[228:231], v14 offset:15360\n"
    "v_mfma_f32_16x16x32_bf16 v[216:219], v[32:35], v[96:99], v[216:219]\n"
    "s_add_u32 m0, s36, 61440\n"
    "ds_read_b64_tr_b16 v[52:53], v4 offset:40960\n"
    "v_lshrrev_b32_e64 v13, 3, v30\n"
    "v_mfma_f32_16x16x32_bf16 v[220:223], v[32:35], v[100:103], v[220:223]\n"
    "s_waitcnt lgkmcnt(8)\n"
    "ds_read_b64_tr_b16 v[54:55], v4 offset:45056\n"
    "ds_read_b64_tr_b16 v[56:57], v5 offset:40960\n"
    "v_mfma_f32_16x16x32_bf16 v[192:195], v[36:39], v[96:99], v[192:195]\n"
    "s_lshl_b32 s94, s36, 2\n"
    "buffer_load_dwordx4 v17, s[12:15], s84 offen lds\n"
    "v_mfma_f32_16x16x32_bf16 v[196:199], v[36:39], v[100:103], v[196:199]\n"
    "s_add_u32 s94, s94, 0x24000\n"
    "ds_read_b64_tr_b16 v[58:59], v5 offset:45056\n"
    "v_lshl_add_u32 v13, v13, 7, s94\n"
    "v_mfma_f32_16x16x32_bf16 v[224:227], v[40:43], v[96:99], v[224:227]\n"
    "s_add_u32 s84, s55, 196608\n"
    "ds_read_b64_tr_b16 v[32:33], v4 offset:41216\n"
    "v_lshrrev_b32_e64 v161, 3, v30\n"
    "v_mfma_f32_16x16x32_bf16 v[228:231], v[40:43], v[100:103], v[228:231]\n"
    "s_waitcnt lgkmcnt(6)\n"
    "ds_read_b64_tr_b16 v[34:35], v4 offset:45312\n"
    "v_and_b32 v160, 7, v30\n"
    "v_mfma_f32_16x16x32_bf16 v[200:203], v[44:47], v[96:99], v[200:203]\n"
    "s_add_u32 m0, s36, 65536\n"
    "ds_read_b64_tr_b16 v[36:37], v5 offset:41216\n"
    "ds_read_b64_tr_b16 v[38:39], v5 offset:45312\n"
    "v_mfma_f32_16x16x32_bf16 v[204:207], v[44:47], v[100:103], v[204:207]\n"
    "buffer_load_dwordx4 v17, s[12:15], s84 offen lds\n"
    "v_mfma_f32_16x16x32_bf16 v[232:235], v[48:51], v[96:99], v[232:235]\n"
    "s_add_u32 s84, s55, 229376\n"
    "ds_read_b64_tr_b16 v[40:41], v4 offset:41472\n"
    "v_xor_b32_e64 v161, v160, v161\n"
    "v_mfma_f32_16x16x32_bf16 v[236:239], v[48:51], v[100:103], v[236:239]\n"
    "s_waitcnt lgkmcnt(5)\n"
    "ds_read_b64_tr_b16 v[42:43], v4 offset:45568\n"
    "v_lshl_add_u32 v13, v161, 4, v13\n"
    "v_mfma_f32_16x16x32_bf16 v[176:179], v[52:55], v[104:107], v[176:179]\n"
    "s_add_u32 m0, s36, 69632\n"
    "ds_read_b64_tr_b16 v[44:45], v5 offset:41472\n"
    "ds_read_b64_tr_b16 v[46:47], v5 offset:45568\n"
    "v_mfma_f32_16x16x32_bf16 v[180:183], v[52:55], v[108:111], v[180:183]\n"
    "s_nop 0\n"
    "buffer_load_dwordx4 v17, s[12:15], s84 offen lds\n"
    "v_mfma_f32_16x16x32_bf16 v[208:211], v[56:59], v[104:107], v[208:211]\n"
    "ds_read_b64_tr_b16 v[48:49], v4 offset:41728\n"
    "v_lshrrev_b32_e64 v10, 4, v30\n"
    "v_mfma_f32_16x16x32_bf16 v[212:215], v[56:59], v[108:111], v[212:215]\n"
    "s_waitcnt lgkmcnt(5)\n"
    "ds_read_b64_tr_b16 v[50:51], v4 offset:45824\n"
    "v_lshlrev_b32 v10, 15, v10\n"
    "v_mfma_f32_16x16x32_bf16 v[184:187], v[32:35], v[104:107], v[184:187]\n"
    "s_add_u32 s84, s58, 196608\n"
    "ds_read_b64_tr_b16 v[52:53], v5 offset:41728\n"
    "v_bfe_u32 v161, v30, 3, 1\n"
    "v_mfma_f32_16x16x32_bf16 v[188:191], v[32:35], v[108:111], v[188:191]\n"
    "s_add_u32 m0, s36, 73728\n"
    "ds_read_b64_tr_b16 v[54:55], v5 offset:45824\n"
    "ds_read_b64_tr_b16 v[56:57], v4 offset:49152\n"
    "v_mfma_f32_16x16x32_bf16 v[216:219], v[36:39], v[104:107], v[216:219]\n"
    "buffer_load_dwordx4 v17, s[12:15], s84 offen lds\n"
    "v_mfma_f32_16x16x32_bf16 v[220:223], v[36:39], v[108:111], v[220:223]\n"
    "s_waitcnt lgkmcnt(5)\n"
    "ds_read_b64_tr_b16 v[58:59], v4 offset:53248\n"
    "v_lshl_add_u32 v10, v161, 13, v10\n"
    "v_mfma_f32_16x16x32_bf16 v[192:195], v[40:43], v[104:107], v[192:195]\n"
    "s_add_u32 s84, s58, 229376\n"
    "ds_read_b64_tr_b16 v[32:33], v5 offset:49152\n"
    "v_and_b32 v161, 7, v30\n"
    "v_mfma_f32_16x16x32_bf16 v[196:199], v[40:43], v[108:111], v[196:199]\n"
    "s_add_u32 m0, s36, 77824\n"
    "ds_read_b64_tr_b16 v[34:35], v5 offset:53248\n"
    "ds_read_b64_tr_b16 v[36:37], v4 offset:49408\n"
    "v_mfma_f32_16x16x32_bf16 v[224:227], v[44:47], v[104:107], v[224:227]\n"
    "buffer_load_dwordx4 v17, s[12:15], s84 offen lds\n"
    "v_mfma_f32_16x16x32_bf16 v[228:231], v[44:47], v[108:111], v[228:231]\n"
    "s_waitcnt lgkmcnt(5)\n"
    "ds_read_b64_tr_b16 v[38:39], v4 offset:53504\n"
    "v_lshl_add_u32 v10, v161, 4, v10\n"
    "v_mfma_f32_16x16x32_bf16 v[200:203], v[48:51], v[104:107], v[200:203]\n"
    "s_nop 0\n"
    "ds_read_b64_tr_b16 v[40:41], v5 offset:49408\n"
    "v_mfma_f32_16x16x32_bf16 v[204:207], v[48:51], v[108:111], v[204:207]\n"
    "ds_read_b64_tr_b16 v[42:43], v5 offset:53504\n"
    "v_mfma_f32_16x16x32_bf16 v[232:235], v[52:55], v[104:107], v[232:235]\n"
    "s_add_u32 s85, s71, 24576\n"
    "ds_read_b64_tr_b16 v[44:45], v4 offset:49664\n"
    "ds_read_b64_tr_b16 v[46:47], v4 offset:53760\n"
    "v_mfma_f32_16x16x32_bf16 v[236:239], v[52:55], v[108:111], v[236:239]\n"
    "s_waitcnt vmcnt(12) lgkmcnt(6)\n"
    "buffer_load_dwordx4 a[72:75], v20, s[16:19], s85 offen offset:0 sc1\n"
    "v_mfma_f32_16x16x32_bf16 v[176:179], v[56:59], v[112:115], v[176:179]\n"
    "s_barrier\n"
    "ds_read_b64_tr_b16 v[48:49], v5 offset:49664\n"
    "v_mfma_f32_16x16x32_bf16 v[180:183], v[56:59], v[116:119], v[180:183]\n"
    "ds_read_b64_tr_b16 v[50:51], v5 offset:53760\n"
    "v_mfma_f32_16x16x32_bf16 v[208:211], v[32:35], v[112:115], v[208:211]\n"
    "s_add_u32 s83, s96, 13631488\n"
    "ds_read_b64_tr_b16 v[52:53], v4 offset:49920\n"
    "v_mfma_f32_16x16x32_bf16 v[212:215], v[32:35], v[116:119], v[212:215]\n"
    "s_waitcnt lgkmcnt(5)\n"
    "ds_read_b64_tr_b16 v[54:55], v4 offset:54016\n"
    "ds_read_b64_tr_b16 v[56:57], v5 offset:49920\n"
    "v_mfma_f32_16x16x32_bf16 v[184:187], v[36:39], v[112:115], v[184:187]\n"
    "s_nop 0\n"
    "buffer_store_dwordx2 v[60:61], off, s[16:19], s83 sc1\n"
    "v_mfma_f32_16x16x32_bf16 v[188:191], v[36:39], v[116:119], v[188:191]\n"
    "ds_read_b64_tr_b16 v[58:59], v5 offset:54016\n"
    "v_mfma_f32_16x16x32_bf16 v[216:219], v[40:43], v[112:115], v[216:219]\n"
    "ds_read_b64_tr_b16 v[32:33], v4 offset:57344\n"
    "v_mfma_f32_16x16x32_bf16 v[220:223], v[40:43], v[116:119], v[220:223]\n"
    "s_waitcnt vmcnt(9) lgkmcnt(5)\n"
    "ds_read_b64_tr_b16 v[34:35], v4 offset:61440\n"
    "ds_read_b64_tr_b16 v[36:37], v5 offset:57344\n"
    "v_cmp_ne_u64 vcc, s[98:99], v[62:63]\n"
    "v_mfma_f32_16x16x32_bf16 v[192:195], v[44:47], v[112:115], v[192:195]\n"
    "buffer_load_dwordx4 a[76:79], v20, s[16:19], s85 offen offset:1024 sc1\n"
    "v_mfma_f32_16x16x32_bf16 v[196:199], v[44:47], v[116:119], v[196:199]\n"
    "ds_read_b64_tr_b16 v[38:39], v5 offset:61440\n"
    "s_cbranch_vccnz LPLH3_%=s\n"
    "LPLH3_%=:\n"
    "v_mfma_f32_16x16x32_bf16 v[224:227], v[48:51], v[112:115], v[224:227]\n"
    "s_nop 0\n"
    "ds_read_b64_tr_b16 v[40:41], v4 offset:57600\n"
    "v_mfma_f32_16x16x32_bf16 v[228:231], v[48:51], v[116:119], v[228:231]\n"
    "s_waitcnt lgkmcnt(5)\n"
    "ds_read_b64_tr_b16 v[42:43], v4 offset:61696\n"
    "v_mfma_f32_16x16x32_bf16 v[200:203], v[52:55], v[112:115], v[200:203]\n"
    "s_add_u32 s85, s74, 24576\n"
    "ds_read_b64_tr_b16 v[44:45], v5 offset:57600\n"
    "ds_read_b64_tr_b16 v[46:47], v5 offset:61696\n"
    "v_mfma_f32_16x16x32_bf16 v[204:207], v[52:55], v[116:119], v[204:207]\n"
    "s_nop 0\n"
    "buffer_load_dwordx4 a[80:83], v20, s[16:19], s85 offen offset:0 sc1\n"
    "v_mfma_f32_16x16x32_bf16 v[232:235], v[56:59], v[112:115], v[232:235]\n"
    "ds_read_b64_tr_b16 v[48:49], v4 offset:57856\n"
    "v_mfma_f32_16x16x32_bf16 v[236:239], v[56:59], v[116:119], v[236:239]\n"
    "s_waitcnt lgkmcnt(5)\n"
    "ds_read_b64_tr_b16 v[50:51], v4 offset:61952\n"
    "v_mfma_f32_16x16x32_bf16 v[176:179], v[32:35], a[0:3], v[176:179]\n"
    "s_nop 0\n"
    "ds_read_b64_tr_b16 v[52:53], v5 offset:57856\n"
    "v_mfma_f32_16x16x32_bf16 v[180:183], v[32:35], a[4:7], v[180:183]\n"
    "ds_read_b64_tr_b16 v[54:55], v5 offset:61952\n"
    "ds_read_b64_tr_b16 v[56:57], v4 offset:58112\n"
    "v_mfma_f32_16x16x32_bf16 v[208:211], v[36:39], a[0:3], v[208:211]\n"
    "buffer_load_dwordx4 a[84:87], v20, s[16:19], s85 offen offset:1024 sc1\n"
    "v_mfma_f32_16x16x32_bf16 v[212:215], v[36:39], a[4:7], v[212:215]\n"
    "s_waitcnt lgkmcnt(5)\n"
    "ds_read_b64_tr_b16 v[58:59], v4 offset:62208\n"
    "v_mfma_f32_16x16x32_bf16 v[184:187], v[40:43], a[0:3], v[184:187]\n"
    "s_barrier\n"
    "ds_read_b64_tr_b16 v[32:33], v5 offset:58112\n"
    "v_mfma_f32_16x16x32_bf16 v[188:191], v[40:43], a[4:7], v[188:191]\n"
    "s_add_u32 s84, s61, 196608\n"
    "ds_read_b64_tr_b16 v[34:35], v5 offset:62208\n"
    "v_mfma_f32_16x16x32_bf16 v[216:219], v[44:47], a[0:3], v[216:219]\n"
    "s_add_u32 m0, s36, 81920\n"
    "ds_read_b64_tr_b16 v[36:37], v2 offset:0\n"
    "ds_read_b64_tr_b16 v[38:39], v2 offset:4096\n"
    "v_mfma_f32_16x16x32_bf16 v[220:223], v[44:47], a[4:7], v[220:223]\n"
    "s_waitcnt lgkmcnt(6)\n"
    "buffer_load_dwordx4 v17, s[12:15], s84 offen lds\n"
    "v_mfma_f32_16x16x32_bf16 v[192:195], v[48:51], a[0:3], v[192:195]\n"
    "s_nop 0\n"
    "ds_read_b64_tr_b16 v[40:41], v3 offset:0\n"
    "v_mfma_f32_16x16x32_bf16 v[196:199], v[48:51], a[4:7], v[196:199]\n"
    "s_add_u32 s84, s61, 229376\n"
    "ds_read_b64_tr_b16 v[42:43], v3 offset:4096\n"
    "v_mfma_f32_16x16x32_bf16 v[224:227], v[52:55], a[0:3], v[224:227]\n"
    "s_add_u32 m0, s36, 86016\n"
    "ds_read_b64_tr_b16 v[44:45], v2 offset:256\n"
    "ds_read_b64_tr_b16 v[46:47], v2 offset:4352\n"
    "v_mfma_f32_16x16x32_bf16 v[228:231], v[52:55], a[4:7], v[228:231]\n"
    "s_waitcnt lgkmcnt(6)\n"
    "buffer_load_dwordx4 v17, s[12:15], s84 offen lds\n"
    "v_mfma_f32_16x16x32_bf16 v[200:203], v[56:59], a[0:3], v[200:203]\n"
    "s_nop 0\n"
    "ds_read_b64_tr_b16 v[48:49], v3 offset:256\n"
    "v_mfma_f32_16x16x32_bf16 v[204:207], v[56:59], a[4:7], v[204:207]\n"
    "s_add_u32 s84, s56, 0\n"
    "ds_read_b64_tr_b16 v[50:51], v3 offset:4352\n"
    "v_mfma_f32_16x16x32_bf16 v[232:235], v[32:35], a[0:3], v[232:235]\n"
    "s_add_u32 m0, s36, 90112\n"
    "ds_read_b64_tr_b16 v[52:53], v2 offset:512\n"
    "v_mfma_f32_16x16x32_bf16 v[236:239], v[32:35], a[4:7], v[236:239]\n"
    "s_waitcnt lgkmcnt(5)\n"
    "ds_read_b64_tr_b16 v[54:55], v2 offset:4608\n"
    "ds_read_b64_tr_b16 v[56:57], v3 offset:512\n"
    "v_mfma_f32_16x16x32_bf16 v[176:179], v[36:39], a[8:11], v[176:179]\n"
    "buffer_load_dwordx4 v17, s[12:15], s84 offen lds\n"
    "v_mfma_f32_16x16x32_bf16 v[180:183], v[36:39], a[12:15], v[180:183]\n"
    "s_add_u32 s84, s56, 32768\n"
    "ds_read_b64_tr_b16 v[58:59], v3 offset:4608\n"
    "v_mfma_f32_16x16x32_bf16 v[208:211], v[40:43], a[8:11], v[208:211]\n"
    "s_add_u32 m0, s36, 94208\n"
    "ds_read_b64_tr_b16 v[32:33], v2 offset:768\n"
    "v_mfma_f32_16x16x32_bf16 v[212:215], v[40:43], a[12:15], v[212:215]\n"
    "s_waitcnt lgkmcnt(5)\n"
    "ds_read_b64_tr_b16 v[34:35], v2 offset:4864\n"
    "ds_read_b64_tr_b16 v[36:37], v3 offset:768\n"
    "v_mfma_f32_16x16x32_bf16 v[184:187], v[44:47], a[8:11], v[184:187]\n"
    "s_nop 0\n"
    "buffer_load_dwordx4 v17, s[12:15], s84 offen lds\n"
    "v_mfma_f32_16x16x32_bf16 v[188:191], v[44:47], a[12:15], v[188:191]\n"
    "ds_read_b64_tr_b16 v[38:39], v3 offset:4864\n"
    "v_mfma_f32_16x16x32_bf16 v[216:219], v[48:51], a[8:11], v[216:219]\n"
    "ds_read_b64_tr_b16 v[40:41], v2 offset:8192\n"
    "v_mfma_f32_16x16x32_bf16 v[220:223], v[48:51], a[12:15], v[220:223]\n"
    "s_waitcnt lgkmcnt(5)\n"
    "ds_read_b64_tr_b16 v[42:43], v2 offset:12288\n"
    "v_mfma_f32_16x16x32_bf16 v[192:195], v[52:55], a[8:11], v[192:195]\n"
    "s_add_u32 s85, s77, 24576\n"
    "ds_read_b64_tr_b16 v[44:45], v3 offset:8192\n"
    "ds_read_b64_tr_b16 v[46:47], v3 offset:12288\n"
    "v_mfma_f32_16x16x32_bf16 v[196:199], v[52:55], a[12:15], v[196:199]\n"
    "s_nop 0\n"
    "buffer_load_dwordx4 a[88:91], v20, s[16:19], s85 offen offset:0 sc1\n"
    "v_mfma_f32_16x16x32_bf16 v[224:227], v[56:59], a[8:11], v[224:227]\n"
    "ds_read_b64_tr_b16 v[48:49], v2 offset:8448\n"
    "v_mfma_f32_16x16x32_bf16 v[228:231], v[56:59], a[12:15], v[228:231]\n"
    "s_waitcnt lgkmcnt(5)\n"
    "ds_read_b64_tr_b16 v[50:51], v2 offset:12544\n"
    "v_mfma_f32_16x16x32_bf16 v[200:203], v[32:35], a[8:11], v[200:203]\n"
    "s_nop 0\n"
    "ds_read_b64_tr_b16 v[52:53], v3 offset:8448\n"
    "ds_read_b64_tr_b16 v[54:55], v3 offset:12544\n"
    "v_mfma_f32_16x16x32_bf16 v[204:207], v[32:35], a[12:15], v[204:207]\n"
    "buffer_load_dwordx4 a[92:95], v20, s[16:19], s85 offen offset:1024 sc1\n"
    "v_mfma_f32_16x16x32_bf16 v[232:235], v[36:39], a[8:11], v[232:235]\n"
    "ds_read_b64_tr_b16 v[56:57], v2 offset:8704\n"
    "v_mfma_f32_16x16x32_bf16 v[236:239], v[36:39], a[12:15], v[236:239]\n"
    "s_waitcnt lgkmcnt(5)\n"
    "ds_read_b64_tr_b16 v[58:59], v2 offset:12800\n"
    "v_mfma_f32_16x16x32_bf16 v[176:179], v[40:43], a[16:19], v[176:179]\n"
    "s_nop 0\n"
    "ds_read_b64_tr_b16 v[32:33], v3 offset:8704\n"
    "v_mfma_f32_16x16x32_bf16 v[180:183], v[40:43], a[20:23], v[180:183]\n"
    "s_add_u32 s85, s72, 0\n"
    "ds_read_b64_tr_b16 v[34:35], v3 offset:12800\n"
    "ds_read_b64_tr_b16 v[36:37], v2 offset:8960\n"
    "v_mfma_f32_16x16x32_bf16 v[208:211], v[44:47], a[16:19], v[208:211]\n"
    "LPAD8_%=:\n"
    "s_cbranch_execz LPAD12_%=\n"
    "buffer_load_dwordx4 a[96:99], v20, s[16:19], s85 offen offset:0 sc1\n"
    "v_mfma_f32_16x16x32_bf16 v[212:215], v[44:47], a[20:23], v[212:215]\n"
    "s_waitcnt lgkmcnt(5)\n"
    "ds_read_b64_tr_b16 v[38:39], v2 offset:13056\n"
    "v_mfma_f32_16x16x32_bf16 v[184:187], v[48:51], a[16:19], v[184:187]\n"
    "s_nop 0\n"
    "ds_read_b64_tr_b16 v[40:41], v3 offset:8960\n"
    "v_mfma_f32_16x16x32_bf16 v[188:191], v[48:51], a[20:23], v[188:191]\n"
    "ds_read_b64_tr_b16 v[42:43], v3 offset:13056\n"
    "v_mfma_f32_16x16x32_bf16 v[216:219], v[52:55], a[16:19], v[216:219]\n"
    "ds_read_b64_tr_b16 v[44:45], v2 offset:16384\n"
    "ds_read_b64_tr_b16 v[46:47], v2 offset:20480\n"
    "v_mfma_f32_16x16x32_bf16 v[220:223], v[52:55], a[20:23], v[220:223]\n"
    "s_waitcnt lgkmcnt(6)\n"
    "buffer_load_dwordx4 a[100:103], v20, s[16:19], s85 offen offset:1024 sc1\n"
    "v_mfma_f32_16x16x32_bf16 v[192:195], v[56:59], a[16:19], v[192:195]\n"
    "s_nop 0\n"
    "ds_read_b64_tr_b16 v[48:49], v3 offset:16384\n"
    "v_mfma_f32_16x16x32_bf16 v[196:199], v[56:59], a[20:23], v[196:199]\n"
    "s_add_u32 s84, s59, 0\n"
    "ds_read_b64_tr_b16 v[50:51], v3 offset:20480\n"
    "v_mfma_f32_16x16x32_bf16 v[224:227], v[32:35], a[16:19], v[224:227]\n"
    "s_add_u32 m0, s36, 98304\n"
    "ds_read_b64_tr_b16 v[52:53], v2 offset:16640\n"
    "v_mfma_f32_16x16x32_bf16 v[228:231], v[32:35], a[20:23], v[228:231]\n"
    "s_waitcnt lgkmcnt(5)\n"
    "ds_read_b64_tr_b16 v[54:55], v2 offset:20736\n"
    "ds_read_b64_tr_b16 v[56:57], v3 offset:16640\n"
    "v_mfma_f32_16x16x32_bf16 v[200:203], v[36:39], a[16:19], v[200:203]\n"
    "buffer_load_dwordx4 v17, s[12:15], s84 offen lds\n"
    "v_mfma_f32_16x16x32_bf16 v[204:207], v[36:39], a[20:23], v[204:207]\n"
    "s_add_u32 s84, s59, 32768\n"
    "ds_read_b64_tr_b16 v[58:59], v3 offset:20736\n"
    "v_mfma_f32_16x16x32_bf16 v[232:235], v[40:43], a[16:19], v[232:235]\n"
    "s_add_u32 m0, s36, 102400\n"
    "ds_read_b64_tr_b16 v[32:33], v2 offset:16896\n"
    "v_mfma_f32_16x16x32_bf16 v[236:239], v[40:43], a[20:23], v[236:239]\n"
    "s_waitcnt lgkmcnt(5)\n"
    "ds_read_b64_tr_b16 v[34:35], v2 offset:20992\n"
    "ds_read_b64_tr_b16 v[36:37], v3 offset:16896\n"
    "v_mfma_f32_16x16x32_bf16 v[176:179], v[44:47], a[24:27], v[176:179]\n"
    "s_nop 0\n"
    "buffer_load_dwordx4 v17, s[12:15], s84 offen lds\n"
    "v_mfma_f32_16x16x32_bf16 v[180:183], v[44:47], a[28:31], v[180:183]\n"
    "ds_read_b64_tr_b16 v[38:39], v3 offset:20992\n"
    "v_mfma_f32_16x16x32_bf16 v[208:211], v[48:51], a[24:27], v[208:211]\n"
    "s_add_u32 s84, s62, 0\n"
    "ds_read_b64_tr_b16 v[40:41], v2 offset:17152\n"
    "v_mfma_f32_16x16x32_bf16 v[212:215], v[48:51], a[28:31], v[212:215]\n"
    "s_waitcnt lgkmcnt(5)\n"
    "ds_read_b64_tr_b16 v[42:43], v2 offset:21248\n"
    "v_mfma_f32_16x16x32_bf16 v[184:187], v[52:55], a[24:27], v[184:187]\n"
    "s_add_u32 m0, s36, 106496\n"
    "ds_read_b64_tr_b16 v[44:45], v3 offset:17152\n"
    "ds_read_b64_tr_b16 v[46:47], v3 offset:21248\n"
    "v_mfma_f32_16x16x32_bf16 v[188:191], v[52:55], a[28:31], v[188:191]\n"
    "buffer_load_dwordx4 v17, s[12:15], s84 offen lds\n"
    "v_mfma_f32_16x16x32_bf16 v[216:219], v[56:59], a[24:27], v[216:219]\n"
    "s_add_u32 s84, s62, 32768\n"
    "ds_read_b64_tr_b16 v[48:49], v2 offset:24576\n"
    "v_mfma_f32_16x16x32_bf16 v[220:223], v[56:59], a[28:31], v[220:223]\n"
    "s_waitcnt lgkmcnt(5)\n"
    "ds_read_b64_tr_b16 v[50:51], v2 offset:28672\n"
    "v_mfma_f32_16x16x32_bf16 v[192:195], v[32:35], a[24:27], v[192:195]\n"
    "s_add_u32 m0, s36, 110592\n"
    "ds_read_b64_tr_b16 v[52:53], v3 offset:24576\n"
    "ds_read_b64_tr_b16 v[54:55], v3 offset:28672\n"
    "v_mfma_f32_16x16x32_bf16 v[196:199], v[32:35], a[28:31], v[196:199]\n"
    "s_nop 0\n"
    "buffer_load_dwordx4 v17, s[12:15], s84 offen lds\n"
    "v_mfma_f32_16x16x32_bf16 v[224:227], v[36:39], a[24:27], v[224:227]\n"
    "ds_read_b64_tr_b16 v[56:57], v2 offset:24832\n"
    "v_mfma_f32_16x16x32_bf16 v[228:231], v[36:39], a[28:31], v[228:231]\n"
    "s_waitcnt lgkmcnt(5)\n"
    "ds_read_b64_tr_b16 v[58:59], v2 offset:28928\n"
    "v_mfma_f32_16x16x32_bf16 v[200:203], v[40:43], a[24:27], v[200:203]\n"
    "s_nop 0\n"
    "ds_read_b64_tr_b16 v[32:33], v3 offset:24832\n"
    "v_mfma_f32_16x16x32_bf16 v[204:207], v[40:43], a[28:31], v[204:207]\n"
    "s_add_u32 s85, s75, 0\n"
    "ds_read_b64_tr_b16 v[34:35], v3 offset:28928\n"
    "ds_read_b64_tr_b16 v[36:37], v2 offset:25088\n"
    "v_mfma_f32_16x16x32_bf16 v[232:235], v[44:47], a[24:27], v[232:235]\n"
    "s_nop 0\n"
    "buffer_load_dwordx4 a[104:107], v20, s[16:19], s85 offen offset:0 sc1\n"
    "v_mfma_f32_16x16x32_bf16 v[236:239], v[44:47], a[28:31], v[236:239]\n"
    "s_waitcnt lgkmcnt(5)\n"
    "ds_read_b64_tr_b16 v[38:39], v2 offset:29184\n"
    "v_mfma_f32_16x16x32_bf16 v[176:179], v[48:51], a[32:35], v[176:179]\n"
    "s_nop 0\n"
    "ds_read_b64_tr_b16 v[40:41], v3 offset:25088\n"
    "v_mfma_f32_16x16x32_bf16 v[180:183], v[48:51], a[36:39], v[180:183]\n"
    "ds_read_b64_tr_b16 v[42:43], v3 offset:29184\n"
    "v_mfma_f32_16x16x32_bf16 v[208:211], v[52:55], a[32:35], v[208:211]\n"
    "ds_read_b64_tr_b16 v[44:45], v2 offset:25344\n"
    "ds_read_b64_tr_b16 v[46:47], v2 offset:29440\n"
    "v_mfma_f32_16x16x32_bf16 v[212:215], v[52:55], a[36:39], v[212:215]\n"
    "s_waitcnt vmcnt(22) lgkmcnt(6)\n"
    "buffer_load_dwordx4 a[108:111], v20, s[16:19], s85 offen offset:1024 sc1\n"
    "v_mfma_f32_16x16x32_bf16 v[184:187], v[56:59], a[32:35], v[184:187]\n"
    "s_barrier\n"
    "ds_read_b64_tr_b16 v[48:49], v3 offset:25344\n"
    "v_mfma_f32_16x16x32_bf16 v[188:191], v[56:59], a[36:39], v[188:191]\n"
    "s_add_u32 s84, s56, 65536\n"
    "ds_read_b64_tr_b16 v[50:51], v3 offset:29440\n"
    "v_mfma_f32_16x16x32_bf16 v[216:219], v[32:35], a[32:35], v[216:219]\n"
    "s_add_u32 m0, s36, 114688\n"
    "ds_read_b64_tr_b16 v[52:53], v2 offset:32768\n"
    "ds_read_b64_tr_b16 v[54:55], v2 offset:36864\n"
    "v_mfma_f32_16x16x32_bf16 v[220:223], v[32:35], a[36:39], v[220:223]\n"
    "s_waitcnt lgkmcnt(6)\n"
    "buffer_load_dwordx4 v17, s[12:15], s84 offen lds\n"
    "v_mfma_f32_16x16x32_bf16 v[192:195], v[36:39], a[32:35], v[192:195]\n"
    "s_nop 0\n"
    "ds_read_b64_tr_b16 v[56:57], v3 offset:32768\n"
    "v_mfma_f32_16x16x32_bf16 v[196:199], v[36:39], a[36:39], v[196:199]\n"
    "s_add_u32 s84, s56, 98304\n"
    "ds_read_b64_tr_b16 v[58:59], v3 offset:36864\n"
    "v_mfma_f32_16x16x32_bf16 v[224:227], v[40:43], a[32:35], v[224:227]\n"
    "s_add_u32 m0, s36, 118784\n"
    "ds_read_b64_tr_b16 v[32:33], v2 offset:33024\n"
    "v_mfma_f32_16x16x32_bf16 v[228:231], v[40:43], a[36:39], v[228:231]\n"
    "s_waitcnt lgkmcnt(5)\n"
    "ds_read_b64_tr_b16 v[34:35], v2 offset:37120\n"
    "ds_read_b64_tr_b16 v[36:37], v3 offset:33024\n"
    "v_mfma_f32_16x16x32_bf16 v[200:203], v[44:47], a[32:35], v[200:203]\n"
    "s_nop 0\n"
    "buffer_load_dwordx4 v17, s[12:15], s84 offen lds\n"
    "v_mfma_f32_16x16x32_bf16 v[204:207], v[44:47], a[36:39], v[204:207]\n"
    "s_add_u32 s84, s59, 65536\n"
    "ds_read_b64_tr_b16 v[38:39], v3 offset:37120\n"
    "v_mfma_f32_16x16x32_bf16 v[232:235], v[48:51], a[32:35], v[232:235]\n"
    "s_add_u32 m0, s36, 122880\n"
    "ds_read_b64_tr_b16 v[40:41], v2 offset:33280\n"
    "v_mfma_f32_16x16x32_bf16 v[236:239], v[48:51], a[36:39], v[236:239]\n"
    "s_waitcnt lgkmcnt(5)\n"
    "ds_read_b64_tr_b16 v[42:43], v2 offset:37376\n"
    "ds_read_b64_tr_b16 v[44:45], v3 offset:33280\n"
    "v_mfma_f32_16x16x32_bf16 v[176:179], v[52:55], a[40:43], v[176:179]\n"
    "s_nop 0\n"
    "buffer_load_dwordx4 v17, s[12:15], s84 offen lds\n"
    "v_mfma_f32_16x16x32_bf16 v[180:183], v[52:55], a[44:47], v[180:183]\n"
    "ds_read_b64_tr_b16 v[46:47], v3 offset:37376\n"
    "v_mfma_f32_16x16x32_bf16 v[208:211], v[56:59], a[40:43], v[208:211]\n"
    "s_add_u32 s84, s59, 98304\n"
    "ds_read_b64_tr_b16 v[48:49], v2 offset:33536\n"
    "v_mfma_f32_16x16x32_bf16 v[212:215], v[56:59], a[44:47], v[212:215]\n"
    "s_waitcnt lgkmcnt(5)\n"
    "ds_read_b64_tr_b16 v[50:51], v2 offset:37632\n"
    "v_mfma_f32_16x16x32_bf16 v[184:187], v[32:35], a[40:43], v[184:187]\n"
    "s_add_u32 m0, s36, 126976\n"
    "ds_read_b64_tr_b16 v[52:53], v3 offset:33536\n"
    "ds_read_b64_tr_b16 v[54:55], v3 offset:37632\n"
    "v_mfma_f32_16x16x32_bf16 v[188:191], v[32:35], a[44:47], v[188:191]\n"
    "s_nop 0\n"
    "buffer_load_dwordx4 v17, s[12:15], s84 offen lds\n"
    "v_mfma_f32_16x16x32_bf16 v[216:219], v[36:39], a[40:43], v[216:219]\n"
    "ds_read_b64_tr_b16 v[56:57], v2 offset:40960\n"
    "v_mfma_f32_16x16x32_bf16 v[220:223], v[36:39], a[44:47], v[220:223]\n"
    "s_waitcnt lgkmcnt(5)\n"
    "ds_read_b64_tr_b16 v[58:59], v2 offset:45056\n"
    "v_mfma_f32_16x16x32_bf16 v[192:195], v[40:43], a[40:43], v[192:195]\n"
    "s_add_u32 s85, s78, 0\n"
    "ds_read_b64_tr_b16 v[32:33], v3 offset:40960\n"
    "ds_read_b64_tr_b16 v[34:35], v3 offset:45056\n"
    "v_mfma_f32_16x16x32_bf16 v[196:199], v[40:43], a[44:47], v[196:199]\n"
    "buffer_load_dwordx4 a[112:115], v20, s[16:19], s85 offen offset:0 sc1\n"
    "v_mfma_f32_16x16x32_bf16 v[224:227], v[44:47], a[40:43], v[224:227]\n"
    "ds_read_b64_tr_b16 v[36:37], v2 offset:41216\n"
    "v_mfma_f32_16x16x32_bf16 v[228:231], v[44:47], a[44:47], v[228:231]\n"
    "s_waitcnt lgkmcnt(5)\n"
    "ds_read_b64_tr_b16 v[38:39], v2 offset:45312\n"
    "v_mfma_f32_16x16x32_bf16 v[200:203], v[48:51], a[40:43], v[200:203]\n"
    "s_nop 0\n"
    "ds_read_b64_tr_b16 v[40:41], v3 offset:41216\n"
    "v_mfma_f32_16x16x32_bf16 v[204:207], v[48:51], a[44:47], v[204:207]\n"
    "ds_read_b64_tr_b16 v[42:43], v3 offset:45312\n"
    "ds_read_b64_tr_b16 v[44:45], v2 offset:41472\n"
    "v_mfma_f32_16x16x32_bf16 v[232:235], v[52:55], a[40:43], v[232:235]\n"
    "buffer_load_dwordx4 a[116:119], v20, s[16:19], s85 offen offset:1024 sc1\n"
    "v_mfma_f32_16x16x32_bf16 v[236:239], v[52:55], a[44:47], v[236:239]\n"
    "s_waitcnt lgkmcnt(5)\n"
    "ds_read_b64_tr_b16 v[46:47], v2 offset:45568\n"
    "v_mfma_f32_16x16x32_bf16 v[176:179], v[56:59], a[48:51], v[176:179]\n"
    "s_nop 0\n"
    "ds_read_b64_tr_b16 v[48:49], v3 offset:41472\n"
    "v_mfma_f32_16x16x32_bf16 v[180:183], v[56:59], a[52:55], v[180:183]\n"
    "ds_read_b64_tr_b16 v[50:51], v3 offset:45568\n"
    "v_mfma_f32_16x16x32_bf16 v[208:211], v[32:35], a[48:51], v[208:211]\n"
    "s_add_u32 s85, s72, 8192\n"
    "ds_read_b64_tr_b16 v[52:53], v2 offset:41728\n"
    "ds_read_b64_tr_b16 v[54:55], v2 offset:45824\n"
    "v_mfma_f32_16x16x32_bf16 v[212:215], v[32:35], a[52:55], v[212:215]\n"
    "s_waitcnt lgkmcnt(6)\n"
    "buffer_load_dwordx4 a[120:123], v20, s[16:19], s85 offen offset:0 sc1\n"
    "v_mfma_f32_16x16x32_bf16 v[184:187], v[36:39], a[48:51], v[184:187]\n"
    "s_nop 0\n"
    "ds_read_b64_tr_b16 v[56:57], v3 offset:41728\n"
    "v_mfma_f32_16x16x32_bf16 v[188:191], v[36:39], a[52:55], v[188:191]\n"
    "ds_read_b64_tr_b16 v[58:59], v3 offset:45824\n"
    "v_mfma_f32_16x16x32_bf16 v[216:219], v[40:43], a[48:51], v[216:219]\n"
    "ds_read_b64_tr_b16 v[32:33], v2 offset:49152\n"
    "v_mfma_f32_16x16x32_bf16 v[220:223], v[40:43], a[52:55], v[220:223]\n"
    "s_waitcnt lgkmcnt(5)\n"
    "ds_read_b64_tr_b16 v[34:35], v2 offset:53248\n"
    "ds_read_b64_tr_b16 v[36:37], v3 offset:49152\n"
    "v_mfma_f32_16x16x32_bf16 v[192:195], v[44:47], a[48:51], v[192:195]\n"
    "s_nop 0\n"
    "buffer_load_dwordx4 a[124:127], v20, s[16:19], s85 offen offset:1024 sc1\n"
    "v_mfma_f32_16x16x32_bf16 v[196:199], v[44:47], a[52:55], v[196:199]\n"
    "ds_read_b64_tr_b16 v[38:39], v3 offset:53248\n"
    "v_mfma_f32_16x16x32_bf16 v[224:227], v[48:51], a[48:51], v[224:227]\n"
    "ds_read_b64_tr_b16 v[40:41], v2 offset:49408\n"
    "v_mfma_f32_16x16x32_bf16 v[228:231], v[48:51], a[52:55], v[228:231]\n"
    "s_waitcnt lgkmcnt(5)\n"
    "ds_read_b64_tr_b16 v[42:43], v2 offset:53504\n"
    "v_mfma_f32_16x16x32_bf16 v[200:203], v[52:55], a[48:51], v[200:203]\n"
    "s_add_u32 s85, s75, 8192\n"
    "ds_read_b64_tr_b16 v[44:45], v3 offset:49408\n"
    "ds_read_b64_tr_b16 v[46:47], v3 offset:53504\n"
    "v_mfma_f32_16x16x32_bf16 v[204:207], v[52:55], a[52:55], v[204:207]\n"
    "s_nop 0\n"
    "buffer_load_dwordx4 a[128:131], v20, s[16:19], s85 offen offset:0 sc1\n"
    "v_mfma_f32_16x16x32_bf16 v[232:235], v[56:59], a[48:51], v[232:235]\n"
    "ds_read_b64_tr_b16 v[48:49], v2 offset:49664\n"
    "v_mfma_f32_16x16x32_bf16 v[236:239], v[56:59], a[52:55], v[236:239]\n"
    "s_waitcnt lgkmcnt(5)\n"
    "ds_read_b64_tr_b16 v[50:51], v2 offset:53760\n"
    "v_mfma_f32_16x16x32_bf16 v[176:179], v[32:35], a[56:59], v[176:179]\n"
    "s_nop 0\n"
    "ds_read_b64_tr_b16 v[52:53], v3 offset:49664\n"
    "ds_read_b64_tr_b16 v[54:55], v3 offset:53760\n"
    "v_mfma_f32_16x16x32_bf16 v[180:183], v[32:35], a[60:63], v[180:183]\n"
    "buffer_load_dwordx4 a[132:135], v20, s[16:19], s85 offen offset:1024 sc1\n"
    "v_mfma_f32_16x16x32_bf16 v[208:211], v[36:39], a[56:59], v[208:211]\n"
    "ds_read_b64_tr_b16 v[56:57], v2 offset:49920\n"
    "v_mfma_f32_16x16x32_bf16 v[212:215], v[36:39], a[60:63], v[212:215]\n"
    "s_waitcnt lgkmcnt(5)\n"
    "ds_read_b64_tr_b16 v[58:59], v2 offset:54016\n"
    "v_mfma_f32_16x16x32_bf16 v[184:187], v[40:43], a[56:59], v[184:187]\n"
    "s_add_u32 s84, s62, 65536\n"
    "ds_read_b64_tr_b16 v[32:33], v3 offset:49920\n"
    "v_mfma_f32_16x16x32_bf16 v[188:191], v[40:43], a[60:63], v[188:191]\n"
    "s_add_u32 m0, s36, 0\n"
    "ds_read_b64_tr_b16 v[34:35], v3 offset:54016\n"
    "ds_read_b64_tr_b16 v[36:37], v2 offset:57344\n"
    "v_mfma_f32_16x16x32_bf16 v[216:219], v[44:47], a[56:59], v[216:219]\n"
    "buffer_load_dwordx4 v17, s[12:15], s84 offen lds\n"
    "v_mfma_f32_16x16x32_bf16 v[220:223], v[44:47], a[60:63], v[220:223]\n"
    "s_waitcnt lgkmcnt(5)\n"
    "ds_read_b64_tr_b16 v[38:39], v2 offset:61440\n"
    "v_mfma_f32_16x16x32_bf16 v[192:195], v[48:51], a[56:59], v[192:195]\n"
    "s_nop 0\n"
    "ds_read_b64_tr_b16 v[40:41], v3 offset:57344\n"
    "v_mfma_f32_16x16x32_bf16 v[196:199], v[48:51], a[60:63], v[196:199]\n"
    "s_add_u32 s84, s62, 98304\n"
    "ds_read_b64_tr_b16 v[42:43], v3 offset:61440\n"
    "v_mfma_f32_16x16x32_bf16 v[224:227], v[52:55], a[56:59], v[224:227]\n"
    "s_add_u32 m0, s36, 4096\n"
    "ds_read_b64_tr_b16 v[44:45], v2 offset:57600\n"
    "ds_read_b64_tr_b16 v[46:47], v2 offset:61696\n"
    "v_mfma_f32_16x16x32_bf16 v[228:231], v[52:55], a[60:63], v[228:231]\n"
    "s_waitcnt lgkmcnt(6)\n"
    "buffer_load_dwordx4 v17, s[12:15], s84 offen lds\n"
    "v_mfma_f32_16x16x32_bf16 v[200:203], v[56:59], a[56:59], v[200:203]\n"
    "s_nop 0\n"
    "ds_read_b64_tr_b16 v[48:49], v3 offset:57600\n"
    "v_mfma_f32_16x16x32_bf16 v[204:207], v[56:59], a[60:63], v[204:207]\n"
    "s_add_u32 s84, s57, 0\n"
    "ds_read_b64_tr_b16 v[50:51], v3 offset:61696\n"
    "v_mfma_f32_16x16x32_bf16 v[232:235], v[32:35], a[56:59], v[232:235]\n"
    "s_add_u32 m0, s36, 8192\n"
    "ds_read_b64_tr_b16 v[52:53], v2 offset:57856\n"
    "ds_read_b64_tr_b16 v[54:55], v2 offset:61952\n"
    "v_mfma_f32_16x16x32_bf16 v[236:239], v[32:35], a[60:63], v[236:239]\n"
    "s_waitcnt lgkmcnt(6)\n"
    "buffer_load_dwordx4 v17, s[12:15], s84 offen lds\n"
    "v_mfma_f32_16x16x32_bf16 v[176:179], v[36:39], a[64:67], v[176:179]\n"
    "ds_read_b64_tr_b16 v[56:57], v3 offset:57856\n"
    "v_mfma_f32_16x16x32_bf16 v[180:183], v[36:39], a[68:71], v[180:183]\n"
    "s_add_u32 s84, s57, 32768\n"
    "ds_read_b64_tr_b16 v[58:59], v3 offset:61952\n"
    "v_mfma_f32_16x16x32_bf16 v[208:211], v[40:43], a[64:67], v[208:211]\n"
    "s_add_u32 m0, s36, 12288\n"
    "ds_read_b64_tr_b16 v[32:33], v2 offset:58112\n"
    "v_mfma_f32_16x16x32_bf16 v[212:215], v[40:43], a[68:71], v[212:215]\n"
    "s_waitcnt vmcnt(23) lgkmcnt(5)\n"
    "ds_read_b64_tr_b16 v[34:35], v2 offset:62208\n"
    "ds_read_b64_tr_b16 v[36:37], v3 offset:58112\n"
    "v_mfma_f32_16x16x32_bf16 v[184:187], v[44:47], a[64:67], v[184:187]\n"
    "s_barrier\n"
    "buffer_load_dwordx4 v17, s[12:15], s84 offen lds\n"
    "v_mfma_f32_16x16x32_bf16 v[188:191], v[44:47], a[68:71], v[188:191]\n"
    "s_add_u32 s84, s60, 0\n"
    "ds_read_b64_tr_b16 v[38:39], v3 offset:62208\n"
    "v_mfma_f32_16x16x32_bf16 v[216:219], v[48:51], a[64:67], v[216:219]\n"
    "s_add_u32 m0, s36, 16384\n"
    "ds_read_b64_tr_b16 v[40:41], v4 offset:0\n"
    "v_mfma_f32_16x16x32_bf16 v[220:223], v[48:51], a[68:71], v[220:223]\n"
    "s_waitcnt lgkmcnt(5)\n"
    "ds_read_b64_tr_b16 v[42:43], v4 offset:4096\n"
    "ds_read_b64_tr_b16 v[44:45], v5 offset:0\n"
    "v_mfma_f32_16x16x32_bf16 v[192:195], v[52:55], a[64:67], v[192:195]\n"
    "buffer_load_dwordx4 v17, s[12:15], s84 offen lds\n"
    "v_mfma_f32_16x16x32_bf16 v[196:199], v[52:55], a[68:71], v[196:199]\n"
    "ds_read_b64_tr_b16 v[46:47], v5 offset:4096\n"
    "v_mfma_f32_16x16x32_bf16 v[224:227], v[56:59], a[64:67], v[224:227]\n"
    "s_add_u32 s84, s60, 32768\n"
    "ds_read_b64_tr_b16 v[48:49], v4 offset:256\n"
    "v_mfma_f32_16x16x32_bf16 v[228:231], v[56:59], a[68:71], v[228:231]\n"
    "s_waitcnt lgkmcnt(5)\n"
    "ds_read_b64_tr_b16 v[50:51], v4 offset:4352\n"
    "v_mfma_f32_16x16x32_bf16 v[200:203], v[32:35], a[64:67], v[200:203]\n"
    "s_add_u32 m0, s36, 20480\n"
    "ds_read_b64_tr_b16 v[52:53], v5 offset:256\n"
    "ds_read_b64_tr_b16 v[54:55], v5 offset:4352\n"
    "v_mfma_f32_16x16x32_bf16 v[204:207], v[32:35], a[68:71], v[204:207]\n"
    "s_nop 0\n"
    "buffer_load_dwordx4 v17, s[12:15], s84 offen lds\n"
    "v_mfma_f32_16x16x32_bf16 v[232:235], v[36:39], a[64:67], v[232:235]\n"
    "s_add_u32 s84, s63, 0\n"
    "ds_read_b64_tr_b16 v[56:57], v4 offset:512\n"
    "v_mfma_f32_16x16x32_bf16 v[236:239], v[36:39], a[68:71], v[236:239]\n"
    "s_waitcnt lgkmcnt(5)\n"
    "ds_read_b64_tr_b16 v[58:59], v4 offset:4608\n"
    "v_mfma_f32_16x16x32_bf16 v[176:179], v[40:43], a[72:75], v[176:179]\n"
    "s_add_u32 m0, s36, 24576\n"
    "ds_read_b64_tr_b16 v[32:33], v5 offset:512\n"
    "ds_read_b64_tr_b16 v[34:35], v5 offset:4608\n"
    "v_mfma_f32_16x16x32_bf16 v[180:183], v[40:43], a[76:79], v[180:183]\n"
    "buffer_load_dwordx4 v17, s[12:15], s84 offen lds\n"
    "v_mfma_f32_16x16x32_bf16 v[208:211], v[44:47], a[72:75], v[208:211]\n"
    "ds_read_b64_tr_b16 v[36:37], v4 offset:768\n"
    "v_mfma_f32_16x16x32_bf16 v[212:215], v[44:47], a[76:79], v[212:215]\n"
    "s_waitcnt lgkmcnt(5)\n"
    "ds_read_b64_tr_b16 v[38:39], v4 offset:4864\n"
    "v_mfma_f32_16x16x32_bf16 v[184:187], v[48:51], a[72:75], v[184:187]\n"
    "s_add_u32 s84, s63, 32768\n"
    "ds_read_b64_tr_b16 v[40:41], v5 offset:768\n"
    "v_mfma_f32_16x16x32_bf16 v[188:191], v[48:51], a[76:79], v[188:191]\n"
    "s_add_u32 m0, s36, 28672\n"
    "ds_read_b64_tr_b16 v[42:43], v5 offset:4864\n"
    "ds_read_b64_tr_b16 v[44:45], v4 offset:8192\n"
    "v_mfma_f32_16x16x32_bf16 v[216:219], v[52:55], a[72:75], v[216:219]\n"
    "s_nop 0\n"
    "buffer_load_dwordx4 v17, s[12:15], s84 offen lds\n"
    "v_mfma_f32_16x16x32_bf16 v[220:223], v[52:55], a[76:79], v[220:223]\n"
    "s_waitcnt lgkmcnt(5)\n"
    "ds_read_b64_tr_b16 v[46:47], v4 offset:12288\n"
    "v_mfma_f32_16x16x32_bf16 v[192:195], v[56:59], a[72:75], v[192:195]\n"
    "s_nop 0\n"
    "ds_read_b64_tr_b16 v[48:49], v5 offset:8192\n"
    "v_mfma_f32_16x16x32_bf16 v[196:199], v[56:59], a[76:79], v[196:199]\n"
    "s_add_u32 s85, s78, 8192\n"
    "ds_read_b64_tr_b16 v[50:51], v5 offset:12288\n"
    "ds_read_b64_tr_b16 v[52:53], v4 offset:8448\n"
    "v_mfma_f32_16x16x32_bf16 v[224:227], v[32:35], a[72:75], v[224:227]\n"
    "buffer_load_dwordx4 a[136:139], v20, s[16:19], s85 offen offset:0 sc1\n"
    "v_mfma_f32_16x16x32_bf16 v[228:231], v[32:35], a[76:79], v[228:231]\n"
    "s_waitcnt lgkmcnt(5)\n"
    "ds_read_b64_tr_b16 v[54:55], v4 offset:12544\n"
    "v_mfma_f32_16x16x32_bf16 v[200:203], v[36:39], a[72:75], v[200:203]\n"
    "s_nop 0\n"
    "ds_read_b64_tr_b16 v[56:57], v5 offset:8448\n"
    "v_mfma_f32_16x16x32_bf16 v[204:207], v[36:39], a[76:79], v[204:207]\n"
    "ds_read_b64_tr_b16 v[58:59], v5 offset:12544\n"
    "v_mfma_f32_16x16x32_bf16 v[232:235], v[40:43], a[72:75], v[232:235]\n"
    "ds_read_b64_tr_b16 v[32:33], v4 offset:8704\n"
    "ds_read_b64_tr_b16 v[34:35], v4 offset:12800\n"
    "v_mfma_f32_16x16x32_bf16 v[236:239], v[40:43], a[76:79], v[236:239]\n"
    "s_waitcnt lgkmcnt(6)\n"
    "buffer_load_dwordx4 a[140:143], v20, s[16:19], s85 offen offset:1024 sc1\n"
    "v_mfma_f32_16x16x32_bf16 v[176:179], v[44:47], a[80:83], v[176:179]\n"
    "s_nop 0\n"
    "ds_read_b64_tr_b16 v[36:37], v5 offset:8704\n"
    "v_mfma_f32_16x16x32_bf16 v[180:183], v[44:47], a[84:87], v[180:183]\n"
    "ds_read_b64_tr_b16 v[38:39], v5 offset:12800\n"
    "v_mfma_f32_16x16x32_bf16 v[208:211], v[48:51], a[80:83], v[208:211]\n"
    "s_add_u32 s85, s73, 0\n"
    "ds_read_b64_tr_b16 v[40:41], v4 offset:8960\n"
    "v_mfma_f32_16x16x32_bf16 v[212:215], v[48:51], a[84:87], v[212:215]\n"
    "s_waitcnt lgkmcnt(5)\n"
    "ds_read_b64_tr_b16 v[42:43], v4 offset:13056\n"
    "ds_read_b64_tr_b16 v[44:45], v5 offset:8960\n"
    "v_mfma_f32_16x16x32_bf16 v[184:187], v[52:55], a[80:83], v[184:187]\n"
    "buffer_load_dwordx4 a[144:147], v20, s[16:19], s85 offen offset:0 sc1\n"
    "v_mfma_f32_16x16x32_bf16 v[188:191], v[52:55], a[84:87], v[188:191]\n"
    "ds_read_b64_tr_b16 v[46:47], v5 offset:13056\n"
    "v_mfma_f32_16x16x32_bf16 v[216:219], v[56:59], a[80:83], v[216:219]\n"
    "ds_read_b64_tr_b16 v[48:49], v4 offset:16384\n"
    "v_mfma_f32_16x16x32_bf16 v[220:223], v[56:59], a[84:87], v[220:223]\n"
    "s_waitcnt lgkmcnt(5)\n"
    "ds_read_b64_tr_b16 v[50:51], v4 offset:20480\n"
    "v_mfma_f32_16x16x32_bf16 v[192:195], v[32:35], a[80:83], v[192:195]\n"
    "s_nop 0\n"
    "ds_read_b64_tr_b16 v[52:53], v5 offset:16384\n"
    "ds_read_b64_tr_b16 v[54:55], v5 offset:20480\n"
    "v_mfma_f32_16x16x32_bf16 v[196:199], v[32:35], a[84:87], v[196:199]\n"
    "buffer_load_dwordx4 a[148:151], v20, s[16:19], s85 offen offset:1024 sc1\n"
    "v_mfma_f32_16x16x32_bf16 v[224:227], v[36:39], a[80:83], v[224:227]\n"
    "ds_read_b64_tr_b16 v[56:57], v4 offset:16640\n"
    "v_mfma_f32_16x16x32_bf16 v[228:231], v[36:39], a[84:87], v[228:231]\n"
    "s_waitcnt lgkmcnt(5)\n"
    "ds_read_b64_tr_b16 v[58:59], v4 offset:20736\n"
    "v_mfma_f32_16x16x32_bf16 v[200:203], v[40:43], a[80:83], v[200:203]\n"
    "s_add_u32 s85, s76, 0\n"
    "ds_read_b64_tr_b16 v[32:33], v5 offset:16640\n"
    "ds_read_b64_tr_b16 v[34:35], v5 offset:20736\n"
    "v_mfma_f32_16x16x32_bf16 v[204:207], v[40:43], a[84:87], v[204:207]\n"
    "buffer_load_dwordx4 a[152:155], v20, s[16:19], s85 offen offset:0 sc1\n"
    "v_mfma_f32_16x16x32_bf16 v[232:235], v[44:47], a[80:83], v[232:235]\n"
    "ds_read_b64_tr_b16 v[36:37], v4 offset:16896\n"
    "v_mfma_f32_16x16x32_bf16 v[236:239], v[44:47], a[84:87], v[236:239]\n"
    "s_waitcnt vmcnt(31) lgkmcnt(5)\n"
    "ds_read_b64_tr_b16 v[38:39], v4 offset:20992\n"
    "v_mfma_f32_16x16x32_bf16 v[176:179], v[48:51], a[88:91], v[176:179]\n"
    "s_nop 0\n"
    "ds_read_b64_tr_b16 v[40:41], v5 offset:16896\n"
    "v_mfma_f32_16x16x32_bf16 v[180:183], v[48:51], a[92:95], v[180:183]\n"
    "ds_read_b64_tr_b16 v[42:43], v5 offset:20992\n"
    "ds_read_b64_tr_b16 v[44:45], v4 offset:17152\n"
    "v_mfma_f32_16x16x32_bf16 v[208:211], v[52:55], a[88:91], v[208:211]\n"
    "buffer_load_dwordx4 a[156:159], v20, s[16:19], s85 offen offset:1024 sc1\n"
    "v_mfma_f32_16x16x32_bf16 v[212:215], v[52:55], a[92:95], v[212:215]\n"
    "s_waitcnt lgkmcnt(5)\n"
    "ds_read_b64_tr_b16 v[46:47], v4 offset:21248\n"
    "v_mfma_f32_16x16x32_bf16 v[184:187], v[56:59], a[88:91], v[184:187]\n"
    "s_nop 0\n"
    "ds_read_b64_tr_b16 v[48:49], v5 offset:17152\n"
    "v_mfma_f32_16x16x32_bf16 v[188:191], v[56:59], a[92:95], v[188:191]\n"
    "ds_read_b64_tr_b16 v[50:51], v5 offset:21248\n"
    "v_mfma_f32_16x16x32_bf16 v[216:219], v[32:35], a[88:91], v[216:219]\n"
    "s_mov_b32 s87, 13631488\n"
    "ds_read_b64_tr_b16 v[52:53], v4 offset:24576\n"
    "ds_read_b64_tr_b16 v[54:55], v4 offset:28672\n"
    "v_mfma_f32_16x16x32_bf16 v[220:223], v[32:35], a[92:95], v[220:223]\n"
    "s_waitcnt lgkmcnt(6)\n"
    "buffer_load_dwordx2 v[62:63], v25, s[16:19], s87 offen sc1\n"
    "v_mfma_f32_16x16x32_bf16 v[192:195], v[36:39], a[88:91], v[192:195]\n"
    "s_nop 0\n"
    "ds_read_b64_tr_b16 v[56:57], v5 offset:24576\n"
    "v_mfma_f32_16x16x32_bf16 v[196:199], v[36:39], a[92:95], v[196:199]\n"
    "ds_read_b64_tr_b16 v[58:59], v5 offset:28672\n"
    "v_mfma_f32_16x16x32_bf16 v[224:227], v[40:43], a[88:91], v[224:227]\n"
    "s_add_u32 s85, s79, 0\n"
    "ds_read_b64_tr_b16 v[32:33], v4 offset:24832\n"
    "ds_read_b64_tr_b16 v[34:35], v4 offset:28928\n"
    "v_mfma_f32_16x16x32_bf16 v[228:231], v[40:43], a[92:95], v[228:231]\n"
    "s_waitcnt lgkmcnt(6)\n"
    "buffer_load_dwordx4 a[160:163], v20, s[16:19], s85 offen offset:0 sc1\n"
    "v_mfma_f32_16x16x32_bf16 v[200:203], v[44:47], a[88:91], v[200:203]\n"
    "ds_read_b64_tr_b16 v[36:37], v5 offset:24832\n"
    "v_mfma_f32_16x16x32_bf16 v[204:207], v[44:47], a[92:95], v[204:207]\n"
    "ds_read_b64_tr_b16 v[38:39], v5 offset:28928\n"
    "v_mfma_f32_16x16x32_bf16 v[232:235], v[48:51], a[88:91], v[232:235]\n"
    "ds_read_b64_tr_b16 v[40:41], v4 offset:25088\n"
    "v_mfma_f32_16x16x32_bf16 v[236:239], v[48:51], a[92:95], v[236:239]\n"
    "s_waitcnt vmcnt(32) lgkmcnt(5)\n"
    "ds_read_b64_tr_b16 v[42:43], v4 offset:29184\n"
    "ds_read_b64_tr_b16 v[44:45], v5 offset:25088\n"
    "v_mfma_f32_16x16x32_bf16 v[176:179], v[52:55], a[96:99], v[176:179]\n"
    "s_nop 0\n"
    "buffer_load_dwordx4 a[164:167], v20, s[16:19], s85 offen offset:1024 sc1\n"
    "v_mfma_f32_16x16x32_bf16 v[180:183], v[52:55], a[100:103], v[180:183]\n"
    "s_add_u32 s84, s52, 0\n"
    "ds_read_b64_tr_b16 v[46:47], v5 offset:29184\n"
    "v_mfma_f32_16x16x32_bf16 v[208:211], v[56:59], a[96:99], v[208:211]\n"
    "s_add_u32 m0, s36, 32768\n"
    "ds_read_b64_tr_b16 v[48:49], v4 offset:25344\n"
    "v_mfma_f32_16x16x32_bf16 v[212:215], v[56:59], a[100:103], v[212:215]\n"
    "s_waitcnt vmcnt(23) lgkmcnt(5)\n"
    "ds_read_b64_tr_b16 v[50:51], v4 offset:29440\n"
    "v_mfma_f32_16x16x32_bf16 v[184:187], v[32:35], a[96:99], v[184:187]\n"
    "s_barrier\n"
    "ds_read_b64_tr_b16 v[52:53], v5 offset:25344\n"
    "ds_read_b64_tr_b16 v[54:55], v5 offset:29440\n"
    "v_mfma_f32_16x16x32_bf16 v[188:191], v[32:35], a[100:103], v[188:191]\n"
    "s_nop 0\n"
    "buffer_load_dwordx4 v18, s[12:15], s84 offen lds\n"
    "v_mfma_f32_16x16x32_bf16 v[216:219], v[36:39], a[96:99], v[216:219]\n"
    "ds_read_b64_tr_b16 v[56:57], v4 offset:32768\n"
    "v_mfma_f32_16x16x32_bf16 v[220:223], v[36:39], a[100:103], v[220:223]\n"
    "s_waitcnt lgkmcnt(5)\n"
    "ds_read_b64_tr_b16 v[58:59], v4 offset:36864\n"
    "v_mfma_f32_16x16x32_bf16 v[192:195], v[40:43], a[96:99], v[192:195]\n"
    "s_add_u32 s84, s52, 65536\n"
    "ds_read_b64_tr_b16 v[32:33], v5 offset:32768\n"
    "v_mfma_f32_16x16x32_bf16 v[196:199], v[40:43], a[100:103], v[196:199]\n"
    "s_add_u32 m0, s36, 36864\n"
    "ds_read_b64_tr_b16 v[34:35], v5 offset:36864\n"
    "ds_read_b64_tr_b16 v[36:37], v4 offset:33024\n"
    "v_mfma_f32_16x16x32_bf16 v[224:227], v[44:47], a[96:99], v[224:227]\n"
    "s_nop 0\n"
    "buffer_load_dwordx4 v18, s[12:15], s84 offen lds\n"
    "v_mfma_f32_16x16x32_bf16 v[228:231], v[44:47], a[100:103], v[228:231]\n"
    "s_waitcnt lgkmcnt(5)\n"
    "ds_read_b64_tr_b16 v[38:39], v4 offset:37120\n"
    "v_mfma_f32_16x16x32_bf16 v[200:203], v[48:51], a[96:99], v[200:203]\n"
    "s_add_u32 s84, s52, 131072\n"
    "ds_read_b64_tr_b16 v[40:41], v5 offset:33024\n"
    "v_mfma_f32_16x16x32_bf16 v[204:207], v[48:51], a[100:103], v[204:207]\n"
    "s_add_u32 m0, s36, 40960\n"
    "ds_read_b64_tr_b16 v[42:43], v5 offset:37120\n"
    "ds_read_b64_tr_b16 v[44:45], v4 offset:33280\n"
    "v_mfma_f32_16x16x32_bf16 v[232:235], v[52:55], a[96:99], v[232:235]\n"
    "s_nop 0\n"
    "buffer_load_dwordx4 v18, s[12:15], s84 offen lds\n"
    "v_mfma_f32_16x16x32_bf16 v[236:239], v[52:55], a[100:103], v[236:239]\n"
    "s_waitcnt lgkmcnt(5)\n"
    "ds_read_b64_tr_b16 v[46:47], v4 offset:37376\n"
    "v_mfma_f32_16x16x32_bf16 v[176:179], v[56:59], a[104:107], v[176:179]\n"
    "s_nop 0\n"
    "ds_read_b64_tr_b16 v[48:49], v5 offset:33280\n"
    "v_mfma_f32_16x16x32_bf16 v[180:183], v[56:59], a[108:111], v[180:183]\n"
    "s_add_u32 s84, s52, 196608\n"
    "ds_read_b64_tr_b16 v[50:51], v5 offset:37376\n"
    "v_mfma_f32_16x16x32_bf16 v[208:211], v[32:35], a[104:107], v[208:211]\n"
    "s_add_u32 m0, s36, 45056\n"
    "ds_read_b64_tr_b16 v[52:53], v4 offset:33536\n"
    "ds_read_b64_tr_b16 v[54:55], v4 offset:37632\n"
    "v_mfma_f32_16x16x32_bf16 v[212:215], v[32:35], a[108:111], v[212:215]\n"
    "s_waitcnt lgkmcnt(6)\n"
    "buffer_load_dwordx4 v18, s[12:15], s84 offen lds\n"
    "v_mfma_f32_16x16x32_bf16 v[184:187], v[36:39], a[104:107], v[184:187]\n"
    "s_nop 0\n"
    "ds_read_b64_tr_b16 v[56:57], v5 offset:33536\n"
    "v_mfma_f32_16x16x32_bf16 v[188:191], v[36:39], a[108:111], v[188:191]\n"
    "s_add_u32 s84, s53, 0\n"
    "ds_read_b64_tr_b16 v[58:59], v5 offset:37632\n"
    "v_mfma_f32_16x16x32_bf16 v[216:219], v[40:43], a[104:107], v[216:219]\n"
    "s_add_u32 m0, s36, 49152\n"
    "ds_read_b64_tr_b16 v[32:33], v4 offset:40960\n"
    "ds_read_b64_tr_b16 v[34:35], v4 offset:45056\n"
    "v_mfma_f32_16x16x32_bf16 v[220:223], v[40:43], a[108:111], v[220:223]\n"
    "s_waitcnt lgkmcnt(6)\n"
    "buffer_load_dwordx4 v18, s[12:15], s84 offen lds\n"
    "v_mfma_f32_16x16x32_bf16 v[192:195], v[44:47], a[104:107], v[192:195]\n"
    "ds_read_b64_tr_b16 v[36:37], v5 offset:40960\n"
    "v_mfma_f32_16x16x32_bf16 v[196:199], v[44:47], a[108:111], v[196:199]\n"
    "s_add_u32 s84, s53, 65536\n"
    "ds_read_b64_tr_b16 v[38:39], v5 offset:45056\n"
    "v_mfma_f32_16x16x32_bf16 v[224:227], v[48:51], a[104:107], v[224:227]\n"
    "s_add_u32 m0, s36, 53248\n"
    "ds_read_b64_tr_b16 v[40:41], v4 offset:41216\n"
    "v_mfma_f32_16x16x32_bf16 v[228:231], v[48:51], a[108:111], v[228:231]\n"
    "s_waitcnt lgkmcnt(5)\n"
    "ds_read_b64_tr_b16 v[42:43], v4 offset:45312\n"
    "ds_read_b64_tr_b16 v[44:45], v5 offset:41216\n"
    "v_mfma_f32_16x16x32_bf16 v[200:203], v[52:55], a[104:107], v[200:203]\n"
    "s_nop 0\n"
    "buffer_load_dwordx4 v18, s[12:15], s84 offen lds\n"
    "v_mfma_f32_16x16x32_bf16 v[204:207], v[52:55], a[108:111], v[204:207]\n"
    "s_add_u32 s84, s54, 0\n"
    "ds_read_b64_tr_b16 v[46:47], v5 offset:45312\n"
    "v_mfma_f32_16x16x32_bf16 v[232:235], v[56:59], a[104:107], v[232:235]\n"
    "s_add_u32 m0, s36, 57344\n"
    "ds_read_b64_tr_b16 v[48:49], v4 offset:41472\n"
    "v_mfma_f32_16x16x32_bf16 v[236:239], v[56:59], a[108:111], v[236:239]\n"
    "s_waitcnt vmcnt(27) lgkmcnt(5)\n"
    "ds_read_b64_tr_b16 v[50:51], v4 offset:45568\n"
    "ds_read_b64_tr_b16 v[52:53], v5 offset:41472\n"
    "v_mfma_f32_16x16x32_bf16 v[176:179], v[32:35], a[112:115], v[176:179]\n"
    "LPAD9_%=:\n"
    "s_cbranch_execz LPAD13_%=\n"
    "buffer_load_dwordx4 v18, s[12:15], s84 offen lds\n"
    "v_mfma_f32_16x16x32_bf16 v[180:183], v[32:35], a[116:119], v[180:183]\n"
    "s_nop 0\n"
    "ds_read_b64_tr_b16 v[54:55], v5 offset:45568\n"
    "v_mfma_f32_16x16x32_bf16 v[208:211], v[36:39], a[112:115], v[208:211]\n"
    "s_add_u32 s84, s55, 0\n"
    "ds_read_b64_tr_b16 v[56:57], v4 offset:41728\n"
    "v_mfma_f32_16x16x32_bf16 v[212:215], v[36:39], a[116:119], v[212:215]\n"
    "s_waitcnt vmcnt(9) lgkmcnt(5)\n"
    "ds_read_b64_tr_b16 v[58:59], v4 offset:45824\n"
    "v_cmp_ne_u64 vcc, s[98:99], v[62:63]\n"
    "v_mfma_f32_16x16x32_bf16 v[184:187], v[40:43], a[112:115], v[184:187]\n"
    "s_add_u32 m0, s36, 61440\n"
    "ds_read_b64_tr_b16 v[32:33], v5 offset:41728\n"
    "ds_read_b64_tr_b16 v[34:35], v5 offset:45824\n"
    "v_mfma_f32_16x16x32_bf16 v[188:191], v[40:43], a[116:119], v[188:191]\n"
    "s_nop 0\n"
    "buffer_load_dwordx4 v18, s[12:15], s84 offen lds\n"
    "s_cbranch_vccnz LPLQ_%=s\n"
    "LPLQ_%=:\n"
    "v_mfma_f32_16x16x32_bf16 v[216:219], v[44:47], a[112:115], v[216:219]\n"
    "s_add_u32 s97, s92, 15728640\n"
    "ds_read_b64_tr_b16 v[36:37], v4 offset:49152\n"
    "v_mfma_f32_16x16x32_bf16 v[220:223], v[44:47], a[116:119], v[220:223]\n"
    "s_waitcnt lgkmcnt(5)\n"
    "ds_read_b64_tr_b16 v[38:39], v4 offset:53248\n"
    "v_mfma_f32_16x16x32_bf16 v[192:195], v[48:51], a[112:115], v[192:195]\n"
    "s_add_u32 m0, s36, 147456\n"
    "ds_read_b64_tr_b16 v[40:41], v5 offset:49152\n"
    "ds_read_b64_tr_b16 v[42:43], v5 offset:53248\n"
    "v_mfma_f32_16x16x32_bf16 v[196:199], v[48:51], a[116:119], v[196:199]\n"
    "buffer_load_dwordx4 v31, s[16:19], s97 offen sc1 lds\n"
    "v_mfma_f32_16x16x32_bf16 v[224:227], v[52:55], a[112:115], v[224:227]\n"
    "ds_read_b64_tr_b16 v[44:45], v4 offset:49408\n"
    "v_mfma_f32_16x16x32_bf16 v[228:231], v[52:55], a[116:119], v[228:231]\n"
    "s_waitcnt lgkmcnt(5)\n"
    "ds_read_b64_tr_b16 v[46:47], v4 offset:53504\n"
    "v_mfma_f32_16x16x32_bf16 v[200:203], v[56:59], a[112:115], v[200:203]\n"
    "s_nop 0\n"
    "ds_read_b64_tr_b16 v[48:49], v5 offset:49408\n"
    "v_mfma_f32_16x16x32_bf16 v[204:207], v[56:59], a[116:119], v[204:207]\n"
    "s_add_u32 s97, s92, 15732736\n"
    "ds_read_b64_tr_b16 v[50:51], v5 offset:53504\n"
    "v_mfma_f32_16x16x32_bf16 v[232:235], v[32:35], a[112:115], v[232:235]\n"
    "s_add_u32 m0, s36, 151552\n"
    "ds_read_b64_tr_b16 v[52:53], v4 offset:49664\n"
    "v_mfma_f32_16x16x32_bf16 v[236:239], v[32:35], a[116:119], v[236:239]\n"
    "s_waitcnt lgkmcnt(5)\n"
    "ds_read_b64_tr_b16 v[54:55], v4 offset:53760\n"
    "ds_read_b64_tr_b16 v[56:57], v5 offset:49664\n"
    "v_mfma_f32_16x16x32_bf16 v[176:179], v[36:39], a[120:123], v[176:179]\n"
    "s_nop 0\n"
    "buffer_load_dwordx4 v31, s[16:19], s97 offen sc1 lds\n"
    "v_mfma_f32_16x16x32_bf16 v[180:183], v[36:39], a[124:127], v[180:183]\n"
    "ds_read_b64_tr_b16 v[58:59], v5 offset:53760\n"
    "v_mfma_f32_16x16x32_bf16 v[208:211], v[40:43], a[120:123], v[208:211]\n"
    "ds_read_b64_tr_b16 v[32:33], v4 offset:49920\n"
    "v_mfma_f32_16x16x32_bf16 v[212:215], v[40:43], a[124:127], v[212:215]\n"
    "s_waitcnt lgkmcnt(5)\n"
    "ds_read_b64_tr_b16 v[34:35], v4 offset:54016\n"
    "v_mfma_f32_16x16x32_bf16 v[184:187], v[44:47], a[120:123], v[184:187]\n"
    "s_nop 0\n"
    "ds_read_b64_tr_b16 v[36:37], v5 offset:49920\n"
    "v_mfma_f32_16x16x32_bf16 v[188:191], v[44:47], a[124:127], v[188:191]\n"
    "s_add_u32 s97, s92, 15736832\n"
    "ds_read_b64_tr_b16 v[38:39], v5 offset:54016\n"
    "v_mfma_f32_16x16x32_bf16 v[216:219], v[48:51], a[120:123], v[216:219]\n"
    "s_add_u32 m0, s36, 155648\n"
    "ds_read_b64_tr_b16 v[40:41], v4 offset:57344\n"
    "ds_read_b64_tr_b16 v[42:43], v4 offset:61440\n"
    "v_mfma_f32_16x16x32_bf16 v[220:223], v[48:51], a[124:127], v[220:223]\n"
    "s_waitcnt lgkmcnt(6)\n"
    "buffer_load_dwordx4 v31, s[16:19], s97 offen sc1 lds\n"
    "v_mfma_f32_16x16x32_bf16 v[192:195], v[52:55], a[120:123], v[192:195]\n"
    "s_nop 0\n"
    "ds_read_b64_tr_b16 v[44:45], v5 offset:57344\n"
    "v_mfma_f32_16x16x32_bf16 v[196:199], v[52:55], a[124:127], v[196:199]\n"
    "ds_read_b64_tr_b16 v[46:47], v5 offset:61440\n"
    "v_mfma_f32_16x16x32_bf16 v[224:227], v[56:59], a[120:123], v[224:227]\n"
    "ds_read_b64_tr_b16 v[48:49], v4 offset:57600\n"
    "v_mfma_f32_16x16x32_bf16 v[228:231], v[56:59], a[124:127], v[228:231]\n"
    "s_waitcnt lgkmcnt(5)\n"
    "ds_read_b64_tr_b16 v[50:51], v4 offset:61696\n"
    "v_mfma_f32_16x16x32_bf16 v[200:203], v[32:35], a[120:123], v[200:203]\n"
    "s_mov_b32 s97, 0\n"
    "ds_read_b64_tr_b16 v[52:53], v5 offset:57600\n"
    "v_mfma_f32_16x16x32_bf16 v[204:207], v[32:35], a[124:127], v[204:207]\n"
    "s_add_u32 m0, s36, 131072\n"
    "ds_read_b64_tr_b16 v[54:55], v5 offset:61696\n"
    "ds_read_b64_tr_b16 v[56:57], v4 offset:57856\n"
    "v_mfma_f32_16x16x32_bf16 v[232:235], v[36:39], a[120:123], v[232:235]\n"
    "buffer_load_dwordx4 v21, s[20:23], s97 offen lds\n"
    "v_mfma_f32_16x16x32_bf16 v[236:239], v[36:39], a[124:127], v[236:239]\n"
    "s_waitcnt lgkmcnt(5)\n"
    "ds_read_b64_tr_b16 v[58:59], v4 offset:61952\n"
    "v_mfma_f32_16x16x32_bf16 v[176:179], v[40:43], a[128:131], v[176:179]\n"
    "s_nop 0\n"
    "ds_read_b64_tr_b16 v[32:33], v5 offset:57856\n"
    "v_mfma_f32_16x16x32_bf16 v[180:183], v[40:43], a[132:135], v[180:183]\n"
    "s_add_u32 s97, s92, 15740928\n"
    "ds_read_b64_tr_b16 v[34:35], v5 offset:61952\n"
    "v_mfma_f32_16x16x32_bf16 v[208:211], v[44:47], a[128:131], v[208:211]\n"
    "s_add_u32 m0, s36, 159744\n"
    "ds_read_b64_tr_b16 v[36:37], v4 offset:58112\n"
    "v_mfma_f32_16x16x32_bf16 v[212:215], v[44:47], a[132:135], v[212:215]\n"
    "s_waitcnt lgkmcnt(5)\n"
    "ds_read_b64_tr_b16 v[38:39], v4 offset:62208\n"
    "v_mfma_f32_16x16x32_bf16 v[184:187], v[48:51], a[128:131], v[184:187]\n"
    "s_barrier\n"
    "ds_read_b64_tr_b16 v[40:41], v5 offset:58112\n"
    "ds_read_b64_tr_b16 v[42:43], v5 offset:62208\n"
    "v_mfma_f32_16x16x32_bf16 v[188:191], v[48:51], a[132:135], v[188:191]\n"
    "buffer_load_dwordx4 v31, s[16:19], s97 offen sc1 lds\n"
    "v_mfma_f32_16x16x32_bf16 v[216:219], v[52:55], a[128:131], v[216:219]\n"
    "ds_read_b64_tr_b16 v[44:45], v2 offset:0\n"
    "v_mfma_f32_16x16x32_bf16 v[220:223], v[52:55], a[132:135], v[220:223]\n"
    "s_waitcnt lgkmcnt(5)\n"
    "ds_read_b64_tr_b16 v[46:47], v2 offset:4096\n"
    "v_mfma_f32_16x16x32_bf16 v[192:195], v[56:59], a[128:131], v[192:195]\n"
    "s_nop 0\n"
    "ds_read_b64_tr_b16 v[48:49], v3 offset:0\n"
    "v_mfma_f32_16x16x32_bf16 v[196:199], v[56:59], a[132:135], v[196:199]\n"
    "s_mov_b32 s97, 16384\n"
    "ds_read_b64_tr_b16 v[50:51], v3 offset:4096\n"
    "v_mfma_f32_16x16x32_bf16 v[224:227], v[32:35], a[128:131], v[224:227]\n"
    "s_add_u32 m0, s36, 135168\n"
    "ds_read_b64_tr_b16 v[52:53], v2 offset:256\n"
    "v_mfma_f32_16x16x32_bf16 v[228:231], v[32:35], a[132:135], v[228:231]\n"
    "s_waitcnt lgkmcnt(5)\n"
    "ds_read_b64_tr_b16 v[54:55], v2 offset:4352\n"
    "ds_read_b64_tr_b16 v[56:57], v3 offset:256\n"
    "v_mfma_f32_16x16x32_bf16 v[200:203], v[36:39], a[128:131], v[200:203]\n"
    "s_nop 0\n"
    "buffer_load_dwordx4 v21, s[20:23], s97 offen lds\n"
    "v_mfma_f32_16x16x32_bf16 v[204:207], v[36:39], a[132:135], v[204:207]\n"
    "ds_read_b64_tr_b16 v[58:59], v3 offset:4352\n"
    "v_mfma_f32_16x16x32_bf16 v[232:235], v[40:43], a[128:131], v[232:235]\n"
    "ds_read_b64_tr_b16 v[32:33], v2 offset:512\n"
    "v_mfma_f32_16x16x32_bf16 v[236:239], v[40:43], a[132:135], v[236:239]\n"
    "s_waitcnt lgkmcnt(5)\n"
    "ds_read_b64_tr_b16 v[34:35], v2 offset:4608\n"
    "v_mfma_f32_16x16x32_bf16 v[176:179], v[44:47], a[136:139], v[176:179]\n"
    "s_nop 0\n"
    "ds_read_b64_tr_b16 v[36:37], v3 offset:512\n"
    "v_mfma_f32_16x16x32_bf16 v[180:183], v[44:47], a[140:143], v[180:183]\n"
    "s_mov_b32 s97, 32768\n"
    "ds_read_b64_tr_b16 v[38:39], v3 offset:4608\n"
    "v_mfma_f32_16x16x32_bf16 v[208:211], v[48:51], a[136:139], v[208:211]\n"
    "s_add_u32 m0, s36, 139264\n"
    "ds_read_b64_tr_b16 v[40:41], v2 offset:768\n"
    "ds_read_b64_tr_b16 v[42:43], v2 offset:4864\n"
    "v_mfma_f32_16x16x32_bf16 v[212:215], v[48:51], a[140:143], v[212:215]\n"
    "s_waitcnt lgkmcnt(6)\n"
    "buffer_load_dwordx4 v21, s[20:23], s97 offen lds\n"
    "v_mfma_f32_16x16x32_bf16 v[184:187], v[52:55], a[136:139], v[184:187]\n"
    "s_nop 0\n"
    "ds_read_b64_tr_b16 v[44:45], v3 offset:768\n"
    "v_mfma_f32_16x16x32_bf16 v[188:191], v[52:55], a[140:143], v[188:191]\n"
    "ds_read_b64_tr_b16 v[46:47], v3 offset:4864\n"
    "v_mfma_f32_16x16x32_bf16 v[216:219], v[56:59], a[136:139], v[216:219]\n"
    "ds_read_b64_tr_b16 v[48:49], v2 offset:8192\n"
    "v_mfma_f32_16x16x32_bf16 v[220:223], v[56:59], a[140:143], v[220:223]\n"
    "s_waitcnt lgkmcnt(5)\n"
    "ds_read_b64_tr_b16 v[50:51], v2 offset:12288\n"
    "v_mfma_f32_16x16x32_bf16 v[192:195], v[32:35], a[136:139], v[192:195]\n"
    "s_mov_b32 s97, 49152\n"
    "ds_read_b64_tr_b16 v[52:53], v3 offset:8192\n"
    "v_mfma_f32_16x16x32_bf16 v[196:199], v[32:35], a[140:143], v[196:199]\n"
    "s_add_u32 m0, s36, 143360\n"
    "ds_read_b64_tr_b16 v[54:55], v3 offset:12288\n"
    "ds_read_b64_tr_b16 v[56:57], v2 offset:8448\n"
    "v_mfma_f32_16x16x32_bf16 v[224:227], v[36:39], a[136:139], v[224:227]\n"
    "s_nop 0\n"
    "buffer_load_dwordx4 v21, s[20:23], s97 offen lds\n"
    "v_mfma_f32_16x16x32_bf16 v[228:231], v[36:39], a[140:143], v[228:231]\n"
    "s_waitcnt lgkmcnt(5)\n"
    "ds_read_b64_tr_b16 v[58:59], v2 offset:12544\n"
    "v_mfma_f32_16x16x32_bf16 v[200:203], v[40:43], a[136:139], v[200:203]\n"
    "s_nop 0\n"
    "ds_read_b64_tr_b16 v[32:33], v3 offset:8448\n"
    "v_mfma_f32_16x16x32_bf16 v[204:207], v[40:43], a[140:143], v[204:207]\n"
    "ds_read_b64_tr_b16 v[34:35], v3 offset:12544\n"
    "v_mfma_f32_16x16x32_bf16 v[232:235], v[44:47], a[136:139], v[232:235]\n"
    "s_add_u32 s84, s58, 0\n"
    "ds_read_b64_tr_b16 v[36:37], v2 offset:8704\n"
    "v_mfma_f32_16x16x32_bf16 v[236:239], v[44:47], a[140:143], v[236:239]\n"
    "s_waitcnt lgkmcnt(5)\n"
    "ds_read_b64_tr_b16 v[38:39], v2 offset:12800\n"
    "v_mfma_f32_16x16x32_bf16 v[176:179], v[48:51], a[144:147], v[176:179]\n"
    "s_add_u32 m0, s36, 65536\n"
    "ds_read_b64_tr_b16 v[40:41], v3 offset:8704\n"
    "ds_read_b64_tr_b16 v[42:43], v3 offset:12800\n"
    "v_mfma_f32_16x16x32_bf16 v[180:183], v[48:51], a[148:151], v[180:183]\n"
    "buffer_load_dwordx4 v18, s[12:15], s84 offen lds\n"
    "v_mfma_f32_16x16x32_bf16 v[208:211], v[52:55], a[144:147], v[208:211]\n"
    "ds_read_b64_tr_b16 v[44:45], v2 offset:8960\n"
    "v_mfma_f32_16x16x32_bf16 v[212:215], v[52:55], a[148:151], v[212:215]\n"
    "s_waitcnt lgkmcnt(5)\n"
    "ds_read_b64_tr_b16 v[46:47], v2 offset:13056\n"
    "v_mfma_f32_16x16x32_bf16 v[184:187], v[56:59], a[144:147], v[184:187]\n"
    "s_add_u32 s84, s61, 0\n"
    "ds_read_b64_tr_b16 v[48:49], v3 offset:8960\n"
    "v_mfma_f32_16x16x32_bf16 v[188:191], v[56:59], a[148:151], v[188:191]\n"
    "s_add_u32 m0, s36, 69632\n"
    "ds_read_b64_tr_b16 v[50:51], v3 offset:13056\n"
    "ds_read_b64_tr_b16 v[52:53], v2 offset:16384\n"
    "v_mfma_f32_16x16x32_bf16 v[216:219], v[32:35], a[144:147], v[216:219]\n"
    "buffer_load_dwordx4 v18, s[12:15], s84 offen lds\n"
    "v_mfma_f32_16x16x32_bf16 v[220:223], v[32:35], a[148:151], v[220:223]\n"
    "s_waitcnt lgkmcnt(5)\n"
    "ds_read_b64_tr_b16 v[54:55], v2 offset:20480\n"
    "v_mfma_f32_16x16x32_bf16 v[192:195], v[36:39], a[144:147], v[192:195]\n"
    "s_add_u32 s84, s55, 65536\n"
    "ds_read_b64_tr_b16 v[56:57], v3 offset:16384\n"
    "v_mfma_f32_16x16x32_bf16 v[196:199], v[36:39], a[148:151], v[196:199]\n"
    "s_add_u32 m0, s36, 73728\n"
    "ds_read_b64_tr_b16 v[58:59], v3 offset:20480\n"
    "ds_read_b64_tr_b16 v[32:33], v2 offset:16640\n"
    "v_mfma_f32_16x16x32_bf16 v[224:227], v[40:43], a[144:147], v[224:227]\n"
    "s_nop 0\n"
    "buffer_load_dwordx4 v18, s[12:15], s84 offen lds\n"
    "v_mfma_f32_16x16x32_bf16 v[228:231], v[40:43], a[148:151], v[228:231]\n"
    "s_waitcnt lgkmcnt(5)\n"
    "ds_read_b64_tr_b16 v[34:35], v2 offset:20736\n"
    "v_mfma_f32_16x16x32_bf16 v[200:203], v[44:47], a[144:147], v[200:203]\n"
    "s_nop 0\n"
    "ds_read_b64_tr_b16 v[36:37], v3 offset:16640\n"
    "v_mfma_f32_16x16x32_bf16 v[204:207], v[44:47], a[148:151], v[204:207]\n"
    "s_add_u32 s84, s58, 65536\n"
    "ds_read_b64_tr_b16 v[38:39], v3 offset:20736\n"
    "v_mfma_f32_16x16x32_bf16 v[232:235], v[48:51], a[144:147], v[232:235]\n"
    "s_add_u32 m0, s36, 77824\n"
    "ds_read_b64_tr_b16 v[40:41], v2 offset:16896\n"
    "ds_read_b64_tr_b16 v[42:43], v2 offset:20992\n"
    "v_mfma_f32_16x16x32_bf16 v[236:239], v[48:51], a[148:151], v[236:239]\n"
    "s_waitcnt lgkmcnt(6)\n"
    "buffer_load_dwordx4 v18, s[12:15], s84 offen lds\n"
    "v_mfma_f32_16x16x32_bf16 v[176:179], v[52:55], a[152:155], v[176:179]\n"
    "s_nop 0\n"
    "ds_read_b64_tr_b16 v[44:45], v3 offset:16896\n"
    "v_mfma_f32_16x16x32_bf16 v[180:183], v[52:55], a[156:159], v[180:183]\n"
    "s_add_u32 s84, s61, 65536\n"
    "ds_read_b64_tr_b16 v[46:47], v3 offset:20992\n"
    "v_mfma_f32_16x16x32_bf16 v[208:211], v[56:59], a[152:155], v[208:211]\n"
    "s_add_u32 m0, s36, 81920\n"
    "ds_read_b64_tr_b16 v[48:49], v2 offset:17152\n"
    "ds_read_b64_tr_b16 v[50:51], v2 offset:21248\n"
    "v_mfma_f32_16x16x32_bf16 v[212:215], v[56:59], a[156:159], v[212:215]\n"
    "s_waitcnt lgkmcnt(6)\n"
    "buffer_load_dwordx4 v18, s[12:15], s84 offen lds\n"
    "v_mfma_f32_16x16x32_bf16 v[184:187], v[32:35], a[152:155], v[184:187]\n"
    "s_nop 0\n"
    "ds_read_b64_tr_b16 v[52:53], v3 offset:17152\n"
    "v_mfma_f32_16x16x32_bf16 v[188:191], v[32:35], a[156:159], v[188:191]\n"
    "s_add_u32 s84, s55, 131072\n"
    "ds_read_b64_tr_b16 v[54:55], v3 offset:21248\n"
    "v_mfma_f32_16x16x32_bf16 v[216:219], v[36:39], a[152:155], v[216:219]\n"
    "s_add_u32 m0, s36, 86016\n"
    "ds_read_b64_tr_b16 v[56:57], v2 offset:24576\n"
    "v_mfma_f32_16x16x32_bf16 v[220:223], v[36:39], a[156:159], v[220:223]\n"
    "s_waitcnt lgkmcnt(5)\n"
    "ds_read_b64_tr_b16 v[58:59], v2 offset:28672\n"
    "ds_read_b64_tr_b16 v[32:33], v3 offset:24576\n"
    "v_mfma_f32_16x16x32_bf16 v[192:195], v[40:43], a[152:155], v[192:195]\n"
    "s_nop 0\n"
    "buffer_load_dwordx4 v18, s[12:15], s84 offen lds\n"
    "v_mfma_f32_16x16x32_bf16 v[196:199], v[40:43], a[156:159], v[196:199]\n"
    "s_add_u32 s84, s58, 131072\n"
    "ds_read_b64_tr_b16 v[34:35], v3 offset:28672\n"
    "v_mfma_f32_16x16x32_bf16 v[224:227], v[44:47], a[152:155], v[224:227]\n"
    "s_add_u32 m0, s36, 90112\n"
    "ds_read_b64_tr_b16 v[36:37], v2 offset:24832\n"
    "v_mfma_f32_16x16x32_bf16 v[228:231], v[44:47], a[156:159], v[228:231]\n"
    "s_waitcnt lgkmcnt(5)\n"
    "ds_read_b64_tr_b16 v[38:39], v2 offset:28928\n"
    "ds_read_b64_tr_b16 v[40:41], v3 offset:24832\n"
    "v_mfma_f32_16x16x32_bf16 v[200:203], v[48:51], a[152:155], v[200:203]\n"
    "s_nop 0\n"
    "buffer_load_dwordx4 v18, s[12:15], s84 offen lds\n"
    "v_mfma_f32_16x16x32_bf16 v[204:207], v[48:51], a[156:159], v[204:207]\n"
    "ds_read_b64_tr_b16 v[42:43], v3 offset:28928\n"
    "v_mfma_f32_16x16x32_bf16 v[232:235], v[52:55], a[152:155], v[232:235]\n"
    "s_add_u32 s84, s61, 131072\n"
    "ds_read_b64_tr_b16 v[44:45], v2 offset:25088\n"
    "v_mfma_f32_16x16x32_bf16 v[236:239], v[52:55], a[156:159], v[236:239]\n"
    "s_waitcnt vmcnt(23) lgkmcnt(5)\n"
    "ds_read_b64_tr_b16 v[46:47], v2 offset:29184\n"
    "v_mfma_f32_16x16x32_bf16 v[176:179], v[56:59], a[160:163], v[176:179]\n"
    "s_add_u32 m0, s36, 94208\n"
    "ds_read_b64_tr_b16 v[48:49], v3 offset:25088\n"
    "ds_read_b64_tr_b16 v[50:51], v3 offset:29184\n"
    "v_mfma_f32_16x16x32_bf16 v[180:183], v[56:59], a[164:167], v[180:183]\n"
    "s_nop 0\n"
    "buffer_load_dwordx4 v18, s[12:15], s84 offen lds\n"
    "v_mfma_f32_16x16x32_bf16 v[208:211], v[32:35], a[160:163], v[208:211]\n"
    "s_add_u32 m0, s36, 98304\n"
    "ds_read_b64_tr_b16 v[52:53], v2 offset:25344\n"
    "v_mfma_f32_16x16x32_bf16 v[212:215], v[32:35], a[164:167], v[212:215]\n"
    "s_waitcnt vmcnt(11) lgkmcnt(5)\n"
    "ds_read_b64_tr_b16 v[54:55], v2 offset:29440\n"
    "v_mfma_f32_16x16x32_bf16 v[184:187], v[36:39], a[160:163], v[184:187]\n"
    "s_barrier\n"
    "ds_read_b64_tr_b16 v[56:57], v3 offset:25344\n"
    "ds_read_b64_tr_b16 v[58:59], v3 offset:29440\n"
    "v_mfma_f32_16x16x32_bf16 v[188:191], v[36:39], a[164:167], v[188:191]\n"
    "s_add_u32 s84, s55, 196608\n"
    "buffer_load_dwordx4 v18, s[12:15], s84 offen lds\n"
    "v_mfma_f32_16x16x32_bf16 v[216:219], v[40:43], a[160:163], v[216:219]\n"
    "ds_read_b64_tr_b16 v[32:33], v6 offset:32768\n"
    "v_mfma_f32_16x16x32_bf16 v[220:223], v[40:43], a[164:167], v[220:223]\n"
    "s_waitcnt lgkmcnt(5)\n"
    "ds_read_b64_tr_b16 v[34:35], v6 offset:34816\n"
    "v_mfma_f32_16x16x32_bf16 v[192:195], v[44:47], a[160:163], v[192:195]\n"
    "s_add_u32 s84, s58, 196608\n"
    "ds_read_b64_tr_b16 v[36:37], v7 offset:32768\n"
    "v_mfma_f32_16x16x32_bf16 v[196:199], v[44:47], a[164:167], v[196:199]\n"
    "s_add_u32 m0, s36, 102400\n"
    "ds_read_b64_tr_b16 v[38:39], v7 offset:34816\n"
    "ds_read_b64_tr_b16 v[40:41], v6 offset:33024\n"
    "v_mfma_f32_16x16x32_bf16 v[224:227], v[48:51], a[160:163], v[224:227]\n"
    "s_nop 0\n"
    "buffer_load_dwordx4 v18, s[12:15], s84 offen lds\n"
    "v_mfma_f32_16x16x32_bf16 v[228:231], v[48:51], a[164:167], v[228:231]\n"
    "s_waitcnt lgkmcnt(5)\n"
    "ds_read_b64_tr_b16 v[42:43], v6 offset:35072\n"
    "v_mfma_f32_16x16x32_bf16 v[200:203], v[52:55], a[160:163], v[200:203]\n"
    "s_add_u32 s84, s61, 196608\n"
    "ds_read_b64_tr_b16 v[44:45], v7 offset:33024\n"
    "v_mfma_f32_16x16x32_bf16 v[204:207], v[52:55], a[164:167], v[204:207]\n"
    "s_add_u32 m0, s36, 106496\n"
    "ds_read_b64_tr_b16 v[46:47], v7 offset:35072\n"
    "ds_read_b64_tr_b16 v[48:49], v6 offset:36864\n"
    "v_mfma_f32_16x16x32_bf16 v[232:235], v[56:59], a[160:163], v[232:235]\n"
    "s_nop 0\n"
    "buffer_load_dwordx4 v18, s[12:15], s84 offen lds\n"
    "v_mfma_f32_16x16x32_bf16 v[236:239], v[56:59], a[164:167], v[236:239]\n"
    "s_waitcnt lgkmcnt(5)\n"
    "ds_read_b64_tr_b16 v[50:51], v6 offset:38912\n"
    "ds_read_b64 a[208:209], v15 offset:8\n"
    "ds_read_b64 a[210:211], v15 offset:4104\n"
    "ds_read_b64 a[212:213], v15 offset:8200\n"
    "ds_read_b64 a[214:215], v15 offset:12296\n"
    "ds_read_b64 a[200:201], v15 offset:0\n"
    "ds_read_b64 a[202:203], v15 offset:4096\n"
    "ds_read_b64 a[204:205], v15 offset:8192\n"
    "ds_read_b64 a[206:207], v15 offset:12288\n"
    "s_add_u32 s84, s56, 0\n"
    "ds_read_b128 v[168:171], v172\n"
    "v_cvt_pk_bf16_f32 v176, v176, v177\n"
    "v_cvt_pk_bf16_f32 v177, v178, v179\n"
    "v_cvt_pk_bf16_f32 v178, v208, v209\n"
    "s_add_u32 m0, s36, 110592\n"
    "v_cvt_pk_bf16_f32 v179, v210, v211\n"
    "v_cvt_pk_bf16_f32 v180, v180, v181\n"
    "v_cvt_pk_bf16_f32 v181, v182, v183\n"
    "v_cvt_pk_bf16_f32 v182, v212, v213\n"
    "buffer_load_dwordx4 v18, s[12:15], s84 offen lds\n"
    "v_cvt_pk_bf16_f32 v183, v214, v215\n"
    "v_cvt_pk_bf16_f32 v184, v184, v185\n"
    "v_cvt_pk_bf16_f32 v185, v186, v187\n"
    "v_cvt_pk_bf16_f32 v186, v216, v217\n"
    "v_cvt_pk_bf16_f32 v187, v218, v219\n"
    "s_waitcnt lgkmcnt(7)\n"
    "v_mfma_f32_32x32x16_bf16 v[152:167], a[208:211], a[216:219], 0\n"
    "s_add_u32 s84, s59, 0\n"
    "v_cvt_pk_bf16_f32 v188, v188, v189\n"
    "v_cvt_pk_bf16_f32 v189, v190, v191\n"
    "v_cvt_pk_bf16_f32 v190, v220, v221\n"
    "s_add_u32 m0, s36, 114688\n"
    "v_cvt_pk_bf16_f32 v191, v222, v223\n"
    "v_cvt_pk_bf16_f32 v192, v192, v193\n"
    "v_mfma_f32_32x32x16_bf16 v[240:255], a[208:211], a[224:227], 0\n"
    "buffer_load_dwordx4 v18, s[12:15], s84 offen lds\n"
    "v_cvt_pk_bf16_f32 v193, v194, v195\n"
    "v_cvt_pk_bf16_f32 v194, v224, v225\n"
    "v_cvt_pk_bf16_f32 v195, v226, v227\n"
    "v_cvt_pk_bf16_f32 v196, v196, v197\n"
    "v_cvt_pk_bf16_f32 v197, v198, v199\n"
    "s_waitcnt lgkmcnt(5)\n"
    "v_mfma_f32_32x32x16_bf16 v[152:167], a[212:215], a[220:223], v[152:167]\n"
    "v_cvt_pk_bf16_f32 v198, v228, v229\n"
    "v_cvt_pk_bf16_f32 v199, v230, v231\n"
    "v_cvt_pk_bf16_f32 v200, v200, v201\n"
    "s_add_u32 s84, s62, 0\n"
    "v_cvt_pk_bf16_f32 v201, v202, v203\n"
    "v_cvt_pk_bf16_f32 v202, v232, v233\n"
    "v_mfma_f32_32x32x16_bf16 v[240:255], a[212:215], a[228:231], v[240:255]\n"
    "s_add_u32 m0, s36, 118784\n"
    "v_cvt_pk_bf16_f32 v203, v234, v235\n"
    "v_cvt_pk_bf16_f32 v204, v204, v205\n"
    "v_cvt_pk_bf16_f32 v205, v206, v207\n"
    "buffer_load_dwordx4 v18, s[12:15], s84 offen lds\n"
    "v_cvt_pk_bf16_f32 v206, v236, v237\n"
    "v_cvt_pk_bf16_f32 v207, v238, v239\n"
    "s_waitcnt lgkmcnt(3)\n"
    "v_mfma_f32_32x32x16_bf16 v[208:223], a[200:203], a[216:219], 0\n"
    "s_waitcnt lgkmcnt(0)\n"
    "v_and_b32 v168, 0xffff0000, v168\n"
    "v_lshlrev_b32_e64 v169, 16, v170\n"
    "v_mfma_f32_32x32x16_bf16 v[224:239], a[200:203], a[224:227], 0\n"
    "s_add_u32 s86, s38, 0\n"
    "v_and_b32 v170, 0xffff0000, v170\n"
    "v_mul_f32 v169, s51, v169\n"
    "buffer_store_dwordx4 v[176:179], v20, s[16:19], s86 offen offset:0 sc1\n"
    "v_mfma_f32_32x32x16_bf16 v[208:223], a[204:207], a[220:223], v[208:223]\n"
    "v_fmac_f32_e64 v169, s50, v170\n"
    "v_mfma_f32_32x32x16_bf16 v[224:239], a[204:207], a[228:231], v[224:239]\n"
    "v_pk_mul_f32 v[172:173], v[152:153], s[50:51] op_sel:[0,1]\n"
    "s_add_u32 s84, s56, 65536\n"
    "v_pk_mul_f32 v[152:153], v[152:153], s[50:51] op_sel_hi:[1,0]\n"
    "v_pk_fma_f32 v[152:153], v[240:241], s[50:51], v[152:153] op_sel:[0,1,0] neg_lo:[0,1,0] neg_hi:[0,1,0]\n"
    "v_pk_fma_f32 v[240:241], v[240:241], s[50:51], v[172:173] op_sel_hi:[1,0,1]\n"
    "v_pk_mul_f32 v[174:175], v[154:155], s[50:51] op_sel:[0,1]\n"
    "s_add_u32 m0, s36, 122880\n"
    "v_pk_mul_f32 v[154:155], v[154:155], s[50:51] op_sel_hi:[1,0]\n"
    "v_pk_fma_f32 v[154:155], v[242:243], s[50:51], v[154:155] op_sel:[0,1,0] neg_lo:[0,1,0] neg_hi:[0,1,0]\n"
    "v_pk_fma_f32 v[242:243], v[242:243], s[50:51], v[174:175] op_sel_hi:[1,0,1]\n"
    "v_pk_mul_f32 v[172:173], v[156:157], s[50:51] op_sel:[0,1]\n"
    "buffer_load_dwordx4 v18, s[12:15], s84 offen lds\n"
    "v_pk_mul_f32 v[156:157], v[156:157], s[50:51] op_sel_hi:[1,0]\n"
    "v_pk_fma_f32 v[156:157], v[244:245], s[50:51], v[156:157] op_sel:[0,1,0] neg_lo:[0,1,0] neg_hi:[0,1,0]\n"
    "v_pk_fma_f32 v[244:245], v[244:245], s[50:51], v[172:173] op_sel_hi:[1,0,1]\n"
    "v_pk_mul_f32 v[174:175], v[158:159], s[50:51] op_sel:[0,1]\n"
    "v_pk_mul_f32 v[158:159], v[158:159], s[50:51] op_sel_hi:[1,0]\n"
    "v_pk_fma_f32 v[158:159], v[246:247], s[50:51], v[158:159] op_sel:[0,1,0] neg_lo:[0,1,0] neg_hi:[0,1,0]\n"
    "v_pk_fma_f32 v[246:247], v[246:247], s[50:51], v[174:175] op_sel_hi:[1,0,1]\n"
    "v_pk_mul_f32 v[172:173], v[160:161], s[50:51] op_sel:[0,1]\n"
    "v_pk_mul_f32 v[160:161], v[160:161], s[50:51] op_sel_hi:[1,0]\n"
    "v_pk_fma_f32 v[160:161], v[248:249], s[50:51], v[160:161] op_sel:[0,1,0] neg_lo:[0,1,0] neg_hi:[0,1,0]\n"
    "v_pk_fma_f32 v[248:249], v[248:249], s[50:51], v[172:173] op_sel_hi:[1,0,1]\n"
    "v_pk_mul_f32 v[174:175], v[162:163], s[50:51] op_sel:[0,1]\n"
    "v_pk_mul_f32 v[162:163], v[162:163], s[50:51] op_sel_hi:[1,0]\n"
    "v_pk_fma_f32 v[162:163], v[250:251], s[50:51], v[162:163] op_sel:[0,1,0] neg_lo:[0,1,0] neg_hi:[0,1,0]\n"
    "v_pk_fma_f32 v[250:251], v[250:251], s[50:51], v[174:175] op_sel_hi:[1,0,1]\n"
    "v_pk_mul_f32 v[172:173], v[164:165], s[50:51] op_sel:[0,1]\n"
    "v_pk_mul_f32 v[164:165], v[164:165], s[50:51] op_sel_hi:[1,0]\n"
    "v_pk_fma_f32 v[164:165], v[252:253], s[50:51], v[164:165] op_sel:[0,1,0] neg_lo:[0,1,0] neg_hi:[0,1,0]\n"
    "v_pk_fma_f32 v[252:253], v[252:253], s[50:51], v[172:173] op_sel_hi:[1,0,1]\n"
    "v_pk_mul_f32 v[174:175], v[166:167], s[50:51] op_sel:[0,1]\n"
    "buffer_store_dwordx4 v[180:183], v20, s[16:19], s86 offen offset:1024 sc1\n"
    "v_pk_mul_f32 v[166:167], v[166:167], s[50:51] op_sel_hi:[1,0]\n"
    "v_pk_fma_f32 v[166:167], v[254:255], s[50:51], v[166:167] op_sel:[0,1,0] neg_lo:[0,1,0] neg_hi:[0,1,0]\n"
    "v_pk_fma_f32 v[254:255], v[254:255], s[50:51], v[174:175] op_sel_hi:[1,0,1]\n"
    "v_fmac_f32 v208, v168, v16\n"
    "v_fmac_f32 v152, v169, v16\n"
    "v_pk_mul_f32 v[172:173], v[208:209], v[136:137]\n"
    "v_pk_mul_f32 v[208:209], v[208:209], v[120:121]\n"
    "v_pk_fma_f32 v[208:209], v[224:225], v[136:137], v[208:209] neg_lo:[0,1,0] neg_hi:[0,1,0]\n"
    "v_pk_fma_f32 v[224:225], v[224:225], v[120:121], v[172:173]\n"
    "v_pk_mul_f32 v[174:175], v[210:211], v[138:139]\n"
    "v_pk_mul_f32 v[210:211], v[210:211], v[122:123]\n"
    "v_pk_fma_f32 v[210:211], v[226:227], v[138:139], v[210:211] neg_lo:[0,1,0] neg_hi:[0,1,0]\n"
    "v_pk_fma_f32 v[226:227], v[226:227], v[122:123], v[174:175]\n"
    "v_pk_mul_f32 v[172:173], v[212:213], v[140:141]\n"
    "v_pk_mul_f32 v[212:213], v[212:213], v[124:125]\n"
    "v_pk_fma_f32 v[212:213], v[228:229], v[140:141], v[212:213] neg_lo:[0,1,0] neg_hi:[0,1,0]\n"
    "v_pk_fma_f32 v[228:229], v[228:229], v[124:125], v[172:173]\n"
    "v_pk_mul_f32 v[174:175], v[214:215], v[142:143]\n"
    "v_pk_mul_f32 v[214:215], v[214:215], v[126:127]\n"
    "v_pk_fma_f32 v[214:215], v[230:231], v[142:143], v[214:215] neg_lo:[0,1,0] neg_hi:[0,1,0]\n"
    "s_add_u32 s84, s59, 65536\n"
    "v_pk_fma_f32 v[230:231], v[230:231], v[126:127], v[174:175]\n"
    "v_pk_mul_f32 v[172:173], v[216:217], v[144:145]\n"
    "v_pk_mul_f32 v[216:217], v[216:217], v[128:129]\n"
    "v_pk_fma_f32 v[216:217], v[232:233], v[144:145], v[216:217] neg_lo:[0,1,0] neg_hi:[0,1,0]\n"
    "s_add_u32 m0, s36, 126976\n"
    "v_pk_fma_f32 v[232:233], v[232:233], v[128:129], v[172:173]\n"
    "v_pk_mul_f32 v[174:175], v[218:219], v[146:147]\n"
    "v_pk_mul_f32 v[218:219], v[218:219], v[130:131]\n"
    "v_pk_fma_f32 v[218:219], v[234:235], v[146:147], v[218:219] neg_lo:[0,1,0] neg_hi:[0,1,0]\n"
    "buffer_load_dwordx4 v18, s[12:15], s84 offen lds\n"
    "v_pk_fma_f32 v[234:235], v[234:235], v[130:131], v[174:175]\n"
    "v_pk_mul_f32 v[172:173], v[220:221], v[148:149]\n"
    "v_pk_mul_f32 v[220:221], v[220:221], v[132:133]\n"
    "v_pk_fma_f32 v[220:221], v[236:237], v[148:149], v[220:221] neg_lo:[0,1,0] neg_hi:[0,1,0]\n"
    "v_pk_fma_f32 v[236:237], v[236:237], v[132:133], v[172:173]\n"
    "v_pk_mul_f32 v[174:175], v[222:223], v[150:151]\n"
    "v_pk_mul_f32 v[222:223], v[222:223], v[134:135]\n"
    "v_pk_fma_f32 v[222:223], v[238:239], v[150:151], v[222:223] neg_lo:[0,1,0] neg_hi:[0,1,0]\n"
    "v_pk_fma_f32 v[238:239], v[238:239], v[134:135], v[174:175]\n"
    "v_pk_mul_f32 v[172:173], v[152:153], v[136:137]\n"
    "v_pk_mul_f32 v[152:153], v[152:153], v[120:121]\n"
    "v_pk_fma_f32 v[152:153], v[240:241], v[136:137], v[152:153] neg_lo:[0,1,0] neg_hi:[0,1,0]\n"
    "s_add_u32 s86, s38, 8192\n"
    "v_pk_fma_f32 v[240:241], v[240:241], v[120:121], v[172:173]\n"
    "v_pk_mul_f32 v[174:175], v[154:155], v[138:139]\n"
    "v_pk_mul_f32 v[154:155], v[154:155], v[122:123]\n"
    "v_pk_fma_f32 v[154:155], v[242:243], v[138:139], v[154:155] neg_lo:[0,1,0] neg_hi:[0,1,0]\n"
    "buffer_store_dwordx4 v[184:187], v20, s[16:19], s86 offen offset:0 sc1\n"
    "v_pk_fma_f32 v[242:243], v[242:243], v[122:123], v[174:175]\n"
    "v_pk_mul_f32 v[172:173], v[156:157], v[140:141]\n"
    "v_pk_mul_f32 v[156:157], v[156:157], v[124:125]\n"
    "v_pk_fma_f32 v[156:157], v[244:245], v[140:141], v[156:157] neg_lo:[0,1,0] neg_hi:[0,1,0]\n"
    "v_pk_fma_f32 v[244:245], v[244:245], v[124:125], v[172:173]\n"
    "v_pk_mul_f32 v[174:175], v[158:159], v[142:143]\n"
    "v_pk_mul_f32 v[158:159], v[158:159], v[126:127]\n"
    "v_pk_fma_f32 v[158:159], v[246:247], v[142:143], v[158:159] neg_lo:[0,1,0] neg_hi:[0,1,0]\n"
    "v_pk_fma_f32 v[246:247], v[246:247], v[126:127], v[174:175]\n"
    "v_pk_mul_f32 v[172:173], v[160:161], v[144:145]\n"
    "v_pk_mul_f32 v[160:161], v[160:161], v[128:129]\n"
    "v_pk_fma_f32 v[160:161], v[248:249], v[144:145], v[160:161] neg_lo:[0,1,0] neg_hi:[0,1,0]\n"
    "v_pk_fma_f32 v[248:249], v[248:249], v[128:129], v[172:173]\n"
    "v_pk_mul_f32 v[174:175], v[162:163], v[146:147]\n"
    "v_pk_mul_f32 v[162:163], v[162:163], v[130:131]\n"
    "v_pk_fma_f32 v[162:163], v[250:251], v[146:147], v[162:163] neg_lo:[0,1,0] neg_hi:[0,1,0]\n"
    "v_pk_fma_f32 v[250:251], v[250:251], v[130:131], v[174:175]\n"
    "v_pk_mul_f32 v[172:173], v[164:165], v[148:149]\n"
    "v_pk_mul_f32 v[164:165], v[164:165], v[132:133]\n"
    "v_pk_fma_f32 v[164:165], v[252:253], v[148:149], v[164:165] neg_lo:[0,1,0] neg_hi:[0,1,0]\n"
    "s_add_u32 s84, s62, 65536\n"
    "v_pk_fma_f32 v[252:253], v[252:253], v[132:133], v[172:173]\n"
    "v_pk_mul_f32 v[174:175], v[166:167], v[150:151]\n"
    "v_pk_mul_f32 v[166:167], v[166:167], v[134:135]\n"
    "v_pk_fma_f32 v[166:167], v[254:255], v[150:151], v[166:167] neg_lo:[0,1,0] neg_hi:[0,1,0]\n"
    "s_add_u32 m0, s36, 0\n"
    "s_nop 0\n"
    "v_pk_fma_f32 v[254:255], v[254:255], v[134:135], v[174:175]\n"
    "v_cvt_pk_bf16_f32 v120, v208, v209\n"
    "v_cvt_pk_bf16_f32 v121, v210, v211\n"
    "v_cvt_pk_bf16_f32 v122, v212, v213\n"
    "buffer_load_dwordx4 v18, s[12:15], s84 offen lds\n"
    "v_cvt_pk_bf16_f32 v123, v214, v215\n"
    "v_cvt_pk_bf16_f32 v124, v216, v217\n"
    "v_cvt_pk_bf16_f32 v125, v218, v219\n"
    "v_cvt_pk_bf16_f32 v126, v220, v221\n"
    "v_cvt_pk_bf16_f32 v127, v222, v223\n"
    "v_cvt_pk_bf16_f32 v128, v224, v225\n"
    "v_cvt_pk_bf16_f32 v129, v226, v227\n"
    "v_cvt_pk_bf16_f32 v130, v228, v229\n"
    "v_cvt_pk_bf16_f32 v131, v230, v231\n"
    "v_cvt_pk_bf16_f32 v132, v232, v233\n"
    "v_cvt_pk_bf16_f32 v133, v234, v235\n"
    "v_cvt_pk_bf16_f32 v134, v236, v237\n"
    "v_cvt_pk_bf16_f32 v135, v238, v239\n"
    "v_mfma_f32_32x32x16_bf16 v[208:223], v[120:123], a[168:171], 0\n"
    "v_cvt_pk_bf16_f32 v136, v152, v153\n"
    "v_cvt_pk_bf16_f32 v137, v154, v155\n"
    "v_cvt_pk_bf16_f32 v138, v156, v157\n"
    "v_cvt_pk_bf16_f32 v139, v158, v159\n"
    "buffer_store_dwordx4 v[188:191], v20, s[16:19], s86 offen offset:1024 sc1\n"
    "v_mfma_f32_32x32x16_bf16 v[208:223], v[128:131], a[176:179], v[208:223]\n"
    "v_cvt_pk_bf16_f32 v140, v160, v161\n"
    "v_cvt_pk_bf16_f32 v141, v162, v163\n"
    "v_cvt_pk_bf16_f32 v142, v164, v165\n"
    "v_cvt_pk_bf16_f32 v143, v166, v167\n"
    "v_mfma_f32_32x32x16_bf16 v[208:223], v[124:127], a[172:175], v[208:223]\n"
    "v_cvt_pk_bf16_f32 v144, v240, v241\n"
    "v_cvt_pk_bf16_f32 v145, v242, v243\n"
    "v_cvt_pk_bf16_f32 v146, v244, v245\n"
    "v_cvt_pk_bf16_f32 v147, v246, v247\n"
    "v_mfma_f32_32x32x16_bf16 v[208:223], v[132:135], a[180:183], v[208:223]\n"
    "v_cvt_pk_bf16_f32 v148, v248, v249\n"
    "LPAD10_%=:\n"
    "s_cbranch_execz LPAD14_%=\n"
    "s_add_u32 s84, s57, 0\n"
    "v_cvt_pk_bf16_f32 v149, v250, v251\n"
    "v_cvt_pk_bf16_f32 v150, v252, v253\n"
    "v_cvt_pk_bf16_f32 v151, v254, v255\n"
    "s_add_u32 m0, s36, 4096\n"
    "v_mfma_f32_32x32x16_bf16 v[224:239], v[136:139], a[168:171], 0\n"
    "buffer_load_dwordx4 v18, s[12:15], s84 offen lds\n"
    "v_mfma_f32_32x32x16_bf16 v[224:239], v[144:147], a[176:179], v[224:239]\n"
    "s_add_u32 s86, s39, 0\n"
    "v_mfma_f32_32x32x16_bf16 v[224:239], v[140:143], a[172:175], v[224:239]\n"
    "buffer_store_dwordx4 v[192:195], v20, s[16:19], s86 offen offset:0 sc1\n"
    "v_mfma_f32_32x32x16_bf16 v[224:239], v[148:151], a[180:183], v[224:239]\n"
    "s_barrier\n"
    "s_nop 10\n"
    "v_pk_add_f32 v[240:241], v[208:209], v[224:225]\n"
    "v_pk_add_f32 v[208:209], v[208:209], v[224:225] neg_lo:[0,1] neg_hi:[0,1]\n"
    "s_add_u32 s84, s60, 0\n"
    "v_pk_add_f32 v[242:243], v[210:211], v[226:227]\n"
    "v_pk_add_f32 v[210:211], v[210:211], v[226:227] neg_lo:[0,1] neg_hi:[0,1]\n"
    "v_pk_add_f32 v[244:245], v[212:213], v[228:229]\n"
    "v_pk_add_f32 v[212:213], v[212:213], v[228:229] neg_lo:[0,1] neg_hi:[0,1]\n"
    "s_add_u32 m0, s36, 8192\n"
    "v_pk_add_f32 v[246:247], v[214:215], v[230:231]\n"
    "v_pk_add_f32 v[214:215], v[214:215], v[230:231] neg_lo:[0,1] neg_hi:[0,1]\n"
    "v_pk_add_f32 v[248:249], v[216:217], v[232:233]\n"
    "v_pk_add_f32 v[216:217], v[216:217], v[232:233] neg_lo:[0,1] neg_hi:[0,1]\n"
    "buffer_load_dwordx4 v18, s[12:15], s84 offen lds\n"
    "v_pk_add_f32 v[250:251], v[218:219], v[234:235]\n"
    "v_pk_add_f32 v[218:219], v[218:219], v[234:235] neg_lo:[0,1] neg_hi:[0,1]\n"
    "v_pk_add_f32 v[252:253], v[220:221], v[236:237]\n"
    "v_pk_add_f32 v[220:221], v[220:221], v[236:237] neg_lo:[0,1] neg_hi:[0,1]\n"
    "v_pk_add_f32 v[254:255], v[222:223], v[238:239]\n"
    "v_pk_add_f32 v[222:223], v[222:223], v[238:239] neg_lo:[0,1] neg_hi:[0,1]\n"
    "v_cvt_pk_bf16_f32 v152, v240, v241\n"
    "v_cvt_pk_bf16_f32 v153, v242, v243\n"
    "v_cvt_pk_bf16_f32 v154, v248, v249\n"
    "v_cvt_pk_bf16_f32 v155, v250, v251\n"
    "ds_write_b128 v11, v[152:155] offset:0\n"
    "v_cvt_pk_bf16_f32 v156, v244, v245\n"
    "v_cvt_pk_bf16_f32 v157, v246, v247\n"
    "v_cvt_pk_bf16_f32 v158, v252, v253\n"
    "buffer_store_dwordx4 v[196:199], v20, s[16:19], s86 offen offset:1024 sc1\n"
    "v_cvt_pk_bf16_f32 v159, v254, v255\n"
    "ds_write_b128 v11, v[156:159] offset:8192\n"
    "v_cvt_pk_bf16_f32 v160, v208, v209\n"
    "v_cvt_pk_bf16_f32 v161, v210, v211\n"
    "v_cvt_pk_bf16_f32 v162, v216, v217\n"
    "v_cvt_pk_bf16_f32 v163, v218, v219\n"
    "ds_write_b128 v12, v[160:163] offset:0\n"
    "v_cvt_pk_bf16_f32 v164, v212, v213\n"
    "s_add_u32 s84, s63, 0\n"
    "v_cvt_pk_bf16_f32 v165, v214, v215\n"
    "v_cvt_pk_bf16_f32 v166, v220, v221\n"
    "v_cvt_pk_bf16_f32 v167, v222, v223\n"
    "ds_write_b128 v12, v[164:167] offset:8192\n"
    "s_add_u32 m0, s36, 12288\n"
    "s_nop 0\n"
    "buffer_load_dwordx4 v18, s[12:15], s84 offen lds\n"
    "s_waitcnt lgkmcnt(0)\n"
    "s_barrier\n"
    "s_add_u32 s86, s39, 8192\n"
    "ds_read_b128 v[152:155], v13 offset:0\n"
    "ds_read_b128 v[156:159], v13 offset:1024\n"
    "ds_read_b128 v[160:163], v13 offset:2048\n"
    "ds_read_b128 v[164:167], v13 offset:3072\n"
    "buffer_store_dwordx4 v[200:203], v20, s[16:19], s86 offen offset:0 sc1\n"
    "v_mfma_f32_16x16x32_bf16 v[120:123], v[32:35], v[64:67], 0\n"
    "ds_read_b64_tr_b16 v[52:53], v7 offset:36864\n"
    "v_mfma_f32_16x16x32_bf16 v[124:127], v[32:35], v[68:71], 0\n"
    "ds_read_b64_tr_b16 v[54:55], v7 offset:38912\n"
    "v_mfma_f32_16x16x32_bf16 v[136:139], v[36:39], v[64:67], 0\n"
    "ds_read_b64_tr_b16 v[56:57], v6 offset:37120\n"
    "v_mfma_f32_16x16x32_bf16 v[140:143], v[36:39], v[68:71], 0\n"
    "s_waitcnt lgkmcnt(3)\n"
    "ds_read_b64_tr_b16 v[58:59], v6 offset:39168\n"
    "v_mfma_f32_16x16x32_bf16 v[128:131], v[40:43], v[64:67], 0\n"
    "s_add_u32 s97, s95, 0\n"
    "ds_read_b64_tr_b16 v[32:33], v7 offset:37120\n"
    "ds_read_b64_tr_b16 v[34:35], v7 offset:39168\n"
    "v_mfma_f32_16x16x32_bf16 v[132:135], v[40:43], v[68:71], 0\n"
    "buffer_store_dwordx4 v[152:155], v10, s[16:19], s97 offen sc1\n"
    "v_mfma_f32_16x16x32_bf16 v[144:147], v[44:47], v[64:67], 0\n"
    "ds_read_b64_tr_b16 v[36:37], v6 offset:40960\n"
    "v_bfe_u32 v14, v30, 3, 1\n"
    "v_mfma_f32_16x16x32_bf16 v[148:151], v[44:47], v[68:71], 0\n"
    "s_waitcnt lgkmcnt(5)\n"
    "ds_read_b64_tr_b16 v[38:39], v6 offset:43008\n"
    "v_lshl_add_u32 v14, v14, 12, s93\n"
    "v_mfma_f32_16x16x32_bf16 v[120:123], v[48:51], v[72:75], v[120:123]\n"
    "s_nop 0\n"
    "ds_read_b64_tr_b16 v[40:41], v7 offset:40960\n"
    "v_and_b32_e64 v12, 7, v30\n"
    "v_mfma_f32_16x16x32_bf16 v[124:127], v[48:51], v[76:79], v[124:127]\n"
    "ds_read_b64_tr_b16 v[42:43], v7 offset:43008\n"
    "v_lshl_add_u32 v14, v12, 7, v14\n"
    "v_mfma_f32_16x16x32_bf16 v[136:139], v[52:55], v[72:75], v[136:139]\n"
    "ds_read_b64_tr_b16 v[44:45], v6 offset:41216\n"
    "ds_read_b64_tr_b16 v[46:47], v6 offset:43264\n"
    "v_mfma_f32_16x16x32_bf16 v[140:143], v[52:55], v[76:79], v[140:143]\n"
    "s_waitcnt lgkmcnt(6)\n"
    "buffer_store_dwordx4 v[204:207], v20, s[16:19], s86 offen offset:1024 sc1\n"
    "v_mfma_f32_16x16x32_bf16 v[128:131], v[56:59], v[72:75], v[128:131]\n"
    "s_nop 0\n"
    "ds_read_b64_tr_b16 v[48:49], v7 offset:41216\n"
    "v_bfe_u32 v12, v30, 1, 2\n"
    "v_mfma_f32_16x16x32_bf16 v[132:135], v[56:59], v[76:79], v[132:135]\n"
    "ds_read_b64_tr_b16 v[50:51], v7 offset:43264\n"
    "v_lshrrev_b32_e64 v11, 5, v30\n"
    "v_mfma_f32_16x16x32_bf16 v[144:147], v[32:35], v[72:75], v[144:147]\n"
    "ds_read_b64_tr_b16 v[52:53], v6 offset:45056\n"
    "v_xor_b32_e64 v12, v11, v12\n"
    "v_mfma_f32_16x16x32_bf16 v[148:151], v[32:35], v[76:79], v[148:151]\n"
    "s_waitcnt lgkmcnt(5)\n"
    "ds_read_b64_tr_b16 v[54:55], v6 offset:47104\n"
    "v_lshl_add_u32 v14, v12, 4, v14\n"
    "v_mfma_f32_16x16x32_bf16 v[120:123], v[36:39], v[80:83], v[120:123]\n"
    "s_nop 0\n"
    "ds_read_b64_tr_b16 v[56:57], v7 offset:45056\n"
    "v_bfe_u32 v12, v30, 4, 1\n"
    "v_mfma_f32_16x16x32_bf16 v[124:127], v[36:39], v[84:87], v[124:127]\n"
    "s_add_u32 s97, s95, 1048576\n"
    "ds_read_b64_tr_b16 v[58:59], v7 offset:47104\n"
    "ds_read_b64_tr_b16 v[32:33], v6 offset:45312\n"
    "v_mfma_f32_16x16x32_bf16 v[136:139], v[40:43], v[80:83], v[136:139]\n"
    "buffer_store_dwordx4 v[156:159], v10, s[16:19], s97 offen sc1\n"
    "v_mfma_f32_16x16x32_bf16 v[140:143], v[40:43], v[84:87], v[140:143]\n"
    "s_waitcnt lgkmcnt(5)\n"
    "ds_read_b64_tr_b16 v[34:35], v6 offset:47360\n"
    "v_lshl_add_u32 v14, v12, 3, v14\n"
    "v_mfma_f32_16x16x32_bf16 v[128:131], v[44:47], v[80:83], v[128:131]\n"
    "s_nop 0\n"
    "ds_read_b64_tr_b16 v[36:37], v7 offset:45312\n"
    "v_xor_b32_e64 v15, 32, v14\n"
    "v_mfma_f32_16x16x32_bf16 v[132:135], v[44:47], v[84:87], v[132:135]\n"
    "ds_read_b64_tr_b16 v[38:39], v7 offset:47360\n"
    "v_and_b32_e64 v22, 3, v30\n"
    "v_mfma_f32_16x16x32_bf16 v[144:147], v[48:51], v[80:83], v[144:147]\n"
    "ds_read_b64_tr_b16 v[40:41], v6 offset:49152\n"
    "v_lshlrev_b32_e64 v22, 5, v22\n"
    "v_mfma_f32_16x16x32_bf16 v[148:151], v[48:51], v[84:87], v[148:151]\n"
    "s_waitcnt vmcnt(9) lgkmcnt(5)\n"
    "ds_read_b64_tr_b16 v[42:43], v6 offset:51200\n"
    "v_and_b32 v12, 4, v30\n"
    "v_mfma_f32_16x16x32_bf16 v[120:123], v[52:55], v[88:91], v[120:123]\n"
    "s_mov_b32 s83, 13107200\n"
    "ds_read_b64_tr_b16 v[44:45], v7 offset:49152\n"
    "ds_read_b64_tr_b16 v[46:47], v7 offset:51200\n"
    "v_mfma_f32_16x16x32_bf16 v[124:127], v[52:55], v[92:95], v[124:127]\n"
    "buffer_store_dwordx2 v[60:61], v23, s[16:19], s83 offen sc1\n"
    "v_mfma_f32_16x16x32_bf16 v[136:139], v[56:59], v[88:91], v[136:139]\n"
    "ds_read_b64_tr_b16 v[48:49], v6 offset:49408\n"
    "v_lshl_add_u32 v22, v12, 8, v22\n"
    "v_mfma_f32_16x16x32_bf16 v[140:143], v[56:59], v[92:95], v[140:143]\n"
    "s_waitcnt lgkmcnt(5)\n"
    "ds_read_b64_tr_b16 v[50:51], v6 offset:51456\n"
    "v_and_b32 v12, 8, v30\n"
    "v_mfma_f32_16x16x32_bf16 v[128:131], v[32:35], v[88:91], v[128:131]\n"
    "ds_read_b64_tr_b16 v[52:53], v7 offset:49408\n"
    "v_add_u32_e64 v22, v22, v12\n"
    "v_mfma_f32_16x16x32_bf16 v[132:135], v[32:35], v[92:95], v[132:135]\n"
    "s_add_u32 s97, s95, 2097152\n"
    "ds_read_b64_tr_b16 v[54:55], v7 offset:51456\n"
    "ds_read_b64_tr_b16 v[56:57], v6 offset:53248\n"
    "v_mfma_f32_16x16x32_bf16 v[144:147], v[36:39], v[88:91], v[144:147]\n"
    "buffer_store_dwordx4 v[160:163], v10, s[16:19], s97 offen sc1\n"
    "v_mfma_f32_16x16x32_bf16 v[148:151], v[36:39], v[92:95], v[148:151]\n"
    "s_waitcnt lgkmcnt(5)\n"
    "ds_read_b64_tr_b16 v[58:59], v6 offset:55296\n"
    "v_lshl_add_u32 v22, v22, 11, s101\n"
    "v_mfma_f32_16x16x32_bf16 v[120:123], v[40:43], v[96:99], v[120:123]\n"
    "s_nop 0\n"
    "ds_read_b64_tr_b16 v[32:33], v7 offset:53248\n"
    "v_bfe_u32 v12, v30, 4, 1\n"
    "v_mfma_f32_16x16x32_bf16 v[124:127], v[40:43], v[100:103], v[124:127]\n"
    "ds_read_b64_tr_b16 v[34:35], v7 offset:55296\n"
    "v_lshl_add_u32 v22, v12, 5, v22\n"
    "v_mfma_f32_16x16x32_bf16 v[136:139], v[44:47], v[96:99], v[136:139]\n"
    "ds_read_b64_tr_b16 v[36:37], v6 offset:53504\n"
    "v_lshrrev_b32_e64 v12, 5, v30\n"
    "v_mfma_f32_16x16x32_bf16 v[140:143], v[44:47], v[100:103], v[140:143]\n"
    "s_waitcnt lgkmcnt(5)\n"
    "ds_read_b64_tr_b16 v[38:39], v6 offset:55552\n"
    "v_lshl_add_u32 v22, v12, 4, v22\n"
    "v_mfma_f32_16x16x32_bf16 v[128:131], v[48:51], v[96:99], v[128:131]\n"
    "s_add_u32 s97, s95, 3145728\n"
    "ds_read_b64_tr_b16 v[40:41], v7 offset:53504\n"
    "ds_read_b64_tr_b16 v[42:43], v7 offset:55552\n"
    "v_mfma_f32_16x16x32_bf16 v[132:135], v[48:51], v[100:103], v[132:135]\n"
    "s_nop 0\n"
    "buffer_store_dwordx4 v[164:167], v10, s[16:19], s97 offen sc1\n"
    "v_mfma_f32_16x16x32_bf16 v[144:147], v[52:55], v[96:99], v[144:147]\n"
    "ds_read_b64_tr_b16 v[44:45], v6 offset:57344\n"
    "ds_read_b64 v[224:225], v14 offset:0\n"
    "v_mfma_f32_16x16x32_bf16 v[148:151], v[52:55], v[100:103], v[148:151]\n"
    "s_waitcnt lgkmcnt(6)\n"
    "ds_read_b64_tr_b16 v[46:47], v6 offset:59392\n"
    "ds_read_b64 v[226:227], v15 offset:0\n"
    "v_mfma_f32_16x16x32_bf16 v[120:123], v[56:59], v[104:107], v[120:123]\n"
    "s_nop 0\n"
    "ds_read_b64_tr_b16 v[48:49], v7 offset:57344\n"
    "ds_read_b64 v[228:229], v14 offset:8192\n"
    "v_mfma_f32_16x16x32_bf16 v[124:127], v[56:59], v[108:111], v[124:127]\n"
    "ds_read_b64_tr_b16 v[50:51], v7 offset:59392\n"
    "ds_read_b64 v[230:231], v15 offset:8192\n"
    "v_mfma_f32_16x16x32_bf16 v[136:139], v[32:35], v[104:107], v[136:139]\n"
    "ds_read_b64_tr_b16 v[52:53], v6 offset:57600\n"
    "ds_read_b64 v[232:233], v14 offset:64\n"
    "v_mfma_f32_16x16x32_bf16 v[140:143], v[32:35], v[108:111], v[140:143]\n"
    "s_waitcnt lgkmcnt(10)\n"
    "ds_read_b64_tr_b16 v[54:55], v6 offset:59648\n"
    "ds_read_b64 v[234:235], v15 offset:64\n"
    "v_mfma_f32_16x16x32_bf16 v[128:131], v[36:39], v[104:107], v[128:131]\n"
    "s_nop 0\n"
    "ds_read_b64_tr_b16 v[56:57], v7 offset:57600\n"
    "ds_read_b64 v[236:237], v14 offset:8256\n"
    "v_mfma_f32_16x16x32_bf16 v[132:135], v[36:39], v[108:111], v[132:135]\n"
    "ds_read_b64_tr_b16 v[58:59], v7 offset:59648\n"
    "ds_read_b64 v[238:239], v15 offset:8256\n"
    "v_mfma_f32_16x16x32_bf16 v[144:147], v[40:43], v[104:107], v[144:147]\n"
    "ds_read_b64_tr_b16 v[32:33], v6 offset:61440\n"
    "v_mfma_f32_16x16x32_bf16 v[148:151], v[40:43], v[108:111], v[148:151]\n"
    "s_waitcnt lgkmcnt(10)\n"
    "ds_read_b64_tr_b16 v[34:35], v6 offset:63488\n"
    "v_mfma_f32_16x16x32_bf16 v[120:123], v[44:47], v[112:115], v[120:123]\n"
    "s_nop 0\n"
    "ds_read_b64_tr_b16 v[36:37], v7 offset:61440\n"
    "v_mfma_f32_16x16x32_bf16 v[124:127], v[44:47], v[116:119], v[124:127]\n"
    "s_add_u32 s84, s52, 0\n"
    "ds_read_b64_tr_b16 v[38:39], v7 offset:63488\n"
    "v_mfma_f32_16x16x32_bf16 v[136:139], v[48:51], v[112:115], v[136:139]\n"
    "s_add_u32 m0, s36, 16384\n"
    "ds_read_b64_tr_b16 v[40:41], v6 offset:61696\n"
    "v_mfma_f32_16x16x32_bf16 v[140:143], v[48:51], v[116:119], v[140:143]\n"
    "s_waitcnt lgkmcnt(5)\n"
    "ds_read_b64_tr_b16 v[42:43], v6 offset:63744\n"
    "v_mfma_f32_16x16x32_bf16 v[128:131], v[52:55], v[112:115], v[128:131]\n"
    "s_barrier\n"
    "ds_read_b64_tr_b16 v[44:45], v7 offset:61696\n"
    "ds_read_b64_tr_b16 v[46:47], v7 offset:63744\n"
    "v_mfma_f32_16x16x32_bf16 v[132:135], v[52:55], v[116:119], v[132:135]\n"
    "s_nop 0\n"
    "buffer_load_dwordx4 v19, s[12:15], s84 offen lds\n"
    "v_mfma_f32_16x16x32_bf16 v[144:147], v[56:59], v[112:115], v[144:147]\n"
    "s_add_u32 m0, s36, 20480\n"
    "ds_read_b64_tr_b16 v[48:49], v8 offset:0\n"
    "ds_read_b64_tr_b16 v[50:51], v8 offset:2048\n"
    "v_mfma_f32_16x16x32_bf16 v[148:151], v[56:59], v[116:119], v[148:151]\n"
    "s_waitcnt lgkmcnt(6)\n"
    "s_add_u32 s84, s52, 65536\n"
    "buffer_load_dwordx4 v19, s[12:15], s84 offen lds\n"
    "v_mfma_f32_16x16x32_bf16 v[120:123], v[32:35], a[0:3], v[120:123]\n"
    "s_add_u32 m0, s36, 24576\n"
    "ds_read_b64_tr_b16 v[52:53], v9 offset:0\n"
    "ds_read_b64_tr_b16 v[54:55], v9 offset:2048\n"
    "v_mfma_f32_16x16x32_bf16 v[124:127], v[32:35], a[4:7], v[124:127]\n"
    "s_add_u32 s84, s52, 131072\n"
    "buffer_load_dwordx4 v19, s[12:15], s84 offen lds\n"
    "v_mfma_f32_16x16x32_bf16 v[136:139], v[36:39], a[0:3], v[136:139]\n"
    "s_nop 0\n"
    "ds_read_b64_tr_b16 v[56:57], v8 offset:256\n"
    "v_lshlrev_b32_e64 v176, 16, v224\n"
    "v_mfma_f32_16x16x32_bf16 v[140:143], v[36:39], a[4:7], v[140:143]\n"
    "s_waitcnt lgkmcnt(5)\n"
    "ds_read_b64_tr_b16 v[58:59], v8 offset:2304\n"
    "v_and_b32 v177, 0xffff0000, v224\n"
    "v_mfma_f32_16x16x32_bf16 v[128:131], v[40:43], a[0:3], v[128:131]\n"
    "s_add_u32 s84, s52, 196608\n"
    "ds_read_b64_tr_b16 v[32:33], v9 offset:256\n"
    "v_lshlrev_b32 v178, 16, v225\n"
    "v_mfma_f32_16x16x32_bf16 v[132:135], v[40:43], a[4:7], v[132:135]\n"
    "s_add_u32 m0, s36, 28672\n"
    "ds_read_b64_tr_b16 v[34:35], v9 offset:2304\n"
    "ds_read_b64_tr_b16 v[36:37], v8 offset:4096\n"
    "v_mfma_f32_16x16x32_bf16 v[144:147], v[44:47], a[0:3], v[144:147]\n"
    "buffer_load_dwordx4 v19, s[12:15], s84 offen lds\n"
    "v_mfma_f32_16x16x32_bf16 v[148:151], v[44:47], a[4:7], v[148:151]\n"
    "s_waitcnt lgkmcnt(5)\n"
    "ds_read_b64_tr_b16 v[38:39], v8 offset:6144\n"
    "v_and_b32 v179, 0xffff0000, v225\n"
    "v_mfma_f32_16x16x32_bf16 v[120:123], v[48:51], a[8:11], v[120:123]\n"
    "s_add_u32 s84, s53, 0\n"
    "ds_read_b64_tr_b16 v[40:41], v9 offset:4096\n"
    "v_lshlrev_b32_e64 v180, 16, v226\n"
    "v_mfma_f32_16x16x32_bf16 v[124:127], v[48:51], a[12:15], v[124:127]\n"
    "s_add_u32 m0, s36, 32768\n"
    "ds_read_b64_tr_b16 v[42:43], v9 offset:6144\n"
    "ds_read_b64_tr_b16 v[44:45], v8 offset:4352\n"
    "v_mfma_f32_16x16x32_bf16 v[136:139], v[52:55], a[8:11], v[136:139]\n"
    "buffer_load_dwordx4 v19, s[12:15], s84 offen lds\n"
    "v_mfma_f32_16x16x32_bf16 v[140:143], v[52:55], a[12:15], v[140:143]\n"
    "s_waitcnt vmcnt(9) lgkmcnt(5)\n"
    "ds_read_b64_tr_b16 v[46:47], v8 offset:6400\n"
    "v_and_b32 v181, 0xffff0000, v226\n"
    "v_mfma_f32_16x16x32_bf16 v[128:131], v[56:59], a[8:11], v[128:131]\n"
    "s_nop 0\n"
    "ds_read_b64_tr_b16 v[48:49], v9 offset:4352\n"
    "v_lshlrev_b32_e64 v182, 16, v227\n"
    "v_mfma_f32_16x16x32_bf16 v[132:135], v[56:59], a[12:15], v[132:135]\n"
    "ds_read_b64_tr_b16 v[50:51], v9 offset:6400\n"
    "v_and_b32 v183, 0xffff0000, v227\n"
    "v_mfma_f32_16x16x32_bf16 v[144:147], v[32:35], a[8:11], v[144:147]\n"
    "s_mov_b32 s83, 13238272\n"
    "ds_read_b64_tr_b16 v[52:53], v8 offset:8192\n"
    "ds_read_b64_tr_b16 v[54:55], v8 offset:10240\n"
    "v_mfma_f32_16x16x32_bf16 v[148:151], v[32:35], a[12:15], v[148:151]\n"
    "s_waitcnt lgkmcnt(6)\n"
    "buffer_store_dwordx2 v[60:61], v23, s[16:19], s83 offen sc1\n"
    "v_mfma_f32_16x16x32_bf16 v[120:123], v[36:39], a[16:19], v[120:123]\n"
    "s_nop 0\n"
    "ds_read_b64_tr_b16 v[56:57], v9 offset:8192\n"
    "v_lshlrev_b32_e64 v184, 16, v228\n"
    "v_mfma_f32_16x16x32_bf16 v[124:127], v[36:39], a[20:23], v[124:127]\n"
    "ds_read_b64_tr_b16 v[58:59], v9 offset:10240\n"
    "v_and_b32 v185, 0xffff0000, v228\n"
    "v_xor_b32 v25, 0x1000, v23\n"
    "v_mfma_f32_16x16x32_bf16 v[136:139], v[40:43], a[16:19], v[136:139]\n"
    "s_mov_b32 s87, 13107200\n"
    "ds_read_b64_tr_b16 v[32:33], v8 offset:8448\n"
    "ds_read_b64_tr_b16 v[34:35], v8 offset:10496\n"
    "v_mfma_f32_16x16x32_bf16 v[140:143], v[40:43], a[20:23], v[140:143]\n"
    "s_waitcnt lgkmcnt(6)\n"
    "buffer_load_dwordx2 v[62:63], v25, s[16:19], s87 offen sc1\n"
    "v_mfma_f32_16x16x32_bf16 v[128:131], v[44:47], a[16:19], v[128:131]\n"
    "s_nop 0\n"
    "ds_read_b64_tr_b16 v[36:37], v9 offset:8448\n"
    "v_lshlrev_b32_e64 v186, 16, v229\n"
    "v_mfma_f32_16x16x32_bf16 v[132:135], v[44:47], a[20:23], v[132:135]\n"
    "s_add_u32 s84, s53, 65536\n"
    "ds_read_b64_tr_b16 v[38:39], v9 offset:10496\n"
    "v_and_b32 v187, 0xffff0000, v229\n"
    "v_mfma_f32_16x16x32_bf16 v[144:147], v[48:51], a[16:19], v[144:147]\n"
    "s_add_u32 m0, s36, 36864\n"
    "ds_read_b64_tr_b16 v[40:41], v8 offset:12288\n"
    "v_lshlrev_b32_e64 v188, 16, v230\n"
    "v_mfma_f32_16x16x32_bf16 v[148:151], v[48:51], a[20:23], v[148:151]\n"
    "s_waitcnt lgkmcnt(5)\n"
    "ds_read_b64_tr_b16 v[42:43], v8 offset:14336\n"
    "ds_read_b64_tr_b16 v[44:45], v9 offset:12288\n"
    "v_mfma_f32_16x16x32_bf16 v[120:123], v[52:55], a[24:27], v[120:123]\n"
    "s_nop 0\n"
    "buffer_load_dwordx4 v19, s[12:15], s84 offen lds\n"
    "v_mfma_f32_16x16x32_bf16 v[124:127], v[52:55], a[28:31], v[124:127]\n"
    "s_add_u32 s84, s54, 0\n"
    "ds_read_b64_tr_b16 v[46:47], v9 offset:14336\n"
    "v_and_b32 v189, 0xffff0000, v230\n"
    "v_mfma_f32_16x16x32_bf16 v[136:139], v[56:59], a[24:27], v[136:139]\n"
    "s_add_u32 m0, s36, 40960\n"
    "ds_read_b64_tr_b16 v[48:49], v8 offset:12544\n"
    "v_lshlrev_b32 v190, 16, v231\n"
    "v_mfma_f32_16x16x32_bf16 v[140:143], v[56:59], a[28:31], v[140:143]\n"
    "s_waitcnt lgkmcnt(5)\n"
    "ds_read_b64_tr_b16 v[50:51], v8 offset:14592\n"
    "ds_read_b64_tr_b16 v[52:53], v9 offset:12544\n"
    "v_mfma_f32_16x16x32_bf16 v[128:131], v[32:35], a[24:27], v[128:131]\n"
    "s_nop 0\n"
    "buffer_load_dwordx4 v19, s[12:15], s84 offen lds\n"
    "v_mfma_f32_16x16x32_bf16 v[132:135], v[32:35], a[28:31], v[132:135]\n"
    "ds_read_b64_tr_b16 v[54:55], v9 offset:14592\n"
    "v_and_b32 v191, 0xffff0000, v231\n"
    "v_mfma_f32_16x16x32_bf16 v[144:147], v[36:39], a[24:27], v[144:147]\n"
    "s_add_u32 s84, s55, 0\n"
    "ds_read_b64_tr_b16 v[56:57], v8 offset:16384\n"
    "v_lshlrev_b32 v192, 16, v232\n"
    "v_mfma_f32_16x16x32_bf16 v[148:151], v[36:39], a[28:31], v[148:151]\n"
    "s_waitcnt lgkmcnt(5)\n"
    "ds_read_b64_tr_b16 v[58:59], v8 offset:18432\n"
    "v_and_b32 v193, 0xffff0000, v232\n"
    "v_mfma_f32_16x16x32_bf16 v[120:123], v[40:43], a[32:35], v[120:123]\n"
    "s_add_u32 m0, s36, 45056\n"
    "ds_read_b64_tr_b16 v[32:33], v9 offset:16384\n"
    "ds_read_b64_tr_b16 v[34:35], v9 offset:18432\n"
    "v_mfma_f32_16x16x32_bf16 v[124:127], v[40:43], a[36:39], v[124:127]\n"
    "s_nop 0\n"
    "buffer_load_dwordx4 v19, s[12:15], s84 offen lds\n"
    "v_mfma_f32_16x16x32_bf16 v[136:139], v[44:47], a[32:35], v[136:139]\n"
    "ds_read_b64_tr_b16 v[36:37], v8 offset:16640\n"
    "v_lshlrev_b32_e64 v194, 16, v233\n"
    "v_mfma_f32_16x16x32_bf16 v[140:143], v[44:47], a[36:39], v[140:143]\n"
    "s_waitcnt lgkmcnt(5)\n"
    "ds_read_b64_tr_b16 v[38:39], v8 offset:18688\n"
    "v_and_b32 v195, 0xffff0000, v233\n"
    "v_mfma_f32_16x16x32_bf16 v[128:131], v[48:51], a[32:35], v[128:131]\n"
    "s_nop 0\n"
    "ds_read_b64_tr_b16 v[40:41], v9 offset:16640\n"
    "v_lshlrev_b32_e64 v196, 16, v234\n"
    "v_mfma_f32_16x16x32_bf16 v[132:135], v[48:51], a[36:39], v[132:135]\n"
    "ds_read_b64_tr_b16 v[42:43], v9 offset:18688\n"
    "v_and_b32 v197, 0xffff0000, v234\n"
    "v_mfma_f32_16x16x32_bf16 v[144:147], v[52:55], a[32:35], v[144:147]\n"
    "ds_read_b64_tr_b16 v[44:45], v8 offset:20480\n"
    "v_lshlrev_b32_e64 v198, 16, v235\n"
    "v_mfma_f32_16x16x32_bf16 v[148:151], v[52:55], a[36:39], v[148:151]\n"
    "s_waitcnt vmcnt(10) lgkmcnt(5)\n"
    "ds_read_b64_tr_b16 v[46:47], v8 offset:22528\n"
    "v_and_b32 v199, 0xffff0000, v235\n"
    "v_mfma_f32_16x16x32_bf16 v[120:123], v[56:59], a[40:43], v[120:123]\n"
    "s_mov_b32 s83, 13500416\n"
    "ds_read_b64_tr_b16 v[48:49], v9 offset:20480\n"
    "ds_read_b64_tr_b16 v[50:51], v9 offset:22528\n"
    "v_mfma_f32_16x16x32_bf16 v[124:127], v[56:59], a[44:47], v[124:127]\n"
    "s_nop 0\n"
    "buffer_store_dwordx2 v[60:61], v23, s[16:19], s83 offen sc1\n"
    "v_mfma_f32_16x16x32_bf16 v[136:139], v[32:35], a[40:43], v[136:139]\n"
    "ds_read_b64_tr_b16 v[52:53], v8 offset:20736\n"
    "v_lshlrev_b32_e64 v200, 16, v236\n"
    "v_mfma_f32_16x16x32_bf16 v[140:143], v[32:35], a[44:47], v[140:143]\n"
    "s_waitcnt lgkmcnt(5)\n"
    "ds_read_b64_tr_b16 v[54:55], v8 offset:22784\n"
    "v_and_b32 v201, 0xffff0000, v236\n"
    "v_mfma_f32_16x16x32_bf16 v[128:131], v[36:39], a[40:43], v[128:131]\n"
    "s_nop 0\n"
    "ds_read_b64_tr_b16 v[56:57], v9 offset:20736\n"
    "v_lshlrev_b32_e64 v202, 16, v237\n"
    "v_mfma_f32_16x16x32_bf16 v[132:135], v[36:39], a[44:47], v[132:135]\n"
    "ds_read_b64_tr_b16 v[58:59], v9 offset:22784\n"
    "v_and_b32 v203, 0xffff0000, v237\n"
    "v_mfma_f32_16x16x32_bf16 v[144:147], v[40:43], a[40:43], v[144:147]\n"
    "ds_read_b64_tr_b16 v[32:33], v8 offset:24576\n"
    "v_and_b32_e64 v170, 1, v30\n"
    "v_mfma_f32_16x16x32_bf16 v[148:151], v[40:43], a[44:47], v[148:151]\n"
    "s_waitcnt lgkmcnt(5)\n"
    "ds_read_b64_tr_b16 v[34:35], v8 offset:26624\n"
    "v_lshlrev_b32 v204, 16, v238\n"
    "v_mfma_f32_16x16x32_bf16 v[120:123], v[44:47], a[48:51], v[120:123]\n"
    "ds_read_b64_tr_b16 v[36:37], v9 offset:24576\n"
    "v_sub_u32 v170, 0, v170\n"
    "v_mfma_f32_16x16x32_bf16 v[124:127], v[44:47], a[52:55], v[124:127]\n"
    "s_nop 0\n"
    "ds_read_b64_tr_b16 v[38:39], v9 offset:26624\n"
    "v_and_b32 v205, 0xffff0000, v238\n"
    "v_mfma_f32_16x16x32_bf16 v[136:139], v[48:51], a[48:51], v[136:139]\n"
    "ds_read_b64_tr_b16 v[40:41], v8 offset:24832\n"
    "v_xor_b32 v170, 0xffff, v170\n"
    "v_mfma_f32_16x16x32_bf16 v[140:143], v[48:51], a[52:55], v[140:143]\n"
    "s_waitcnt vmcnt(4) lgkmcnt(5)\n"
    "ds_read_b64_tr_b16 v[42:43], v8 offset:26880\n"
    "v_lshlrev_b32 v206, 16, v239\n"
    "v_cmp_ne_u64 vcc, s[98:99], v[62:63]\n"
    "v_mfma_f32_16x16x32_bf16 v[128:131], v[52:55], a[48:51], v[128:131]\n"
    "s_nop 0\n"
    "ds_read_b64_tr_b16 v[44:45], v9 offset:24832\n"
    "v_and_b32 v207, 0xffff0000, v239\n"
    "v_mfma_f32_16x16x32_bf16 v[132:135], v[52:55], a[52:55], v[132:135]\n"
    "ds_read_b64_tr_b16 v[46:47], v9 offset:26880\n"
    "v_and_b32 v170, 0x3f803f80, v170\n"
    "s_cbranch_vccnz LPLPA_%=s\n"
    "LPLPA_%=:\n"
    "v_mfma_f32_16x16x32_bf16 v[144:147], v[56:59], a[48:51], v[144:147]\n"
    "s_nop 0\n"
    "ds_read_b64_tr_b16 v[48:49], v8 offset:28672\n"
    "v_mfma_f32_16x16x32_bf16 v[148:151], v[56:59], a[52:55], v[148:151]\n"
    "s_waitcnt lgkmcnt(5)\n"
    "ds_read_b64_tr_b16 v[50:51], v8 offset:30720\n"
    "v_bfe_u32 v169, v30, 2, 2\n"
    "v_mfma_f32_16x16x32_bf16 v[120:123], v[32:35], a[56:59], v[120:123]\n"
    "s_add_u32 s85, s41, 16384\n"
    "ds_read_b64_tr_b16 v[52:53], v9 offset:28672\n"
    "ds_read_b64_tr_b16 v[54:55], v9 offset:30720\n"
    "v_mfma_f32_16x16x32_bf16 v[124:127], v[32:35], a[60:63], v[124:127]\n"
    "s_nop 0\n"
    "buffer_load_dwordx4 a[184:187], v20, s[16:19], s85 offen offset:0 sc1\n"
    "v_mfma_f32_16x16x32_bf16 v[136:139], v[36:39], a[56:59], v[136:139]\n"
    "ds_read_b64_tr_b16 v[56:57], v8 offset:28928\n"
    "ds_read_b64_tr_b16 v[58:59], v8 offset:30976\n"
    "v_mfma_f32_16x16x32_bf16 v[140:143], v[36:39], a[60:63], v[140:143]\n"
    "s_waitcnt lgkmcnt(6)\n"
    "buffer_load_dwordx4 a[188:191], v20, s[16:19], s85 offen offset:1024 sc1\n"
    "v_mfma_f32_16x16x32_bf16 v[128:131], v[40:43], a[56:59], v[128:131]\n"
    "s_barrier\n"
    "ds_read_b64_tr_b16 v[32:33], v9 offset:28928\n"
    "v_mfma_f32_16x16x32_bf16 v[132:135], v[40:43], a[60:63], v[132:135]\n"
    "ds_read_b64_tr_b16 v[34:35], v9 offset:30976\n"
    "v_lshrrev_b32_e64 v168, 4, v30\n"
    "v_mfma_f32_16x16x32_bf16 v[144:147], v[44:47], a[56:59], v[144:147]\n"
    "ds_read_b64_tr_b16 v[36:37], v8 offset:32768\n"
    "v_mfma_f32_16x16x32_bf16 v[148:151], v[44:47], a[60:63], v[148:151]\n"
    "s_waitcnt lgkmcnt(5)\n"
    "ds_read_b64_tr_b16 v[38:39], v8 offset:34816\n"
    "v_mfma_f32_16x16x32_bf16 v[120:123], v[48:51], a[64:67], v[120:123]\n"
    "s_add_u32 s84, s58, 0\n"
    "ds_read_b64_tr_b16 v[40:41], v9 offset:32768\n"
    "v_xor_b32_e64 v169, v168, v169\n"
    "v_mfma_f32_16x16x32_bf16 v[124:127], v[48:51], a[68:71], v[124:127]\n"
    "s_add_u32 m0, s36, 49152\n"
    "ds_read_b64_tr_b16 v[42:43], v9 offset:34816\n"
    "ds_read_b64_tr_b16 v[44:45], v8 offset:33024\n"
    "v_mfma_f32_16x16x32_bf16 v[136:139], v[52:55], a[64:67], v[136:139]\n"
    "buffer_load_dwordx4 v19, s[12:15], s84 offen lds\n"
    "v_mfma_f32_16x16x32_bf16 v[140:143], v[52:55], a[68:71], v[140:143]\n"
    "s_waitcnt lgkmcnt(5)\n"
    "ds_read_b64_tr_b16 v[46:47], v8 offset:35072\n"
    "v_mfma_f32_16x16x32_bf16 v[128:131], v[56:59], a[64:67], v[128:131]\n"
    "s_nop 0\n"
    "ds_read_b64_tr_b16 v[48:49], v9 offset:33024\n"
    "v_add_u32_e64 v169, 3, v169\n"
    "v_mfma_f32_16x16x32_bf16 v[132:135], v[56:59], a[68:71], v[132:135]\n"
    "ds_read_b64_tr_b16 v[50:51], v9 offset:35072\n"
    "v_xor_b32 v25, 0x1800, v23\n"
    "v_mfma_f32_16x16x32_bf16 v[144:147], v[32:35], a[64:67], v[144:147]\n"
    "s_mov_b32 s87, 13238272\n"
    "ds_read_b64_tr_b16 v[52:53], v8 offset:36864\n"
    "ds_read_b64_tr_b16 v[54:55], v8 offset:38912\n"
    "v_mfma_f32_16x16x32_bf16 v[148:151], v[32:35], a[68:71], v[148:151]\n"
    "s_waitcnt lgkmcnt(6)\n"
    "buffer_load_dwordx2 v[62:63], v25, s[16:19], s87 offen sc1\n"
    "v_mfma_f32_16x16x32_bf16 v[120:123], v[36:39], a[72:75], v[120:123]\n"
    "s_nop 0\n"
    "ds_read_b64_tr_b16 v[56:57], v9 offset:36864\n"
    "v_lshrrev_b32_e64 v169, 2, v169\n"
    "v_mfma_f32_16x16x32_bf16 v[124:127], v[36:39], a[76:79], v[124:127]\n"
    "s_add_u32 s84, s61, 0\n"
    "ds_read_b64_tr_b16 v[58:59], v9 offset:38912\n"
    "v_mfma_f32_16x16x32_bf16 v[136:139], v[40:43], a[72:75], v[136:139]\n"
    "s_add_u32 m0, s36, 53248\n"
    "ds_read_b64_tr_b16 v[32:33], v8 offset:37120\n"
    "ds_read_b64_tr_b16 v[34:35], v8 offset:39168\n"
    "v_mfma_f32_16x16x32_bf16 v[140:143], v[40:43], a[76:79], v[140:143]\n"
    "s_waitcnt lgkmcnt(6)\n"
    "buffer_load_dwordx4 v19, s[12:15], s84 offen lds\n"
    "v_mfma_f32_16x16x32_bf16 v[128:131], v[44:47], a[72:75], v[128:131]\n"
    "ds_read_b64_tr_b16 v[36:37], v9 offset:37120\n"
    "v_mfma_f32_16x16x32_bf16 v[132:135], v[44:47], a[76:79], v[132:135]\n"
    "s_add_u32 s84, s55, 65536\n"
    "ds_read_b64_tr_b16 v[38:39], v9 offset:39168\n"
    "v_add_u32_e64 v169, -1, v169\n"
    "v_mfma_f32_16x16x32_bf16 v[144:147], v[48:51], a[72:75], v[144:147]\n"
    "s_add_u32 m0, s36, 57344\n"
    "ds_read_b64_tr_b16 v[40:41], v8 offset:40960\n"
    "v_mfma_f32_16x16x32_bf16 v[148:151], v[48:51], a[76:79], v[148:151]\n"
    "s_waitcnt lgkmcnt(5)\n"
    "ds_read_b64_tr_b16 v[42:43], v8 offset:43008\n"
    "ds_read_b64_tr_b16 v[44:45], v9 offset:40960\n"
    "v_mfma_f32_16x16x32_bf16 v[120:123], v[52:55], a[80:83], v[120:123]\n"
    "s_nop 0\n"
    "buffer_load_dwordx4 v19, s[12:15], s84 offen lds\n"
    "v_mfma_f32_16x16x32_bf16 v[124:127], v[52:55], a[84:87], v[124:127]\n"
    "s_add_u32 s84, s58, 65536\n"
    "ds_read_b64_tr_b16 v[46:47], v9 offset:43008\n"
    "v_and_b32_e64 v170, v169, v170\n"
    "v_mfma_f32_16x16x32_bf16 v[136:139], v[56:59], a[80:83], v[136:139]\n"
    "s_add_u32 m0, s36, 61440\n"
    "ds_read_b64_tr_b16 v[48:49], v8 offset:41216\n"
    "v_mfma_f32_16x16x32_bf16 v[140:143], v[56:59], a[84:87], v[140:143]\n"
    "s_waitcnt lgkmcnt(5)\n"
    "ds_read_b64_tr_b16 v[50:51], v8 offset:43264\n"
    "ds_read_b64_tr_b16 v[52:53], v9 offset:41216\n"
    "v_mfma_f32_16x16x32_bf16 v[128:131], v[32:35], a[80:83], v[128:131]\n"
    "s_nop 0\n"
    "buffer_load_dwordx4 v19, s[12:15], s84 offen lds\n"
    "v_mfma_f32_16x16x32_bf16 v[132:135], v[32:35], a[84:87], v[132:135]\n"
    "ds_read_b64_tr_b16 v[54:55], v9 offset:43264\n"
    "v_mfma_f32_16x16x32_bf16 v[144:147], v[36:39], a[80:83], v[144:147]\n"
    "ds_read_b64_tr_b16 v[56:57], v8 offset:45056\n"
    "v_bfe_u32 v171, v30, 1, 1\n"
    "v_mfma_f32_16x16x32_bf16 v[148:151], v[36:39], a[84:87], v[148:151]\n"
    "s_waitcnt lgkmcnt(5)\n"
    "ds_read_b64_tr_b16 v[58:59], v8 offset:47104\n"
    "v_mfma_f32_16x16x32_bf16 v[120:123], v[40:43], a[88:91], v[120:123]\n"
    "s_add_u32 s85, s41, 24576\n"
    "LPAD11_%=:\n"
    "s_cbranch_execz LPAD15_%=\n"
    "ds_read_b64_tr_b16 v[32:33], v9 offset:45056\n"
    "ds_read_b64_tr_b16 v[34:35], v9 offset:47104\n"
    "v_mfma_f32_16x16x32_bf16 v[124:127], v[40:43], a[92:95], v[124:127]\n"
    "buffer_load_dwordx4 a[192:195], v20, s[16:19], s85 offen offset:0 sc1\n"
    "v_mfma_f32_16x16x32_bf16 v[136:139], v[44:47], a[88:91], v[136:139]\n"
    "ds_read_b64_tr_b16 v[36:37], v8 offset:45312\n"
    "v_sub_u32 v171, 0, v171\n"
    "v_mfma_f32_16x16x32_bf16 v[140:143], v[44:47], a[92:95], v[140:143]\n"
    "s_waitcnt lgkmcnt(5)\n"
    "ds_read_b64_tr_b16 v[38:39], v8 offset:47360\n"
    "v_mfma_f32_16x16x32_bf16 v[128:131], v[48:51], a[88:91], v[128:131]\n"
    "ds_read_b64_tr_b16 v[40:41], v9 offset:45312\n"
    "v_and_b32_e64 v171, v170, v171\n"
    "v_mfma_f32_16x16x32_bf16 v[132:135], v[48:51], a[92:95], v[132:135]\n"
    "ds_read_b64_tr_b16 v[42:43], v9 offset:47360\n"
    "v_mfma_f32_16x16x32_bf16 v[144:147], v[52:55], a[88:91], v[144:147]\n"
    "ds_read_b64_tr_b16 v[44:45], v8 offset:49152\n"
    "v_mfma_f32_16x16x32_bf16 v[148:151], v[52:55], a[92:95], v[148:151]\n"
    "s_waitcnt lgkmcnt(5)\n"
    "ds_read_b64_tr_b16 v[46:47], v8 offset:51200\n"
    "ds_read_b64_tr_b16 v[48:49], v9 offset:49152\n"
    "v_mfma_f32_16x16x32_bf16 v[120:123], v[56:59], a[96:99], v[120:123]\n"
    "s_nop 0\n"
    "buffer_load_dwordx4 a[196:199], v20, s[16:19], s85 offen offset:1024 sc1\n"
    "v_mfma_f32_16x16x32_bf16 v[124:127], v[56:59], a[100:103], v[124:127]\n"
    "ds_read_b64_tr_b16 v[50:51], v9 offset:51200\n"
    "v_xor_b32_e64 v170, v170, v171\n"
    "v_mfma_f32_16x16x32_bf16 v[136:139], v[32:35], a[96:99], v[136:139]\n"
    "ds_read_b64_tr_b16 v[52:53], v8 offset:49408\n"
    "v_mfma_f32_16x16x32_bf16 v[140:143], v[32:35], a[100:103], v[140:143]\n"
    "s_waitcnt lgkmcnt(5)\n"
    "ds_read_b64_tr_b16 v[54:55], v8 offset:51456\n"
    "v_mov_b32 v168, 0\n"
    "v_mfma_f32_16x16x32_bf16 v[128:131], v[36:39], a[96:99], v[128:131]\n"
    "ds_read_b64_tr_b16 v[56:57], v9 offset:49408\n"
    "v_mfma_f32_16x16x32_bf16 v[132:135], v[36:39], a[100:103], v[132:135]\n"
    "s_add_u32 s84, s61, 65536\n"
    "ds_read_b64_tr_b16 v[58:59], v9 offset:51456\n"
    "v_mov_b32_e64 v169, 0\n"
    "v_mfma_f32_16x16x32_bf16 v[144:147], v[40:43], a[96:99], v[144:147]\n"
    "s_add_u32 m0, s36, 65536\n"
    "ds_read_b64_tr_b16 v[32:33], v8 offset:53248\n"
    "ds_read_b64_tr_b16 v[34:35], v8 offset:55296\n"
    "v_mfma_f32_16x16x32_bf16 v[148:151], v[40:43], a[100:103], v[148:151]\n"
    "s_waitcnt vmcnt(5) lgkmcnt(6)\n"
    "buffer_load_dwordx4 v19, s[12:15], s84 offen lds\n"
    "v_cmp_ne_u64 vcc, s[98:99], v[62:63]\n"
    "v_mfma_f32_16x16x32_bf16 v[120:123], v[44:47], a[104:107], v[120:123]\n"
    "ds_read_b64_tr_b16 v[36:37], v9 offset:53248\n"
    "v_mfma_f32_16x16x32_bf16 v[124:127], v[44:47], a[108:111], v[124:127]\n"
    "s_add_u32 s84, s55, 131072\n"
    "ds_read_b64_tr_b16 v[38:39], v9 offset:55296\n"
    "s_cbranch_vccnz LPLPB_%=s\n"
    "LPLPB_%=:\n"
    "v_mfma_f32_16x16x32_bf16 v[136:139], v[48:51], a[104:107], v[136:139]\n"
    "s_add_u32 m0, s36, 69632\n"
    "ds_read_b64_tr_b16 v[40:41], v8 offset:53504\n"
    "ds_read_b64_tr_b16 v[42:43], v8 offset:55552\n"
    "v_mfma_f32_16x16x32_bf16 v[140:143], v[48:51], a[108:111], v[140:143]\n"
    "s_waitcnt lgkmcnt(6)\n"
    "buffer_load_dwordx4 v19, s[12:15], s84 offen lds\n"
    "v_mfma_f32_16x16x32_bf16 v[128:131], v[52:55], a[104:107], v[128:131]\n"
    "ds_read_b64_tr_b16 v[44:45], v9 offset:53504\n"
    "v_mov_b32_e64 v172, 0\n"
    "v_mfma_f32_16x16x32_bf16 v[132:135], v[52:55], a[108:111], v[132:135]\n"
    "s_add_u32 s84, s58, 131072\n"
    "ds_read_b64_tr_b16 v[46:47], v9 offset:55552\n"
    "v_mfma_f32_16x16x32_bf16 v[144:147], v[56:59], a[104:107], v[144:147]\n"
    "s_add_u32 m0, s36, 73728\n"
    "ds_read_b64_tr_b16 v[48:49], v8 offset:57344\n"
    "v_mov_b32_e64 v173, 0\n"
    "v_mfma_f32_16x16x32_bf16 v[148:151], v[56:59], a[108:111], v[148:151]\n"
    "s_waitcnt lgkmcnt(5)\n"
    "ds_read_b64_tr_b16 v[50:51], v8 offset:59392\n"
    "ds_read_b64_tr_b16 v[52:53], v9 offset:57344\n"
    "v_mfma_f32_16x16x32_bf16 v[120:123], v[32:35], a[112:115], v[120:123]\n"
    "s_nop 0\n"
    "buffer_load_dwordx4 v19, s[12:15], s84 offen lds\n"
    "v_mfma_f32_16x16x32_bf16 v[124:127], v[32:35], a[116:119], v[124:127]\n"
    "s_add_u32 s84, s61, 131072\n"
    "ds_read_b64_tr_b16 v[54:55], v9 offset:59392\n"
    "v_mfma_f32_16x16x32_bf16 v[136:139], v[36:39], a[112:115], v[136:139]\n"
    "s_add_u32 m0, s36, 77824\n"
    "ds_read_b64_tr_b16 v[56:57], v8 offset:57600\n"
    "v_mfma_f32_16x16x32_bf16 v[140:143], v[36:39], a[116:119], v[140:143]\n"
    "s_waitcnt lgkmcnt(5)\n"
    "ds_read_b64_tr_b16 v[58:59], v8 offset:59648\n"
    "ds_read_b64_tr_b16 v[32:33], v9 offset:57600\n"
    "v_mfma_f32_16x16x32_bf16 v[128:131], v[40:43], a[112:115], v[128:131]\n"
    "s_nop 0\n"
    "buffer_load_dwordx4 v19, s[12:15], s84 offen lds\n"
    "v_mfma_f32_16x16x32_bf16 v[132:135], v[40:43], a[116:119], v[132:135]\n"
    "ds_read_b64_tr_b16 v[34:35], v9 offset:59648\n"
    "v_mov_b32 v11, 0xffff0000\n"
    "v_mfma_f32_16x16x32_bf16 v[144:147], v[44:47], a[112:115], v[144:147]\n"
    "ds_read_b64_tr_b16 v[36:37], v8 offset:61440\n"
    "v_mov_b32 v12, 0x10000\n"
    "v_mfma_f32_16x16x32_bf16 v[148:151], v[44:47], a[116:119], v[148:151]\n"
    "s_waitcnt lgkmcnt(5)\n"
    "ds_read_b64_tr_b16 v[38:39], v8 offset:63488\n"
    "v_mfma_f32_16x16x32_bf16 v[120:123], v[48:51], a[120:123], v[120:123]\n"
    "s_add_u32 s85, s41, 32768\n"
    "ds_read_b64_tr_b16 v[40:41], v9 offset:61440\n"
    "ds_read_b64_tr_b16 v[42:43], v9 offset:63488\n"
    "v_mfma_f32_16x16x32_bf16 v[124:127], v[48:51], a[124:127], v[124:127]\n"
    "s_nop 0\n"
    "buffer_load_dwordx4 a[200:203], v20, s[16:19], s85 offen offset:0 sc1\n"
    "v_mfma_f32_16x16x32_bf16 v[136:139], v[52:55], a[120:123], v[136:139]\n"
    "ds_read_b64_tr_b16 v[44:45], v8 offset:61696\n"
    "v_mul_u32_u24_dpp v152, v28, v12 row_newbcast:0 row_mask:0xf bank_mask:0xf\n"
    "v_mfma_f32_16x16x32_bf16 v[140:143], v[52:55], a[124:127], v[140:143]\n"
    "s_waitcnt lgkmcnt(5)\n"
    "ds_read_b64_tr_b16 v[46:47], v8 offset:63744\n"
    "v_mfma_f32_16x16x32_bf16 v[128:131], v[56:59], a[120:123], v[128:131]\n"
    "s_barrier\n"
    "ds_read_b64_tr_b16 v[48:49], v9 offset:61696\n"
    "v_and_b32_dpp v153, v28, v11 row_newbcast:0 row_mask:0xf bank_mask:0xf\n"
    "v_mfma_f32_16x16x32_bf16 v[132:135], v[56:59], a[124:127], v[132:135]\n"
    "ds_read_b64_tr_b16 v[50:51], v9 offset:63744\n"
    "v_mul_u32_u24_dpp v154, v29, v12 row_newbcast:0 row_mask:0xf bank_mask:0xf\n"
    "v_mfma_f32_16x16x32_bf16 v[144:147], v[32:35], a[120:123], v[144:147]\n"
    "s_mov_b32 s87, 13500416\n"
    "ds_read_b64_tr_b16 v[52:53], v6 offset:0\n"
    "v_mfma_f32_16x16x32_bf16 v[148:151], v[32:35], a[124:127], v[148:151]\n"
    "s_waitcnt lgkmcnt(5)\n"
    "ds_read_b64_tr_b16 v[54:55], v6 offset:2048\n"
    "ds_read_b64_tr_b16 v[56:57], v7 offset:0\n"
    "v_mfma_f32_16x16x32_bf16 v[120:123], v[36:39], a[128:131], v[120:123]\n"
    "s_nop 0\n"
    "buffer_load_dwordx2 v[62:63], v27, s[16:19], s87 offen sc1\n"
    "v_mfma_f32_16x16x32_bf16 v[124:127], v[36:39], a[132:135], v[124:127]\n"
    "s_add_u32 s84, s55, 196608\n"
    "ds_read_b64_tr_b16 v[58:59], v7 offset:2048\n"
    "v_and_b32_dpp v155, v29, v11 row_newbcast:0 row_mask:0xf bank_mask:0xf\n"
    "v_mfma_f32_16x16x32_bf16 v[136:139], v[40:43], a[128:131], v[136:139]\n"
    "s_add_u32 m0, s36, 81920\n"
    "ds_read_b64_tr_b16 v[32:33], v6 offset:256\n"
    "v_mfma_f32_16x16x32_bf16 v[140:143], v[40:43], a[132:135], v[140:143]\n"
    "s_waitcnt lgkmcnt(5)\n"
    "ds_read_b64_tr_b16 v[34:35], v6 offset:2304\n"
    "ds_read_b64_tr_b16 v[36:37], v7 offset:256\n"
    "v_mfma_f32_16x16x32_bf16 v[128:131], v[44:47], a[128:131], v[128:131]\n"
    "s_nop 0\n"
    "buffer_load_dwordx4 v19, s[12:15], s84 offen lds\n"
    "v_mfma_f32_16x16x32_bf16 v[132:135], v[44:47], a[132:135], v[132:135]\n"
    "ds_read_b64_tr_b16 v[38:39], v7 offset:2304\n"
    "v_mul_u32_u24_dpp v240, v28, v12 row_newbcast:1 row_mask:0xf bank_mask:0xf\n"
    "v_mfma_f32_16x16x32_bf16 v[144:147], v[48:51], a[128:131], v[144:147]\n"
    "s_add_u32 s84, s58, 196608\n"
    "ds_read_b64_tr_b16 v[40:41], v6 offset:4096\n"
    "v_and_b32_dpp v241, v28, v11 row_newbcast:1 row_mask:0xf bank_mask:0xf\n"
    "v_mfma_f32_16x16x32_bf16 v[148:151], v[48:51], a[132:135], v[148:151]\n"
    "s_waitcnt lgkmcnt(5)\n"
    "ds_read_b64_tr_b16 v[42:43], v6 offset:6144\n"
    "v_mfma_f32_16x16x32_bf16 v[120:123], v[52:55], a[136:139], v[120:123]\n"
    "s_add_u32 m0, s36, 86016\n"
    "ds_read_b64_tr_b16 v[44:45], v7 offset:4096\n"
    "ds_read_b64_tr_b16 v[46:47], v7 offset:6144\n"
    "v_mfma_f32_16x16x32_bf16 v[124:127], v[52:55], a[140:143], v[124:127]\n"
    "s_nop 0\n"
    "buffer_load_dwordx4 v19, s[12:15], s84 offen lds\n"
    "v_mfma_f32_16x16x32_bf16 v[136:139], v[56:59], a[136:139], v[136:139]\n"
    "s_add_u32 s84, s61, 196608\n"
    "ds_read_b64_tr_b16 v[48:49], v6 offset:4352\n"
    "v_mul_u32_u24_dpp v242, v29, v12 row_newbcast:1 row_mask:0xf bank_mask:0xf\n"
    "v_mfma_f32_16x16x32_bf16 v[140:143], v[56:59], a[140:143], v[140:143]\n"
    "s_waitcnt lgkmcnt(5)\n"
    "ds_read_b64_tr_b16 v[50:51], v6 offset:6400\n"
    "v_mfma_f32_16x16x32_bf16 v[128:131], v[32:35], a[136:139], v[128:131]\n"
    "s_add_u32 m0, s36, 90112\n"
    "ds_read_b64_tr_b16 v[52:53], v7 offset:4352\n"
    "ds_read_b64_tr_b16 v[54:55], v7 offset:6400\n"
    "v_mfma_f32_16x16x32_bf16 v[132:135], v[32:35], a[140:143], v[132:135]\n"
    "s_nop 0\n"
    "buffer_load_dwordx4 v19, s[12:15], s84 offen lds\n"
    "v_mfma_f32_16x16x32_bf16 v[144:147], v[36:39], a[136:139], v[144:147]\n"
    "ds_read_b64_tr_b16 v[56:57], v6 offset:8192\n"
    "v_and_b32_dpp v243, v29, v11 row_newbcast:1 row_mask:0xf bank_mask:0xf\n"
    "v_mfma_f32_16x16x32_bf16 v[148:151], v[36:39], a[140:143], v[148:151]\n"
    "s_waitcnt lgkmcnt(5)\n"
    "ds_read_b64_tr_b16 v[58:59], v6 offset:10240\n"
    "v_mul_u32_u24_dpp v160, v28, v12 row_newbcast:2 row_mask:0xf bank_mask:0xf\n"
    "v_mfma_f32_16x16x32_bf16 v[120:123], v[40:43], a[144:147], v[120:123]\n"
    "s_add_u32 s84, s56, 0\n"
    "ds_read_b64_tr_b16 v[32:33], v7 offset:8192\n"
    "v_mfma_f32_16x16x32_bf16 v[124:127], v[40:43], a[148:151], v[124:127]\n"
    "s_add_u32 m0, s36, 94208\n"
    "ds_read_b64_tr_b16 v[34:35], v7 offset:10240\n"
    "ds_read_b64_tr_b16 v[36:37], v6 offset:8448\n"
    "v_mfma_f32_16x16x32_bf16 v[136:139], v[44:47], a[144:147], v[136:139]\n"
    "buffer_load_dwordx4 v19, s[12:15], s84 offen lds\n"
    "v_mfma_f32_16x16x32_bf16 v[140:143], v[44:47], a[148:151], v[140:143]\n"
    "s_waitcnt lgkmcnt(5)\n"
    "ds_read_b64_tr_b16 v[38:39], v6 offset:10496\n"
    "v_and_b32_dpp v161, v28, v11 row_newbcast:2 row_mask:0xf bank_mask:0xf\n"
    "v_mfma_f32_16x16x32_bf16 v[128:131], v[48:51], a[144:147], v[128:131]\n"
    "s_nop 0\n"
    "ds_read_b64_tr_b16 v[40:41], v7 offset:8448\n"
    "v_mfma_f32_16x16x32_bf16 v[132:135], v[48:51], a[148:151], v[132:135]\n"
    "ds_read_b64_tr_b16 v[42:43], v7 offset:10496\n"
    "ds_read_b64_tr_b16 v[44:45], v6 offset:12288\n"
    "v_mfma_f32_16x16x32_bf16 v[144:147], v[52:55], a[144:147], v[144:147]\n"
    "buffer_load_dwordx4 a[204:207], v20, s[16:19], s85 offen offset:1024 sc1\n"
    "v_mfma_f32_16x16x32_bf16 v[148:151], v[52:55], a[148:151], v[148:151]\n"
    "s_waitcnt lgkmcnt(5)\n"
    "ds_read_b64_tr_b16 v[46:47], v6 offset:14336\n"
    "v_mul_u32_u24_dpp v162, v29, v12 row_newbcast:2 row_mask:0xf bank_mask:0xf\n"
    "v_mfma_f32_16x16x32_bf16 v[120:123], v[56:59], a[152:155], v[120:123]\n"
    "s_nop 0\n"
    "ds_read_b64_tr_b16 v[48:49], v7 offset:12288\n"
    "v_and_b32_dpp v163, v29, v11 row_newbcast:2 row_mask:0xf bank_mask:0xf\n"
    "v_mfma_f32_16x16x32_bf16 v[124:127], v[56:59], a[156:159], v[124:127]\n"
    "ds_read_b64_tr_b16 v[50:51], v7 offset:14336\n"
    "v_mfma_f32_16x16x32_bf16 v[136:139], v[32:35], a[152:155], v[136:139]\n"
    "ds_read_b64_tr_b16 v[52:53], v6 offset:12544\n"
    "v_mul_u32_u24_dpp v248, v28, v12 row_newbcast:3 row_mask:0xf bank_mask:0xf\n"
    "v_mfma_f32_16x16x32_bf16 v[140:143], v[32:35], a[156:159], v[140:143]\n"
    "s_waitcnt lgkmcnt(5)\n"
    "ds_read_b64_tr_b16 v[54:55], v6 offset:14592\n"
    "v_mfma_f32_16x16x32_bf16 v[128:131], v[36:39], a[152:155], v[128:131]\n"
    "s_add_u32 s85, s41, 40960\n"
    "ds_read_b64_tr_b16 v[56:57], v7 offset:12544\n"
    "ds_read_b64_tr_b16 v[58:59], v7 offset:14592\n"
    "v_mfma_f32_16x16x32_bf16 v[132:135], v[36:39], a[156:159], v[132:135]\n"
    "s_nop 0\n"
    "buffer_load_dwordx4 a[208:211], v20, s[16:19], s85 offen offset:0 sc1\n"
    "v_mfma_f32_16x16x32_bf16 v[144:147], v[40:43], a[152:155], v[144:147]\n"
    "ds_read_b64_tr_b16 v[32:33], v6 offset:16384\n"
    "v_and_b32_dpp v249, v28, v11 row_newbcast:3 row_mask:0xf bank_mask:0xf\n"
    "v_mfma_f32_16x16x32_bf16 v[148:151], v[40:43], a[156:159], v[148:151]\n"
    "s_waitcnt lgkmcnt(5)\n"
    "ds_read_b64_tr_b16 v[34:35], v6 offset:18432\n"
    "v_mul_u32_u24_dpp v250, v29, v12 row_newbcast:3 row_mask:0xf bank_mask:0xf\n"
    "v_mfma_f32_16x16x32_bf16 v[120:123], v[44:47], a[160:163], v[120:123]\n"
    "s_nop 0\n"
    "ds_read_b64_tr_b16 v[36:37], v7 offset:16384\n"
    "v_mfma_f32_16x16x32_bf16 v[124:127], v[44:47], a[164:167], v[124:127]\n"
    "ds_read_b64_tr_b16 v[38:39], v7 offset:18432\n"
    "v_and_b32_dpp v251, v29, v11 row_newbcast:3 row_mask:0xf bank_mask:0xf\n"
    "v_mfma_f32_16x16x32_bf16 v[136:139], v[48:51], a[160:163], v[136:139]\n"
    "ds_read_b64_tr_b16 v[40:41], v6 offset:16640\n"
    "v_mfma_f32_16x16x32_bf16 v[140:143], v[48:51], a[164:167], v[140:143]\n"
    "s_waitcnt lgkmcnt(5)\n"
    "ds_read_b64_tr_b16 v[42:43], v6 offset:18688\n"
    "ds_read_b64_tr_b16 v[44:45], v7 offset:16640\n"
    "v_mfma_f32_16x16x32_bf16 v[128:131], v[52:55], a[160:163], v[128:131]\n"
    "s_nop 0\n"
    "buffer_load_dwordx4 a[212:215], v20, s[16:19], s85 offen offset:1024 sc1\n"
    "v_mfma_f32_16x16x32_bf16 v[132:135], v[52:55], a[164:167], v[132:135]\n"
    "ds_read_b64_tr_b16 v[46:47], v7 offset:18688\n"
    "v_mfma_f32_16x16x32_bf16 v[144:147], v[56:59], a[160:163], v[144:147]\n"
    "ds_read_b64_tr_b16 v[48:49], v6 offset:20480\n"
    "v_mfma_f32_16x16x32_bf16 v[148:151], v[56:59], a[164:167], v[148:151]\n"
    "s_waitcnt lgkmcnt(5)\n"
    "ds_read_b64_tr_b16 v[50:51], v6 offset:22528\n"
    "v_mfma_f32_16x16x32_bf16 v[156:159], v[32:35], v[68:71], v[152:155]\n"
    "s_nop 0\n"
    "ds_read_b64_tr_b16 v[52:53], v7 offset:20480\n"
    "v_mfma_f32_16x16x32_bf16 v[152:155], v[32:35], v[64:67], v[152:155]\n"
    "s_add_u32 s84, s59, 0\n"
    "ds_read_b64_tr_b16 v[54:55], v7 offset:22528\n"
    "v_mfma_f32_16x16x32_bf16 v[244:247], v[36:39], v[68:71], v[240:243]\n"
    "s_add_u32 m0, s36, 98304\n"
    "ds_read_b64_tr_b16 v[56:57], v6 offset:20736\n"
    "ds_read_b64_tr_b16 v[58:59], v6 offset:22784\n"
    "v_mfma_f32_16x16x32_bf16 v[240:243], v[36:39], v[64:67], v[240:243]\n"
    "s_waitcnt lgkmcnt(6)\n"
    "buffer_load_dwordx4 v19, s[12:15], s84 offen lds\n"
    "v_mfma_f32_16x16x32_bf16 v[164:167], v[40:43], v[68:71], v[160:163]\n"
    "ds_read_b64_tr_b16 v[32:33], v7 offset:20736\n"
    "v_cvt_pk_bf16_f32 v120, v120, v121\n"
    "v_mfma_f32_16x16x32_bf16 v[160:163], v[40:43], v[64:67], v[160:163]\n"
    "s_add_u32 s84, s62, 0\n"
    "ds_read_b64_tr_b16 v[34:35], v7 offset:22784\n"
    "v_cvt_pk_bf16_f32 v121, v122, v123\n"
    "v_mfma_f32_16x16x32_bf16 v[252:255], v[44:47], v[68:71], v[248:251]\n"
    "s_add_u32 m0, s36, 102400\n"
    "ds_read_b64_tr_b16 v[36:37], v6 offset:24576\n"
    "v_cvt_pk_bf16_f32 v122, v136, v137\n"
    "v_mfma_f32_16x16x32_bf16 v[248:251], v[44:47], v[64:67], v[248:251]\n"
    "s_waitcnt vmcnt(8) lgkmcnt(5)\n"
    "ds_read_b64_tr_b16 v[38:39], v6 offset:26624\n"
    "ds_read_b64_tr_b16 v[40:41], v7 offset:24576\n"
    "v_cmp_ne_u64 vcc, s[98:99], v[62:63]\n"
    "v_mfma_f32_16x16x32_bf16 v[152:155], v[48:51], v[72:75], v[152:155]\n"
    "s_nop 0\n"
    "buffer_load_dwordx4 v19, s[12:15], s84 offen lds\n"
    "v_mfma_f32_16x16x32_bf16 v[156:159], v[48:51], v[76:79], v[156:159]\n"
    "ds_read_b64_tr_b16 v[42:43], v7 offset:26624\n"
    "v_cvt_pk_bf16_f32 v123, v138, v139\n"
    "s_cbranch_vccnz LPLS_%=s\n"
    "LPLS_%=:\n"
    "v_mfma_f32_16x16x32_bf16 v[240:243], v[52:55], v[72:75], v[240:243]\n"
    "s_add_u32 s86, s40, 0\n"
    "ds_read_b64_tr_b16 v[44:45], v6 offset:24832\n"
    "v_cvt_pk_bf16_f32 v124, v124, v125\n"
    "v_mfma_f32_16x16x32_bf16 v[244:247], v[52:55], v[76:79], v[244:247]\n"
    "s_waitcnt lgkmcnt(5)\n"
    "ds_read_b64_tr_b16 v[46:47], v6 offset:26880\n"
    "ds_read_b64_tr_b16 v[48:49], v7 offset:24832\n"
    "v_mfma_f32_16x16x32_bf16 v[160:163], v[56:59], v[72:75], v[160:163]\n"
    "s_nop 0\n"
    "buffer_store_dwordx4 v[120:123], v20, s[16:19], s86 offen offset:0 sc1\n"
    "v_mfma_f32_16x16x32_bf16 v[164:167], v[56:59], v[76:79], v[164:167]\n"
    "ds_read_b64_tr_b16 v[50:51], v7 offset:26880\n"
    "v_cvt_pk_bf16_f32 v125, v126, v127\n"
    "v_mfma_f32_16x16x32_bf16 v[248:251], v[32:35], v[72:75], v[248:251]\n"
    "ds_read_b64_tr_b16 v[52:53], v6 offset:28672\n"
    "v_cvt_pk_bf16_f32 v126, v140, v141\n"
    "v_mfma_f32_16x16x32_bf16 v[252:255], v[32:35], v[76:79], v[252:255]\n"
    "s_waitcnt lgkmcnt(5)\n"
    "ds_read_b64_tr_b16 v[54:55], v6 offset:30720\n"
    "v_cvt_pk_bf16_f32 v127, v142, v143\n"
    "v_mfma_f32_16x16x32_bf16 v[152:155], v[36:39], v[80:83], v[152:155]\n"
    "s_nop 0\n"
    "ds_read_b64_tr_b16 v[56:57], v7 offset:28672\n"
    "v_cvt_pk_bf16_f32 v128, v128, v129\n"
    "v_mfma_f32_16x16x32_bf16 v[156:159], v[36:39], v[84:87], v[156:159]\n"
    "ds_read_b64_tr_b16 v[58:59], v7 offset:30720\n"
    "v_cvt_pk_bf16_f32 v129, v130, v131\n"
    "v_mfma_f32_16x16x32_bf16 v[240:243], v[40:43], v[80:83], v[240:243]\n"
    "s_add_u32 s85, s42, 0\n"
    "ds_read_b64_tr_b16 v[32:33], v6 offset:28928\n"
    "ds_read_b64_tr_b16 v[34:35], v6 offset:30976\n"
    "v_mfma_f32_16x16x32_bf16 v[244:247], v[40:43], v[84:87], v[244:247]\n"
    "s_waitcnt lgkmcnt(6)\n"
    "buffer_load_dwordx4 v[208:211], v20, s[16:19], s85 offen offset:0 sc1\n"
    "v_mfma_f32_16x16x32_bf16 v[160:163], v[44:47], v[80:83], v[160:163]\n"
    "s_barrier\n"
    "ds_read_b64_tr_b16 v[36:37], v7 offset:28928\n"
    "v_cvt_pk_bf16_f32 v130, v144, v145\n"
    "v_mfma_f32_16x16x32_bf16 v[164:167], v[44:47], v[84:87], v[164:167]\n"
    "s_nop 0\n"
    "ds_read_b64_tr_b16 v[38:39], v7 offset:30976\n"
    "v_cvt_pk_bf16_f32 v131, v146, v147\n"
    "v_mfma_f32_16x16x32_bf16 v[248:251], v[48:51], v[80:83], v[248:251]\n"
    "ds_read_b64_tr_b16 v[40:41], v6 offset:32768\n"
    "v_cvt_pk_bf16_f32 v132, v132, v133\n"
    "v_mfma_f32_16x16x32_bf16 v[252:255], v[48:51], v[84:87], v[252:255]\n"
    "s_waitcnt lgkmcnt(5)\n"
    "ds_read_b64_tr_b16 v[42:43], v6 offset:34816\n"
    "v_cvt_pk_bf16_f32 v133, v134, v135\n"
    "v_mfma_f32_16x16x32_bf16 v[152:155], v[52:55], v[88:91], v[152:155]\n"
    "s_nop 0\n"
    "ds_read_b64_tr_b16 v[44:45], v7 offset:32768\n"
    "v_cvt_pk_bf16_f32 v134, v148, v149\n"
    "v_mfma_f32_16x16x32_bf16 v[156:159], v[52:55], v[92:95], v[156:159]\n"
    "ds_read_b64_tr_b16 v[46:47], v7 offset:34816\n"
    "ds_read_b64_tr_b16 v[48:49], v6 offset:33024\n"
    "v_mfma_f32_16x16x32_bf16 v[240:243], v[56:59], v[88:91], v[240:243]\n"
    "buffer_store_dwordx4 v[124:127], v20, s[16:19], s86 offen offset:1024 sc1\n"
    "v_mfma_f32_16x16x32_bf16 v[244:247], v[56:59], v[92:95], v[244:247]\n"
    "s_waitcnt lgkmcnt(5)\n"
    "ds_read_b64_tr_b16 v[50:51], v6 offset:35072\n"
    "v_cvt_pk_bf16_f32 v135, v150, v151\n"
    "v_mfma_f32_16x16x32_bf16 v[160:163], v[32:35], v[88:91], v[160:163]\n"
    "s_nop 0\n"
    "ds_read_b64_tr_b16 v[52:53], v7 offset:33024\n"
    "v_mfma_f32_16x16x32_bf16 v[164:167], v[32:35], v[92:95], v[164:167]\n"
    "ds_read_b64_tr_b16 v[54:55], v7 offset:35072\n"
    "v_mfma_f32_16x16x32_bf16 v[248:251], v[36:39], v[88:91], v[248:251]\n"
    "ds_read_b64_tr_b16 v[56:57], v6 offset:36864\n"
    "v_mfma_f32_16x16x32_bf16 v[252:255], v[36:39], v[92:95], v[252:255]\n"
    "s_waitcnt lgkmcnt(5)\n"
    "ds_read_b64_tr_b16 v[58:59], v6 offset:38912\n"
    "v_mfma_f32_16x16x32_bf16 v[152:155], v[40:43], v[96:99], v[152:155]\n"
    "s_add_u32 s86, s40, 8192\n"
    "ds_read_b64_tr_b16 v[32:33], v7 offset:36864\n"
    "ds_read_b64_tr_b16 v[34:35], v7 offset:38912\n"
    "v_mfma_f32_16x16x32_bf16 v[156:159], v[40:43], v[100:103], v[156:159]\n"
    "s_nop 0\n"
    "buffer_store_dwordx4 v[128:131], v20, s[16:19], s86 offen offset:0 sc1\n"
    "v_mfma_f32_16x16x32_bf16 v[240:243], v[44:47], v[96:99], v[240:243]\n"
    "ds_read_b64_tr_b16 v[36:37], v6 offset:37120\n"
    "v_mfma_f32_16x16x32_bf16 v[244:247], v[44:47], v[100:103], v[244:247]\n"
    "s_waitcnt lgkmcnt(5)\n"
    "ds_read_b64_tr_b16 v[38:39], v6 offset:39168\n"
    "v_mfma_f32_16x16x32_bf16 v[160:163], v[48:51], v[96:99], v[160:163]\n"
    "s_nop 0\n"
    "ds_read_b64_tr_b16 v[40:41], v7 offset:37120\n"
    "v_mfma_f32_16x16x32_bf16 v[164:167], v[48:51], v[100:103], v[164:167]\n"
    "ds_read_b64_tr_b16 v[42:43], v7 offset:39168\n"
    "v_mfma_f32_16x16x32_bf16 v[248:251], v[52:55], v[96:99], v[248:251]\n"
    "ds_read_b64_tr_b16 v[44:45], v6 offset:40960\n"
    "v_mfma_f32_16x16x32_bf16 v[252:255], v[52:55], v[100:103], v[252:255]\n"
    "s_waitcnt lgkmcnt(5)\n"
    "ds_read_b64_tr_b16 v[46:47], v6 offset:43008\n"
    "ds_read_b64_tr_b16 v[48:49], v7 offset:40960\n"
    "v_mfma_f32_16x16x32_bf16 v[152:155], v[56:59], v[104:107], v[152:155]\n"
    "s_nop 0\n"
    "buffer_load_dwordx4 v[212:215], v20, s[16:19], s85 offen offset:1024 sc1\n"
    "v_mfma_f32_16x16x32_bf16 v[156:159], v[56:59], v[108:111], v[156:159]\n"
    "ds_read_b64_tr_b16 v[50:51], v7 offset:43008\n"
    "v_mfma_f32_16x16x32_bf16 v[240:243], v[32:35], v[104:107], v[240:243]\n"
    "ds_read_b64_tr_b16 v[52:53], v6 offset:41216\n"
    "v_mfma_f32_16x16x32_bf16 v[244:247], v[32:35], v[108:111], v[244:247]\n"
    "s_waitcnt lgkmcnt(5)\n"
    "ds_read_b64_tr_b16 v[54:55], v6 offset:43264\n"
    "v_mfma_f32_16x16x32_bf16 v[160:163], v[36:39], v[104:107], v[160:163]\n"
    "s_nop 0\n"
    "ds_read_b64_tr_b16 v[56:57], v7 offset:41216\n"
    "v_mfma_f32_16x16x32_bf16 v[164:167], v[36:39], v[108:111], v[164:167]\n"
    "ds_read_b64_tr_b16 v[58:59], v7 offset:43264\n"
    "v_mfma_f32_16x16x32_bf16 v[248:251], v[40:43], v[104:107], v[248:251]\n"
    "ds_read_b64_tr_b16 v[32:33], v6 offset:45056\n"
    "ds_read_b64_tr_b16 v[34:35], v6 offset:47104\n"
    "v_mfma_f32_16x16x32_bf16 v[252:255], v[40:43], v[108:111], v[252:255]\n"
    "s_waitcnt lgkmcnt(6)\n"
    "buffer_store_dwordx4 v[132:135], v20, s[16:19], s86 offen offset:1024 sc1\n"
    "v_mfma_f32_16x16x32_bf16 v[152:155], v[44:47], v[112:115], v[152:155]\n"
    "s_nop 0\n"
    "ds_read_b64_tr_b16 v[36:37], v7 offset:45056\n"
    "v_mfma_f32_16x16x32_bf16 v[156:159], v[44:47], v[116:119], v[156:159]\n"
    "ds_read_b64_tr_b16 v[38:39], v7 offset:47104\n"
    "v_mfma_f32_16x16x32_bf16 v[240:243], v[48:51], v[112:115], v[240:243]\n"
    "ds_read_b64_tr_b16 v[40:41], v6 offset:45312\n"
    "v_mfma_f32_16x16x32_bf16 v[244:247], v[48:51], v[116:119], v[244:247]\n"
    "s_waitcnt lgkmcnt(5)\n"
    "ds_read_b64_tr_b16 v[42:43], v6 offset:47360\n"
    "v_mfma_f32_16x16x32_bf16 v[160:163], v[52:55], v[112:115], v[160:163]\n"
    "s_add_u32 s84, s56, 65536\n"
    "ds_read_b64_tr_b16 v[44:45], v7 offset:45312\n"
    "v_mfma_f32_16x16x32_bf16 v[164:167], v[52:55], v[116:119], v[164:167]\n"
    "s_add_u32 m0, s36, 106496\n"
    "ds_read_b64_tr_b16 v[46:47], v7 offset:47360\n"
    "ds_read_b64_tr_b16 v[48:49], v6 offset:49152\n"
    "v_mfma_f32_16x16x32_bf16 v[248:251], v[56:59], v[112:115], v[248:251]\n"
    "s_nop 0\n"
    "buffer_load_dwordx4 v19, s[12:15], s84 offen lds\n"
    "v_mfma_f32_16x16x32_bf16 v[252:255], v[56:59], v[116:119], v[252:255]\n"
    "s_waitcnt lgkmcnt(5)\n"
    "ds_read_b64_tr_b16 v[50:51], v6 offset:51200\n"
    "v_mfma_f32_16x16x32_bf16 v[152:155], v[32:35], a[0:3], v[152:155]\n"
    "s_nop 0\n"
    "ds_read_b64_tr_b16 v[52:53], v7 offset:49152\n"
    "v_mfma_f32_16x16x32_bf16 v[156:159], v[32:35], a[4:7], v[156:159]\n"
    "s_add_u32 s84, s59, 65536\n"
    "ds_read_b64_tr_b16 v[54:55], v7 offset:51200\n"
    "v_mfma_f32_16x16x32_bf16 v[240:243], v[36:39], a[0:3], v[240:243]\n"
    "s_add_u32 m0, s36, 110592\n"
    "ds_read_b64_tr_b16 v[56:57], v6 offset:49408\n"
    "ds_read_b64_tr_b16 v[58:59], v6 offset:51456\n"
    "v_mfma_f32_16x16x32_bf16 v[244:247], v[36:39], a[4:7], v[244:247]\n"
    "s_waitcnt lgkmcnt(6)\n"
    "buffer_load_dwordx4 v19, s[12:15], s84 offen lds\n"
    "v_mfma_f32_16x16x32_bf16 v[160:163], v[40:43], a[0:3], v[160:163]\n"
    "s_nop 0\n"
    "ds_read_b64_tr_b16 v[32:33], v7 offset:49408\n"
    "v_mfma_f32_16x16x32_bf16 v[164:167], v[40:43], a[4:7], v[164:167]\n"
    "s_add_u32 s84, s62, 65536\n"
    "ds_read_b64_tr_b16 v[34:35], v7 offset:51456\n"
    "v_mfma_f32_16x16x32_bf16 v[248:251], v[44:47], a[0:3], v[248:251]\n"
    "s_add_u32 m0, s36, 114688\n"
    "ds_read_b64_tr_b16 v[36:37], v6 offset:53248\n"
    "ds_read_b64_tr_b16 v[38:39], v6 offset:55296\n"
    "v_mfma_f32_16x16x32_bf16 v[252:255], v[44:47], a[4:7], v[252:255]\n"
    "s_waitcnt lgkmcnt(6)\n"
    "buffer_load_dwordx4 v19, s[12:15], s84 offen lds\n"
    "v_mfma_f32_16x16x32_bf16 v[152:155], v[48:51], a[8:11], v[152:155]\n"
    "s_nop 0\n"
    "ds_read_b64_tr_b16 v[40:41], v7 offset:53248\n"
    "v_mfma_f32_16x16x32_bf16 v[156:159], v[48:51], a[12:15], v[156:159]\n"
    "s_add_u32 s84, s57, 0\n"
    "ds_read_b64_tr_b16 v[42:43], v7 offset:55296\n"
    "v_mfma_f32_16x16x32_bf16 v[240:243], v[52:55], a[8:11], v[240:243]\n"
    "s_add_u32 m0, s36, 118784\n"
    "ds_read_b64_tr_b16 v[44:45], v6 offset:53504\n"
    "v_mfma_f32_16x16x32_bf16 v[244:247], v[52:55], a[12:15], v[244:247]\n"
    "s_waitcnt lgkmcnt(5)\n"
    "ds_read_b64_tr_b16 v[46:47], v6 offset:55552\n"
    "ds_read_b64_tr_b16 v[48:49], v7 offset:53504\n"
    "v_mfma_f32_16x16x32_bf16 v[160:163], v[56:59], a[8:11], v[160:163]\n"
    "buffer_load_dwordx4 v19, s[12:15], s84 offen lds\n"
    "v_mfma_f32_16x16x32_bf16 v[164:167], v[56:59], a[12:15], v[164:167]\n"
    "s_add_u32 s84, s60, 0\n"
    "ds_read_b64_tr_b16 v[50:51], v7 offset:55552\n"
    "v_mfma_f32_16x16x32_bf16 v[248:251], v[32:35], a[8:11], v[248:251]\n"
    "s_add_u32 m0, s36, 122880\n"
    "ds_read_b64_tr_b16 v[52:53], v6 offset:57344\n"
    "v_mfma_f32_16x16x32_bf16 v[252:255], v[32:35], a[12:15], v[252:255]\n"
    "s_waitcnt lgkmcnt(5)\n"
    "ds_read_b64_tr_b16 v[54:55], v6 offset:59392\n"
    "ds_read_b64_tr_b16 v[56:57], v7 offset:57344\n"
    "v_mfma_f32_16x16x32_bf16 v[152:155], v[36:39], a[16:19], v[152:155]\n"
    "buffer_load_dwordx4 v19, s[12:15], s84 offen lds\n"
    "v_mfma_f32_16x16x32_bf16 v[156:159], v[36:39], a[20:23], v[156:159]\n"
    "ds_read_b64_tr_b16 v[58:59], v7 offset:59392\n"
    "v_mfma_f32_16x16x32_bf16 v[240:243], v[40:43], a[16:19], v[240:243]\n"
    "s_add_u32 s84, s63, 0\n"
    "ds_read_b64_tr_b16 v[32:33], v6 offset:57600\n"
    "v_mfma_f32_16x16x32_bf16 v[244:247], v[40:43], a[20:23], v[244:247]\n"
    "s_waitcnt lgkmcnt(5)\n"
    "ds_read_b64_tr_b16 v[34:35], v6 offset:59648\n"
    "v_mfma_f32_16x16x32_bf16 v[160:163], v[44:47], a[16:19], v[160:163]\n"
    "s_add_u32 m0, s36, 126976\n"
    "ds_read_b64_tr_b16 v[36:37], v7 offset:57600\n"
    "ds_read_b64_tr_b16 v[38:39], v7 offset:59648\n"
    "v_mfma_f32_16x16x32_bf16 v[164:167], v[44:47], a[20:23], v[164:167]\n"
    "buffer_load_dwordx4 v19, s[12:15], s84 offen lds\n"
    "v_mfma_f32_16x16x32_bf16 v[248:251], v[48:51], a[16:19], v[248:251]\n"
    "ds_read_b64_tr_b16 v[40:41], v6 offset:61440\n"
    "v_mfma_f32_16x16x32_bf16 v[252:255], v[48:51], a[20:23], v[252:255]\n"
    "s_waitcnt lgkmcnt(5)\n"
    "ds_read_b64_tr_b16 v[42:43], v6 offset:63488\n"
    "v_mfma_f32_16x16x32_bf16 v[152:155], v[52:55], a[24:27], v[152:155]\n"
    "s_add_u32 s85, s42, 8192\n"
    "ds_read_b64_tr_b16 v[44:45], v7 offset:61440\n"
    "ds_read_b64_tr_b16 v[46:47], v7 offset:63488\n"
    "v_mfma_f32_16x16x32_bf16 v[156:159], v[52:55], a[28:31], v[156:159]\n"
    "s_nop 0\n"
    "buffer_load_dwordx4 v[216:219], v20, s[16:19], s85 offen offset:0 sc1\n"
    "v_mfma_f32_16x16x32_bf16 v[240:243], v[56:59], a[24:27], v[240:243]\n"
    "ds_read_b64_tr_b16 v[48:49], v6 offset:61696\n"
    "v_mfma_f32_16x16x32_bf16 v[244:247], v[56:59], a[28:31], v[244:247]\n"
    "s_waitcnt vmcnt(18) lgkmcnt(5)\n"
    "ds_read_b64_tr_b16 v[50:51], v6 offset:63744\n"
    "v_mfma_f32_16x16x32_bf16 v[160:163], v[32:35], a[24:27], v[160:163]\n"
    "s_barrier\n"
    "ds_read_b64_tr_b16 v[52:53], v7 offset:61696\n"
    "v_mfma_f32_16x16x32_bf16 v[164:167], v[32:35], a[28:31], v[164:167]\n"
    "ds_read_b64_tr_b16 v[54:55], v7 offset:63744\n"
    "v_mfma_f32_16x16x32_bf16 v[248:251], v[36:39], a[24:27], v[248:251]\n"
    "ds_read_b64_tr_b16 v[56:57], v8 offset:0\n"
    "v_mfma_f32_16x16x32_bf16 v[252:255], v[36:39], a[28:31], v[252:255]\n"
    "s_waitcnt lgkmcnt(5)\n"
    "ds_read_b64_tr_b16 v[58:59], v8 offset:2048\n"
    "ds_read_b64_tr_b16 v[32:33], v9 offset:0\n"
    "v_mfma_f32_16x16x32_bf16 v[152:155], v[40:43], a[32:35], v[152:155]\n"
    "s_nop 0\n"
    "buffer_load_dwordx4 v[220:223], v20, s[16:19], s85 offen offset:1024 sc1\n"
    "v_mfma_f32_16x16x32_bf16 v[156:159], v[40:43], a[36:39], v[156:159]\n"
    "ds_read_b64_tr_b16 v[34:35], v9 offset:2048\n"
    "v_mfma_f32_16x16x32_bf16 v[240:243], v[44:47], a[32:35], v[240:243]\n"
    "ds_read_b64_tr_b16 v[36:37], v8 offset:256\n"
    "v_mfma_f32_16x16x32_bf16 v[244:247], v[44:47], a[36:39], v[244:247]\n"
    "s_waitcnt lgkmcnt(5)\n"
    "ds_read_b64_tr_b16 v[38:39], v8 offset:2304\n"
    "v_mfma_f32_16x16x32_bf16 v[160:163], v[48:51], a[32:35], v[160:163]\n"
    "s_nop 0\n"
    "ds_read_b64_tr_b16 v[40:41], v9 offset:256\n"
    "v_mfma_f32_16x16x32_bf16 v[164:167], v[48:51], a[36:39], v[164:167]\n"
    "ds_read_b64_tr_b16 v[42:43], v9 offset:2304\n"
    "v_mfma_f32_16x16x32_bf16 v[248:251], v[52:55], a[32:35], v[248:251]\n"
    "ds_read_b64_tr_b16 v[44:45], v8 offset:4096\n"
    "v_mfma_f32_16x16x32_bf16 v[252:255], v[52:55], a[36:39], v[252:255]\n"
    "s_waitcnt vmcnt(8) lgkmcnt(5)\n"
    "ds_read_b64_tr_b16 v[46:47], v8 offset:6144\n"
    "v_mfma_f32_16x16x32_bf16 v[152:155], v[56:59], a[40:43], v[152:155]\n"
    "s_nop 0\n"
    "ds_read_b64_tr_b16 v[48:49], v9 offset:4096\n"
    "v_mfma_f32_16x16x32_bf16 v[156:159], v[56:59], a[44:47], v[156:159]\n"
    "ds_read_b64_tr_b16 v[50:51], v9 offset:6144\n"
    "v_mfma_f32_16x16x32_bf16 v[240:243], v[32:35], a[40:43], v[240:243]\n"
    "s_mov_b32 s83, 13369344\n"
    "ds_read_b64_tr_b16 v[52:53], v8 offset:4352\n"
    "ds_read_b64_tr_b16 v[54:55], v8 offset:6400\n"
    "v_mfma_f32_16x16x32_bf16 v[244:247], v[32:35], a[44:47], v[244:247]\n"
    "s_waitcnt lgkmcnt(6)\n"
    "buffer_store_dwordx2 v[60:61], v23, s[16:19], s83 offen sc1\n"
    "v_mfma_f32_16x16x32_bf16 v[160:163], v[36:39], a[40:43], v[160:163]\n"
    "LPAD12_%=:\n"
    "s_cbranch_execz LPADS_%=\n"
    "ds_read_b64_tr_b16 v[56:57], v9 offset:4352\n"
    "v_mfma_f32_16x16x32_bf16 v[164:167], v[36:39], a[44:47], v[164:167]\n"
    "ds_read_b64_tr_b16 v[58:59], v9 offset:6400\n"
    "v_mfma_f32_16x16x32_bf16 v[248:251], v[40:43], a[40:43], v[248:251]\n"
    "ds_read_b64_tr_b16 v[32:33], v8 offset:8192\n"
    "v_mfma_f32_16x16x32_bf16 v[252:255], v[40:43], a[44:47], v[252:255]\n"
    "s_waitcnt lgkmcnt(5)\n"
    "ds_read_b64_tr_b16 v[34:35], v8 offset:10240\n"
    "v_mfma_f32_16x16x32_bf16 v[152:155], v[44:47], a[48:51], v[152:155]\n"
    "s_nop 0\n"
    "ds_read_b64_tr_b16 v[36:37], v9 offset:8192\n"
    "v_mfma_f32_16x16x32_bf16 v[156:159], v[44:47], a[52:55], v[156:159]\n"
    "ds_read_b64_tr_b16 v[38:39], v9 offset:10240\n"
    "v_mfma_f32_16x16x32_bf16 v[240:243], v[48:51], a[48:51], v[240:243]\n"
    "ds_read_b64_tr_b16 v[40:41], v8 offset:8448\n"
    "v_mfma_f32_16x16x32_bf16 v[244:247], v[48:51], a[52:55], v[244:247]\n"
    "s_waitcnt lgkmcnt(5)\n"
    "ds_read_b64_tr_b16 v[42:43], v8 offset:10496\n"
    "v_mfma_f32_16x16x32_bf16 v[160:163], v[52:55], a[48:51], v[160:163]\n"
    "s_nop 0\n"
    "ds_read_b64_tr_b16 v[44:45], v9 offset:8448\n"
    "v_mfma_f32_16x16x32_bf16 v[164:167], v[52:55], a[52:55], v[164:167]\n"
    "ds_read_b64_tr_b16 v[46:47], v9 offset:10496\n"
    "v_mfma_f32_16x16x32_bf16 v[248:251], v[56:59], a[48:51], v[248:251]\n"
    "ds_read_b64_tr_b16 v[48:49], v8 offset:12288\n"
    "v_mfma_f32_16x16x32_bf16 v[252:255], v[56:59], a[52:55], v[252:255]\n"
    "s_waitcnt lgkmcnt(5)\n"
    "ds_read_b64_tr_b16 v[50:51], v8 offset:14336\n"
    "v_mfma_f32_16x16x32_bf16 v[152:155], v[32:35], a[56:59], v[152:155]\n"
    "s_nop 0\n"
    "ds_read_b64_tr_b16 v[52:53], v9 offset:12288\n"
    "v_mfma_f32_16x16x32_bf16 v[156:159], v[32:35], a[60:63], v[156:159]\n"
    "ds_read_b64_tr_b16 v[54:55], v9 offset:14336\n"
    "v_mfma_f32_16x16x32_bf16 v[240:243], v[36:39], a[56:59], v[240:243]\n"
    "ds_read_b64_tr_b16 v[56:57], v8 offset:12544\n"
    "v_mfma_f32_16x16x32_bf16 v[244:247], v[36:39], a[60:63], v[244:247]\n"
    "s_waitcnt lgkmcnt(5)\n"
    "ds_read_b64_tr_b16 v[58:59], v8 offset:14592\n"
    "v_mfma_f32_16x16x32_bf16 v[160:163], v[40:43], a[56:59], v[160:163]\n"
    "s_nop 0\n"
    "ds_read_b64_tr_b16 v[32:33], v9 offset:12544\n"
    "v_mfma_f32_16x16x32_bf16 v[164:167], v[40:43], a[60:63], v[164:167]\n"
    "ds_read_b64_tr_b16 v[34:35], v9 offset:14592\n"
    "v_mfma_f32_16x16x32_bf16 v[248:251], v[44:47], a[56:59], v[248:251]\n"
    "ds_read_b64_tr_b16 v[36:37], v8 offset:16384\n"
    "v_mfma_f32_16x16x32_bf16 v[252:255], v[44:47], a[60:63], v[252:255]\n"
    "s_waitcnt lgkmcnt(5)\n"
    "ds_read_b64_tr_b16 v[38:39], v8 offset:18432\n"
    "v_mfma_f32_16x16x32_bf16 v[152:155], v[48:51], a[64:67], v[152:155]\n"
    "s_nop 0\n"
    "ds_read_b64_tr_b16 v[40:41], v9 offset:16384\n"
    "v_mfma_f32_16x16x32_bf16 v[156:159], v[48:51], a[68:71], v[156:159]\n"
    "ds_read_b64_tr_b16 v[42:43], v9 offset:18432\n"
    "v_mfma_f32_16x16x32_bf16 v[240:243], v[52:55], a[64:67], v[240:243]\n"
    "ds_read_b64_tr_b16 v[44:45], v8 offset:16640\n"
    "v_mfma_f32_16x16x32_bf16 v[244:247], v[52:55], a[68:71], v[244:247]\n"
    "s_waitcnt lgkmcnt(5)\n"
    "ds_read_b64_tr_b16 v[46:47], v8 offset:18688\n"
    "v_mfma_f32_16x16x32_bf16 v[160:163], v[56:59], a[64:67], v[160:163]\n"
    "s_nop 0\n"
    "ds_read_b64_tr_b16 v[48:49], v9 offset:16640\n"
    "v_mfma_f32_16x16x32_bf16 v[164:167], v[56:59], a[68:71], v[164:167]\n"
    "ds_read_b64_tr_b16 v[50:51], v9 offset:18688\n"
    "v_mfma_f32_16x16x32_bf16 v[248:251], v[32:35], a[64:67], v[248:251]\n"
    "ds_read_b64_tr_b16 v[52:53], v8 offset:20480\n"
    "v_mfma_f32_16x16x32_bf16 v[252:255], v[32:35], a[68:71], v[252:255]\n"
    "s_waitcnt lgkmcnt(5)\n"
    "ds_read_b64_tr_b16 v[54:55], v8 offset:22528\n"
    "v_mfma_f32_16x16x32_bf16 v[152:155], v[36:39], a[72:75], v[152:155]\n"
    "s_nop 0\n"
    "ds_read_b64_tr_b16 v[56:57], v9 offset:20480\n"
    "v_mfma_f32_16x16x32_bf16 v[156:159], v[36:39], a[76:79], v[156:159]\n"
    "ds_read_b64_tr_b16 v[58:59], v9 offset:22528\n"
    "v_mfma_f32_16x16x32_bf16 v[240:243], v[40:43], a[72:75], v[240:243]\n"
    "ds_read_b64_tr_b16 v[32:33], v8 offset:20736\n"
    "v_mfma_f32_16x16x32_bf16 v[244:247], v[40:43], a[76:79], v[244:247]\n"
    "s_waitcnt vmcnt(1) lgkmcnt(5)\n"
    "ds_read_b64_tr_b16 v[34:35], v8 offset:22784\n"
    "v_mfma_f32_16x16x32_bf16 v[160:163], v[44:47], a[72:75], v[160:163]\n"
    "s_nop 0\n"
    "ds_read_b64_tr_b16 v[36:37], v9 offset:20736\n"
    "v_lshlrev_b32_e64 v120, 16, v208\n"
    "v_mfma_f32_16x16x32_bf16 v[164:167], v[44:47], a[76:79], v[164:167]\n"
    "ds_read_b64_tr_b16 v[38:39], v9 offset:22784\n"
    "v_and_b32 v121, 0xffff0000, v208\n"
    "v_xor_b32 v25, 0x800, v23\n"
    "v_mfma_f32_16x16x32_bf16 v[248:251], v[48:51], a[72:75], v[248:251]\n"
    "s_mov_b32 s87, 13369344\n"
    "ds_read_b64_tr_b16 v[40:41], v8 offset:24576\n"
    "ds_read_b64_tr_b16 v[42:43], v8 offset:26624\n"
    "v_mfma_f32_16x16x32_bf16 v[252:255], v[48:51], a[76:79], v[252:255]\n"
    "s_waitcnt lgkmcnt(6)\n"
    "buffer_load_dwordx2 v[62:63], v25, s[16:19], s87 offen sc1\n"
    "v_mfma_f32_16x16x32_bf16 v[152:155], v[52:55], a[80:83], v[152:155]\n"
    "s_nop 0\n"
    "ds_read_b64_tr_b16 v[44:45], v9 offset:24576\n"
    "v_lshlrev_b32_e64 v122, 16, v209\n"
    "v_mfma_f32_16x16x32_bf16 v[156:159], v[52:55], a[84:87], v[156:159]\n"
    "ds_read_b64_tr_b16 v[46:47], v9 offset:26624\n"
    "v_and_b32 v123, 0xffff0000, v209\n"
    "v_mfma_f32_16x16x32_bf16 v[240:243], v[56:59], a[80:83], v[240:243]\n"
    "ds_read_b64_tr_b16 v[48:49], v8 offset:24832\n"
    "v_lshlrev_b32_e64 v124, 16, v210\n"
    "v_mfma_f32_16x16x32_bf16 v[244:247], v[56:59], a[84:87], v[244:247]\n"
    "s_waitcnt lgkmcnt(5)\n"
    "ds_read_b64_tr_b16 v[50:51], v8 offset:26880\n"
    "v_and_b32 v125, 0xffff0000, v210\n"
    "v_mfma_f32_16x16x32_bf16 v[160:163], v[32:35], a[80:83], v[160:163]\n"
    "s_nop 0\n"
    "ds_read_b64_tr_b16 v[52:53], v9 offset:24832\n"
    "v_lshlrev_b32_e64 v126, 16, v211\n"
    "v_mfma_f32_16x16x32_bf16 v[164:167], v[32:35], a[84:87], v[164:167]\n"
    "ds_read_b64_tr_b16 v[54:55], v9 offset:26880\n"
    "v_and_b32 v127, 0xffff0000, v211\n"
    "v_mfma_f32_16x16x32_bf16 v[248:251], v[36:39], a[80:83], v[248:251]\n"
    "ds_read_b64_tr_b16 v[56:57], v8 offset:28672\n"
    "v_lshlrev_b32_e64 v128, 16, v212\n"
    "v_mfma_f32_16x16x32_bf16 v[252:255], v[36:39], a[84:87], v[252:255]\n"
    "s_waitcnt lgkmcnt(5)\n"
    "ds_read_b64_tr_b16 v[58:59], v8 offset:30720\n"
    "v_and_b32 v129, 0xffff0000, v212\n"
    "v_mfma_f32_16x16x32_bf16 v[152:155], v[40:43], a[88:91], v[152:155]\n"
    "s_nop 0\n"
    "ds_read_b64_tr_b16 v[32:33], v9 offset:28672\n"
    "v_lshlrev_b32_e64 v130, 16, v213\n"
    "v_mfma_f32_16x16x32_bf16 v[156:159], v[40:43], a[92:95], v[156:159]\n"
    "ds_read_b64_tr_b16 v[34:35], v9 offset:30720\n"
    "v_and_b32 v131, 0xffff0000, v213\n"
    "v_mfma_f32_16x16x32_bf16 v[240:243], v[44:47], a[88:91], v[240:243]\n"
    "ds_read_b64_tr_b16 v[36:37], v8 offset:28928\n"
    "v_lshlrev_b32_e64 v132, 16, v214\n"
    "v_mfma_f32_16x16x32_bf16 v[244:247], v[44:47], a[92:95], v[244:247]\n"
    "s_waitcnt lgkmcnt(5)\n"
    "ds_read_b64_tr_b16 v[38:39], v8 offset:30976\n"
    "v_and_b32 v133, 0xffff0000, v214\n"
    "v_mfma_f32_16x16x32_bf16 v[160:163], v[48:51], a[88:91], v[160:163]\n"
    "s_barrier\n"
    "ds_read_b64_tr_b16 v[40:41], v9 offset:28928\n"
    "v_lshlrev_b32_e64 v134, 16, v215\n"
    "v_mfma_f32_16x16x32_bf16 v[164:167], v[48:51], a[92:95], v[164:167]\n"
    "ds_read_b64_tr_b16 v[42:43], v9 offset:30976\n"
    "v_and_b32 v135, 0xffff0000, v215\n"
    "v_mfma_f32_16x16x32_bf16 v[248:251], v[52:55], a[88:91], v[248:251]\n"
    "ds_read_b64_tr_b16 v[44:45], v8 offset:32768\n"
    "v_lshlrev_b32_e64 v136, 16, v216\n"
    "v_mfma_f32_16x16x32_bf16 v[252:255], v[52:55], a[92:95], v[252:255]\n"
    "s_waitcnt lgkmcnt(5)\n"
    "ds_read_b64_tr_b16 v[46:47], v8 offset:34816\n"
    "v_and_b32 v137, 0xffff0000, v216\n"
    "v_mfma_f32_16x16x32_bf16 v[152:155], v[56:59], a[96:99], v[152:155]\n"
    "s_nop 0\n"
    "ds_read_b64_tr_b16 v[48:49], v9 offset:32768\n"
    "v_lshlrev_b32_e64 v138, 16, v217\n"
    "v_mfma_f32_16x16x32_bf16 v[156:159], v[56:59], a[100:103], v[156:159]\n"
    "ds_read_b64_tr_b16 v[50:51], v9 offset:34816\n"
    "v_and_b32 v139, 0xffff0000, v217\n"
    "v_mfma_f32_16x16x32_bf16 v[240:243], v[32:35], a[96:99], v[240:243]\n"
    "ds_read_b64_tr_b16 v[52:53], v8 offset:33024\n"
    "v_lshlrev_b32_e64 v140, 16, v218\n"
    "v_mfma_f32_16x16x32_bf16 v[244:247], v[32:35], a[100:103], v[244:247]\n"
    "s_waitcnt lgkmcnt(5)\n"
    "ds_read_b64_tr_b16 v[54:55], v8 offset:35072\n"
    "v_and_b32 v141, 0xffff0000, v218\n"
    "v_mfma_f32_16x16x32_bf16 v[160:163], v[36:39], a[96:99], v[160:163]\n"
    "s_nop 0\n"
    "ds_read_b64_tr_b16 v[56:57], v9 offset:33024\n"
    "v_lshlrev_b32_e64 v142, 16, v219\n"
    "v_mfma_f32_16x16x32_bf16 v[164:167], v[36:39], a[100:103], v[164:167]\n"
    "ds_read_b64_tr_b16 v[58:59], v9 offset:35072\n"
    "v_and_b32 v143, 0xffff0000, v219\n"
    "v_mfma_f32_16x16x32_bf16 v[248:251], v[40:43], a[96:99], v[248:251]\n"
    "ds_read_b64_tr_b16 v[32:33], v8 offset:36864\n"
    "v_lshlrev_b32_e64 v144, 16, v220\n"
    "v_mfma_f32_16x16x32_bf16 v[252:255], v[40:43], a[100:103], v[252:255]\n"
    "s_waitcnt lgkmcnt(5)\n"
    "ds_read_b64_tr_b16 v[34:35], v8 offset:38912\n"
    "v_and_b32 v145, 0xffff0000, v220\n"
    "v_mfma_f32_16x16x32_bf16 v[152:155], v[44:47], a[104:107], v[152:155]\n"
    "s_nop 0\n"
    "ds_read_b64_tr_b16 v[36:37], v9 offset:36864\n"
    "v_lshlrev_b32_e64 v146, 16, v221\n"
    "v_mfma_f32_16x16x32_bf16 v[156:159], v[44:47], a[108:111], v[156:159]\n"
    "ds_read_b64_tr_b16 v[38:39], v9 offset:38912\n"
    "v_and_b32 v147, 0xffff0000, v221\n"
    "v_mfma_f32_16x16x32_bf16 v[240:243], v[48:51], a[104:107], v[240:243]\n"
    "ds_read_b64_tr_b16 v[40:41], v8 offset:37120\n"
    "v_lshlrev_b32_e64 v148, 16, v222\n"
    "v_mfma_f32_16x16x32_bf16 v[244:247], v[48:51], a[108:111], v[244:247]\n"
    "s_waitcnt vmcnt(0) lgkmcnt(5)\n"
    "ds_read_b64_tr_b16 v[42:43], v8 offset:39168\n"
    "v_and_b32 v149, 0xffff0000, v222\n"
    "v_cmp_ne_u64 vcc, s[98:99], v[62:63]\n"
    "v_mfma_f32_16x16x32_bf16 v[160:163], v[52:55], a[104:107], v[160:163]\n"
    "ds_read_b64_tr_b16 v[44:45], v9 offset:37120\n"
    "v_lshlrev_b32_e64 v150, 16, v223\n"
    "v_mfma_f32_16x16x32_bf16 v[164:167], v[52:55], a[108:111], v[164:167]\n"
    "ds_read_b64_tr_b16 v[46:47], v9 offset:39168\n"
    "v_and_b32 v151, 0xffff0000, v223\n"
    "s_cbranch_vccnz LPLPY_%=s\n"
    "LPLPY_%=:\n"
    "v_mfma_f32_16x16x32_bf16 v[248:251], v[56:59], a[104:107], v[248:251]\n"
    "s_nop 0\n"
    "ds_read_b64_tr_b16 v[48:49], v8 offset:40960\n"
    "v_mfma_f32_16x16x32_bf16 v[252:255], v[56:59], a[108:111], v[252:255]\n"
    "s_waitcnt lgkmcnt(5)\n"
    "ds_read_b64_tr_b16 v[50:51], v8 offset:43008\n"
    "v_mfma_f32_16x16x32_bf16 v[152:155], v[32:35], a[112:115], v[152:155]\n"
    "s_add_u32 s85, s41, 0\n"
    "ds_read_b64_tr_b16 v[52:53], v9 offset:40960\n"
    "ds_read_b64_tr_b16 v[54:55], v9 offset:43008\n"
    "v_mfma_f32_16x16x32_bf16 v[156:159], v[32:35], a[116:119], v[156:159]\n"
    "buffer_load_dwordx4 a[168:171], v20, s[16:19], s85 offen offset:0 sc1\n"
    "v_mfma_f32_16x16x32_bf16 v[240:243], v[36:39], a[112:115], v[240:243]\n"
    "ds_read_b64_tr_b16 v[56:57], v8 offset:41216\n"
    "ds_read_b64_tr_b16 v[58:59], v8 offset:43264\n"
    "v_mfma_f32_16x16x32_bf16 v[244:247], v[36:39], a[116:119], v[244:247]\n"
    "s_waitcnt lgkmcnt(6)\n"
    "buffer_load_dwordx4 a[172:175], v20, s[16:19], s85 offen offset:1024 sc1\n"
    "v_mfma_f32_16x16x32_bf16 v[160:163], v[40:43], a[112:115], v[160:163]\n"
    "s_add_u32 s85, s41, 8192\n"
    "ds_read_b64_tr_b16 v[32:33], v9 offset:41216\n"
    "ds_read_b64_tr_b16 v[34:35], v9 offset:43264\n"
    "v_mfma_f32_16x16x32_bf16 v[164:167], v[40:43], a[116:119], v[164:167]\n"
    "s_nop 0\n"
    "buffer_load_dwordx4 a[176:179], v20, s[16:19], s85 offen offset:0 sc1\n"
    "v_mfma_f32_16x16x32_bf16 v[248:251], v[44:47], a[112:115], v[248:251]\n"
    "ds_read_b64_tr_b16 v[36:37], v8 offset:45056\n"
    "ds_read_b64_tr_b16 v[38:39], v8 offset:47104\n"
    "v_mfma_f32_16x16x32_bf16 v[252:255], v[44:47], a[116:119], v[252:255]\n"
    "s_waitcnt lgkmcnt(6)\n"
    "buffer_load_dwordx4 a[180:183], v20, s[16:19], s85 offen offset:1024 sc1\n"
    "v_mfma_f32_16x16x32_bf16 v[152:155], v[48:51], a[120:123], v[152:155]\n"
    "s_nop 0\n"
    "ds_read_b64_tr_b16 v[40:41], v9 offset:45056\n"
    "v_mfma_f32_16x16x32_bf16 v[156:159], v[48:51], a[124:127], v[156:159]\n"
    "ds_read_b64_tr_b16 v[42:43], v9 offset:47104\n"
    "v_mfma_f32_16x16x32_bf16 v[240:243], v[52:55], a[120:123], v[240:243]\n"
    "ds_read_b64_tr_b16 v[44:45], v8 offset:45312\n"
    "v_mfma_f32_16x16x32_bf16 v[244:247], v[52:55], a[124:127], v[244:247]\n"
    "s_waitcnt lgkmcnt(5)\n"
    "ds_read_b64_tr_b16 v[46:47], v8 offset:47360\n"
    "v_mfma_f32_16x16x32_bf16 v[160:163], v[56:59], a[120:123], v[160:163]\n"
    "s_nop 0\n"
    "ds_read_b64_tr_b16 v[48:49], v9 offset:45312\n"
    "v_mfma_f32_16x16x32_bf16 v[164:167], v[56:59], a[124:127], v[164:167]\n"
    "ds_read_b64_tr_b16 v[50:51], v9 offset:47360\n"
    "v_mfma_f32_16x16x32_bf16 v[248:251], v[32:35], a[120:123], v[248:251]\n"
    "ds_read_b64_tr_b16 v[52:53], v8 offset:49152\n"
    "v_mfma_f32_16x16x32_bf16 v[252:255], v[32:35], a[124:127], v[252:255]\n"
    "s_waitcnt lgkmcnt(5)\n"
    "ds_read_b64_tr_b16 v[54:55], v8 offset:51200\n"
    "v_mfma_f32_16x16x32_bf16 v[152:155], v[36:39], a[128:131], v[152:155]\n"
    "s_nop 0\n"
    "ds_read_b64_tr_b16 v[56:57], v9 offset:49152\n"
    "v_mfma_f32_16x16x32_bf16 v[156:159], v[36:39], a[132:135], v[156:159]\n"
    "ds_read_b64_tr_b16 v[58:59], v9 offset:51200\n"
    "v_mfma_f32_16x16x32_bf16 v[240:243], v[40:43], a[128:131], v[240:243]\n"
    "ds_read_b64_tr_b16 v[32:33], v8 offset:49408\n"
    "v_mfma_f32_16x16x32_bf16 v[244:247], v[40:43], a[132:135], v[244:247]\n"
    "s_waitcnt lgkmcnt(5)\n"
    "ds_read_b64_tr_b16 v[34:35], v8 offset:51456\n"
    "v_mfma_f32_16x16x32_bf16 v[160:163], v[44:47], a[128:131], v[160:163]\n"
    "s_nop 0\n"
    "ds_read_b64_tr_b16 v[36:37], v9 offset:49408\n"
    "v_mfma_f32_16x16x32_bf16 v[164:167], v[44:47], a[132:135], v[164:167]\n"
    "ds_read_b64_tr_b16 v[38:39], v9 offset:51456\n"
    "v_mfma_f32_16x16x32_bf16 v[248:251], v[48:51], a[128:131], v[248:251]\n"
    "ds_read_b64_tr_b16 v[40:41], v8 offset:53248\n"
    "v_mfma_f32_16x16x32_bf16 v[252:255], v[48:51], a[132:135], v[252:255]\n"
    "s_waitcnt lgkmcnt(5)\n"
    "ds_read_b64_tr_b16 v[42:43], v8 offset:55296\n"
    "v_mfma_f32_16x16x32_bf16 v[152:155], v[52:55], a[136:139], v[152:155]\n"
    "s_nop 0\n"
    "ds_read_b64_tr_b16 v[44:45], v9 offset:53248\n"
    "v_mfma_f32_16x16x32_bf16 v[156:159], v[52:55], a[140:143], v[156:159]\n"
    "ds_read_b64_tr_b16 v[46:47], v9 offset:55296\n"
    "v_mfma_f32_16x16x32_bf16 v[240:243], v[56:59], a[136:139], v[240:243]\n"
    "ds_read_b64_tr_b16 v[48:49], v8 offset:53504\n"
    "v_mfma_f32_16x16x32_bf16 v[244:247], v[56:59], a[140:143], v[244:247]\n"
    "s_waitcnt lgkmcnt(5)\n"
    "ds_read_b64_tr_b16 v[50:51], v8 offset:55552\n"
    "v_mfma_f32_16x16x32_bf16 v[160:163], v[32:35], a[136:139], v[160:163]\n"
    "s_nop 0\n"
    "ds_read_b64_tr_b16 v[52:53], v9 offset:53504\n"
    "v_mfma_f32_16x16x32_bf16 v[164:167], v[32:35], a[140:143], v[164:167]\n"
    "ds_read_b64_tr_b16 v[54:55], v9 offset:55552\n"
    "v_mfma_f32_16x16x32_bf16 v[248:251], v[36:39], a[136:139], v[248:251]\n"
    "ds_read_b64_tr_b16 v[56:57], v8 offset:57344\n"
    "v_mfma_f32_16x16x32_bf16 v[252:255], v[36:39], a[140:143], v[252:255]\n"
    "s_waitcnt lgkmcnt(5)\n"
    "ds_read_b64_tr_b16 v[58:59], v8 offset:59392\n"
    "v_mfma_f32_16x16x32_bf16 v[152:155], v[40:43], a[144:147], v[152:155]\n"
    "s_nop 0\n"
    "ds_read_b64_tr_b16 v[32:33], v9 offset:57344\n"
    "v_mfma_f32_16x16x32_bf16 v[156:159], v[40:43], a[148:151], v[156:159]\n"
    "ds_read_b64_tr_b16 v[34:35], v9 offset:59392\n"
    "v_mfma_f32_16x16x32_bf16 v[240:243], v[44:47], a[144:147], v[240:243]\n"
    "ds_read_b64_tr_b16 v[36:37], v8 offset:57600\n"
    "v_mfma_f32_16x16x32_bf16 v[244:247], v[44:47], a[148:151], v[244:247]\n"
    "s_waitcnt lgkmcnt(5)\n"
    "ds_read_b64_tr_b16 v[38:39], v8 offset:59648\n"
    "v_mfma_f32_16x16x32_bf16 v[160:163], v[48:51], a[144:147], v[160:163]\n"
    "s_nop 0\n"
    "ds_read_b64_tr_b16 v[40:41], v9 offset:57600\n"
    "v_mfma_f32_16x16x32_bf16 v[164:167], v[48:51], a[148:151], v[164:167]\n"
    "ds_read_b64_tr_b16 v[42:43], v9 offset:59648\n"
    "v_mfma_f32_16x16x32_bf16 v[248:251], v[52:55], a[144:147], v[248:251]\n"
    "ds_read_b64_tr_b16 v[44:45], v8 offset:61440\n"
    "v_mfma_f32_16x16x32_bf16 v[252:255], v[52:55], a[148:151], v[252:255]\n"
    "s_waitcnt lgkmcnt(5)\n"
    "ds_read_b64_tr_b16 v[46:47], v8 offset:63488\n"
    "v_mfma_f32_16x16x32_bf16 v[152:155], v[56:59], a[152:155], v[152:155]\n"
    "s_nop 0\n"
    "ds_read_b64_tr_b16 v[48:49], v9 offset:61440\n"
    "v_mfma_f32_16x16x32_bf16 v[156:159], v[56:59], a[156:159], v[156:159]\n"
    "ds_read_b64_tr_b16 v[50:51], v9 offset:63488\n"
    "v_mfma_f32_16x16x32_bf16 v[240:243], v[32:35], a[152:155], v[240:243]\n"
    "ds_read_b64_tr_b16 v[52:53], v8 offset:61696\n"
    "v_mfma_f32_16x16x32_bf16 v[244:247], v[32:35], a[156:159], v[244:247]\n"
    "s_waitcnt lgkmcnt(5)\n"
    "ds_read_b64_tr_b16 v[54:55], v8 offset:63744\n"
    "v_mfma_f32_16x16x32_bf16 v[160:163], v[36:39], a[152:155], v[160:163]\n"
    "s_nop 0\n"
    "ds_read_b64_tr_b16 v[56:57], v9 offset:61696\n"
    "v_mfma_f32_16x16x32_bf16 v[164:167], v[36:39], a[156:159], v[164:167]\n"
    "ds_read_b64_tr_b16 v[58:59], v9 offset:63744\n"
    "v_mfma_f32_16x16x32_bf16 v[248:251], v[40:43], a[152:155], v[248:251]\n"
    "v_mfma_f32_16x16x32_bf16 v[252:255], v[40:43], a[156:159], v[252:255]\n"
    "s_waitcnt lgkmcnt(4)\n"
    "v_mfma_f32_16x16x32_bf16 v[152:155], v[44:47], a[160:163], v[152:155]\n"
    "s_nop 0\n"
    "v_mfma_f32_16x16x32_bf16 v[156:159], v[44:47], a[164:167], v[156:159]\n"
    "v_mfma_f32_16x16x32_bf16 v[240:243], v[48:51], a[160:163], v[240:243]\n"
    "v_mfma_f32_16x16x32_bf16 v[244:247], v[48:51], a[164:167], v[244:247]\n"
    "s_waitcnt lgkmcnt(0)\n"
    "v_mfma_f32_16x16x32_bf16 v[160:163], v[52:55], a[160:163], v[160:163]\n"
    "s_nop 0\n"
    "v_mfma_f32_16x16x32_bf16 v[164:167], v[52:55], a[164:167], v[164:167]\n"
    "v_mfma_f32_16x16x32_bf16 v[248:251], v[56:59], a[160:163], v[248:251]\n"
    "v_mfma_f32_16x16x32_bf16 v[252:255], v[56:59], a[164:167], v[252:255]\n"
    "v_mfma_f32_16x16x32_bf16 v[152:155], v[170:173], a[184:187], v[152:155]\n"
    "v_mfma_f32_16x16x32_bf16 v[240:243], v[168:171], a[184:187], v[240:243]\n"
    "v_mfma_f32_16x16x32_bf16 v[156:159], v[170:173], a[188:191], v[156:159]\n"
    "v_mfma_f32_16x16x32_bf16 v[244:247], v[168:171], a[188:191], v[244:247]\n"
    "v_mfma_f32_16x16x32_bf16 v[160:163], v[170:173], a[192:195], v[160:163]\n"
    "v_mfma_f32_16x16x32_bf16 v[248:251], v[168:171], a[192:195], v[248:251]\n"
    "v_mfma_f32_16x16x32_bf16 v[164:167], v[170:173], a[196:199], v[164:167]\n"
    "v_mfma_f32_16x16x32_bf16 v[252:255], v[168:171], a[196:199], v[252:255]\n"
    "v_mfma_f32_16x16x32_bf16 v[152:155], v[170:173], a[200:203], v[152:155]\n"
    "v_mfma_f32_16x16x32_bf16 v[240:243], v[168:171], a[200:203], v[240:243]\n"
    "v_mfma_f32_16x16x32_bf16 v[156:159], v[170:173], a[204:207], v[156:159]\n"
    "v_mfma_f32_16x16x32_bf16 v[244:247], v[168:171], a[204:207], v[244:247]\n"
    "v_mfma_f32_16x16x32_bf16 v[160:163], v[170:173], a[208:211], v[160:163]\n"
    "v_mfma_f32_16x16x32_bf16 v[248:251], v[168:171], a[208:211], v[248:251]\n"
    "v_mfma_f32_16x16x32_bf16 v[164:167], v[170:173], a[212:215], v[164:167]\n"
    "v_mfma_f32_16x16x32_bf16 v[252:255], v[168:171], a[212:215], v[252:255]\n"
    "s_waitcnt vmcnt(3)\n"
    "v_mfma_f32_16x16x32_bf16 v[152:155], v[170:173], a[168:171], v[152:155]\n"
    "s_nop 0\n"
    "v_mfma_f32_16x16x32_bf16 v[240:243], v[168:171], a[168:171], v[240:243]\n"
    "s_waitcnt vmcnt(2)\n"
    "v_mfma_f32_16x16x32_bf16 v[156:159], v[170:173], a[172:175], v[156:159]\n"
    "s_nop 0\n"
    "v_mfma_f32_16x16x32_bf16 v[244:247], v[168:171], a[172:175], v[244:247]\n"
    "s_waitcnt vmcnt(1)\n"
    "v_mfma_f32_16x16x32_bf16 v[160:163], v[170:173], a[176:179], v[160:163]\n"
    "s_nop 0\n"
    "v_mfma_f32_16x16x32_bf16 v[248:251], v[168:171], a[176:179], v[248:251]\n"
    "s_waitcnt vmcnt(0)\n"
    "v_mfma_f32_16x16x32_bf16 v[164:167], v[170:173], a[180:183], v[164:167]\n"
    "s_nop 0\n"
    "v_mfma_f32_16x16x32_bf16 v[252:255], v[168:171], a[180:183], v[252:255]\n"
    "s_mov_b32 s84, 0xbfb8aa3b\n"
    "s_mov_b32 s85, 0xbfb8aa3b\n"
    "v_pk_mul_f32 v[208:209], v[152:153], s[84:85]\n"
    "v_pk_mul_f32 v[210:211], v[154:155], s[84:85]\n"
    "v_pk_mul_f32 v[212:213], v[240:241], s[84:85]\n"
    "v_pk_mul_f32 v[214:215], v[242:243], s[84:85]\n"
    "v_exp_f32 v208, v208\n"
    "v_exp_f32 v209, v209\n"
    "v_exp_f32 v210, v210\n"
    "v_exp_f32 v211, v211\n"
    "v_exp_f32 v212, v212\n"
    "v_exp_f32 v213, v213\n"
    "v_exp_f32 v214, v214\n"
    "v_exp_f32 v215, v215\n"
    "v_pk_mul_f32 v[152:153], v[152:153], v[120:121]\n"
    "v_pk_mul_f32 v[154:155], v[154:155], v[122:123]\n"
    "v_pk_mul_f32 v[240:241], v[240:241], v[124:125]\n"
    "v_pk_mul_f32 v[242:243], v[242:243], v[126:127]\n"
    "v_pk_add_f32 v[208:209], v[208:209], 1.0 op_sel_hi:[1,0]\n"
    "v_pk_add_f32 v[210:211], v[210:211], 1.0 op_sel_hi:[1,0]\n"
    "v_pk_add_f32 v[212:213], v[212:213], 1.0 op_sel_hi:[1,0]\n"
    "v_pk_add_f32 v[214:215], v[214:215], 1.0 op_sel_hi:[1,0]\n"
    "v_rcp_f32 v208, v208\n"
    "v_rcp_f32 v209, v209\n"
    "v_rcp_f32 v210, v210\n"
    "v_rcp_f32 v211, v211\n"
    "v_rcp_f32 v212, v212\n"
    "v_rcp_f32 v213, v213\n"
    "v_rcp_f32 v214, v214\n"
    "v_rcp_f32 v215, v215\n"
    "v_pk_fma_f32 v[152:153], v[152:153], v[208:209], v[176:177]\n"
    "v_pk_fma_f32 v[154:155], v[154:155], v[210:211], v[178:179]\n"
    "v_pk_fma_f32 v[240:241], v[240:241], v[212:213], v[180:181]\n"
    "v_pk_fma_f32 v[242:243], v[242:243], v[214:215], v[182:183]\n"
    "v_cvt_pk_bf16_f32 v208, v152, v153\n"
    "v_cvt_pk_bf16_f32 v209, v154, v155\n"
    "v_cvt_pk_bf16_f32 v210, v240, v241\n"
    "v_cvt_pk_bf16_f32 v211, v242, v243\n"
    "s_nop 0\n"
    "v_permlane16_swap_b32 v208, v210\n"
    "v_permlane16_swap_b32 v209, v211\n"
    "buffer_store_dwordx4 v[208:211], v22, s[24:27], 0 offen offset:0 sc1\n"
    "v_pk_mul_f32 v[224:225], v[160:161], s[84:85]\n"
    "v_pk_mul_f32 v[226:227], v[162:163], s[84:85]\n"
    "v_pk_mul_f32 v[228:229], v[248:249], s[84:85]\n"
    "v_pk_mul_f32 v[230:231], v[250:251], s[84:85]\n"
    "v_exp_f32 v224, v224\n"
    "v_exp_f32 v225, v225\n"
    "v_exp_f32 v226, v226\n"
    "v_exp_f32 v227, v227\n"
    "v_exp_f32 v228, v228\n"
    "v_exp_f32 v229, v229\n"
    "v_exp_f32 v230, v230\n"
    "v_exp_f32_e64 v231, v231\n"
    "v_pk_mul_f32 v[160:161], v[160:161], v[136:137]\n"
    "v_pk_mul_f32 v[162:163], v[162:163], v[138:139]\n"
    "v_pk_mul_f32 v[248:249], v[248:249], v[140:141]\n"
    "v_pk_mul_f32 v[250:251], v[250:251], v[142:143]\n"
    "v_pk_add_f32 v[224:225], v[224:225], 1.0 op_sel_hi:[1,0]\n"
    "v_pk_add_f32 v[226:227], v[226:227], 1.0 op_sel_hi:[1,0]\n"
    "v_pk_add_f32 v[228:229], v[228:229], 1.0 op_sel_hi:[1,0]\n"
    "v_pk_add_f32 v[230:231], v[230:231], 1.0 op_sel_hi:[1,0]\n"
    "v_rcp_f32 v224, v224\n"
    "v_rcp_f32 v225, v225\n"
    "v_rcp_f32 v226, v226\n"
    "v_rcp_f32 v227, v227\n"
    "v_rcp_f32 v228, v228\n"
    "v_rcp_f32 v229, v229\n"
    "v_rcp_f32 v230, v230\n"
    "v_rcp_f32 v231, v231\n"
    "v_pk_fma_f32 v[160:161], v[160:161], v[224:225], v[192:193]\n"
    "v_pk_fma_f32 v[162:163], v[162:163], v[226:227], v[194:195]\n"
    "v_pk_fma_f32 v[248:249], v[248:249], v[228:229], v[196:197]\n"
    "v_pk_fma_f32 v[250:251], v[250:251], v[230:231], v[198:199]\n"
    "v_cvt_pk_bf16_f32 v224, v160, v161\n"
    "v_cvt_pk_bf16_f32 v225, v162, v163\n"
    "v_cvt_pk_bf16_f32 v226, v248, v249\n"
    "v_cvt_pk_bf16_f32 v227, v250, v251\n"
    "s_nop 0\n"
    "v_permlane16_swap_b32 v224, v226\n"
    "v_permlane16_swap_b32 v225, v227\n"
    "buffer_store_dwordx4 v[224:227], v22, s[24:27], 0 offen offset:64 sc1\n"
    "v_pk_mul_f32 v[216:217], v[156:157], s[84:85]\n"
    "v_pk_mul_f32 v[218:219], v[158:159], s[84:85]\n"
    "v_pk_mul_f32 v[220:221], v[244:245], s[84:85]\n"
    "v_pk_mul_f32 v[222:223], v[246:247], s[84:85]\n"
    "v_exp_f32 v216, v216\n"
    "v_exp_f32 v217, v217\n"
    "v_exp_f32 v218, v218\n"
    "v_exp_f32 v219, v219\n"
    "v_exp_f32 v220, v220\n"
    "v_exp_f32 v221, v221\n"
    "v_exp_f32 v222, v222\n"
    "v_exp_f32_e64 v223, v223\n"
    "v_pk_mul_f32 v[156:157], v[156:157], v[128:129]\n"
    "v_pk_mul_f32 v[158:159], v[158:159], v[130:131]\n"
    "v_pk_mul_f32 v[244:245], v[244:245], v[132:133]\n"
    "v_pk_mul_f32 v[246:247], v[246:247], v[134:135]\n"
    "v_pk_add_f32 v[216:217], v[216:217], 1.0 op_sel_hi:[1,0]\n"
    "v_pk_add_f32 v[218:219], v[218:219], 1.0 op_sel_hi:[1,0]\n"
    "v_pk_add_f32 v[220:221], v[220:221], 1.0 op_sel_hi:[1,0]\n"
    "v_pk_add_f32 v[222:223], v[222:223], 1.0 op_sel_hi:[1,0]\n"
    "v_rcp_f32 v216, v216\n"
    "v_rcp_f32 v217, v217\n"
    "v_rcp_f32 v218, v218\n"
    "v_rcp_f32 v219, v219\n"
    "v_rcp_f32 v220, v220\n"
    "v_rcp_f32 v221, v221\n"
    "v_rcp_f32 v222, v222\n"
    "v_rcp_f32 v223, v223\n"
    "v_pk_fma_f32 v[156:157], v[156:157], v[216:217], v[184:185]\n"
    "v_pk_fma_f32 v[158:159], v[158:159], v[218:219], v[186:187]\n"
    "v_pk_fma_f32 v[244:245], v[244:245], v[220:221], v[188:189]\n"
    "v_pk_fma_f32 v[246:247], v[246:247], v[222:223], v[190:191]\n"
    "v_cvt_pk_bf16_f32 v216, v156, v157\n"
    "v_cvt_pk_bf16_f32 v217, v158, v159\n"
    "v_cvt_pk_bf16_f32 v218, v244, v245\n"
    "v_cvt_pk_bf16_f32 v219, v246, v247\n"
    "s_nop 0\n"
    "v_permlane16_swap_b32 v216, v218\n"
    "v_permlane16_swap_b32 v217, v219\n"
    "buffer_store_dwordx4 v[216:219], v22, s[24:27], s49 offen offset:0 sc1\n"
    "v_pk_mul_f32 v[232:233], v[164:165], s[84:85]\n"
    "v_pk_mul_f32 v[234:235], v[166:167], s[84:85]\n"
    "v_pk_mul_f32 v[236:237], v[252:253], s[84:85]\n"
    "v_pk_mul_f32 v[238:239], v[254:255], s[84:85]\n"
    "v_exp_f32 v232, v232\n"
    "v_exp_f32 v233, v233\n"
    "v_exp_f32 v234, v234\n"
    "v_exp_f32 v235, v235\n"
    "v_exp_f32 v236, v236\n"
    "v_exp_f32 v237, v237\n"
    "v_exp_f32 v238, v238\n"
    "v_exp_f32_e64 v239, v239\n"
    "v_pk_mul_f32 v[164:165], v[164:165], v[144:145]\n"
    "v_pk_mul_f32 v[166:167], v[166:167], v[146:147]\n"
    "v_pk_mul_f32 v[252:253], v[252:253], v[148:149]\n"
    "v_pk_mul_f32 v[254:255], v[254:255], v[150:151]\n"
    "v_pk_add_f32 v[232:233], v[232:233], 1.0 op_sel_hi:[1,0]\n"
    "v_pk_add_f32 v[234:235], v[234:235], 1.0 op_sel_hi:[1,0]\n"
    "v_pk_add_f32 v[236:237], v[236:237], 1.0 op_sel_hi:[1,0]\n"
    "v_pk_add_f32 v[238:239], v[238:239], 1.0 op_sel_hi:[1,0]\n"
    "v_rcp_f32 v232, v232\n"
    "v_rcp_f32 v233, v233\n"
    "v_rcp_f32 v234, v234\n"
    "v_rcp_f32 v235, v235\n"
    "v_rcp_f32 v236, v236\n"
    "v_rcp_f32 v237, v237\n"
    "v_rcp_f32 v238, v238\n"
    "v_rcp_f32 v239, v239\n"
    "v_pk_fma_f32 v[164:165], v[164:165], v[232:233], v[200:201]\n"
    "v_pk_fma_f32 v[166:167], v[166:167], v[234:235], v[202:203]\n"
    "v_pk_fma_f32 v[252:253], v[252:253], v[236:237], v[204:205]\n"
    "v_pk_fma_f32 v[254:255], v[254:255], v[238:239], v[206:207]\n"
    "v_cvt_pk_bf16_f32 v232, v164, v165\n"
    "v_cvt_pk_bf16_f32 v233, v166, v167\n"
    "v_cvt_pk_bf16_f32 v234, v252, v253\n"
    "v_cvt_pk_bf16_f32 v235, v254, v255\n"
    "s_nop 0\n"
    "v_permlane16_swap_b32 v232, v234\n"
    "v_permlane16_swap_b32 v233, v235\n"
    "buffer_store_dwordx4 v[232:235], v22, s[24:27], s49 offen offset:64 sc1\n"
    "s_or_b32 s86, s100, s48\n"
    "s_cmp_eq_u32 s86, 0\n"
    "s_cselect_b32 s86, 0, 2130706432\n"
    "s_add_u32 s86, s86, 12648448\n"
    "buffer_store_dwordx2 v[60:61], off, s[16:19], s86 sc1\n"
    "s_endpgm\n"
    "LPAD13_%=:\n"
    "s_cbranch_execz LPADS_%=\n"
    "LSPA_%=:\n"
    "s_waitcnt lgkmcnt(0)\n"
    "v_and_b32 v162, 31, v30\n"
    "s_lshr_b32 s91, s48, 1\n"
    "s_lshl_b32 s91, s91, 13\n"
    "s_add_u32 s91, s91, 0x24000\n"
    "v_lshl_add_u32 v162, v162, 2, s91\n"
    "v_lshrrev_b32 v165, 5, v30\n"
    "v_lshl_add_u32 v162, v165, 9, v162\n"
    "v_lshrrev_b32 v160, 5, v30\n"
    "v_lshlrev_b32 v160, 7, v160\n"
    "v_and_b32 v165, 31, v30\n"
    "s_lshr_b32 s90, s48, 1\n"
    "s_sub_u32 s91, 0xa0, s90\n"
    "v_sub_u32 v165, s91, v165\n"
    "v_sub_u32 v160, v165, v160\n"
    "s_lshr_b32 s91, s48, 1\n"
    "s_lshl_b32 s91, s91, 13\n"
    "s_add_u32 s91, s91, 0x24000\n"
    "v_lshl_add_u32 v160, v160, 2, s91\n"
    "v_lshrrev_b32 v161, 5, v30\n"
    "v_lshlrev_b32 v161, 7, v161\n"
    "v_and_b32 v165, 31, v30\n"
    "s_lshr_b32 s90, s48, 1\n"
    "s_sub_u32 s91, 0x400, s90\n"
    "v_sub_u32 v165, s91, v165\n"
    "v_sub_u32 v161, v165, v161\n"
    "v_and_b32 v161, 0x3ff, v161\n"
    "s_lshr_b32 s91, s48, 1\n"
    "s_lshl_b32 s91, s91, 13\n"
    "s_add_u32 s91, s91, 0x24000\n"
    "v_lshl_add_u32 v161, v161, 2, s91\n"
    "v_add_u32 v163, 128, v162\n"
    "s_nop 3\n"
    "ds_write2st64_b32 v162, v176, v192 offset0:0 offset1:16\n"
    "ds_write2st64_b32 v163, v177, v193 offset0:0 offset1:16\n"
    "ds_write2st64_b32 v162, v178, v194 offset0:1 offset1:17\n"
    "ds_write2st64_b32 v163, v179, v195 offset0:1 offset1:17\n"
    "ds_write2st64_b32 v162, v180, v196 offset0:4 offset1:20\n"
    "ds_write2st64_b32 v163, v181, v197 offset0:4 offset1:20\n"
    "ds_write2st64_b32 v162, v182, v198 offset0:5 offset1:21\n"
    "ds_write2st64_b32 v163, v183, v199 offset0:5 offset1:21\n"
    "ds_write2st64_b32 v162, v184, v200 offset0:8 offset1:24\n"
    "ds_write2st64_b32 v163, v185, v201 offset0:8 offset1:24\n"
    "ds_write2st64_b32 v162, v186, v202 offset0:9 offset1:25\n"
    "ds_write2st64_b32 v163, v187, v203 offset0:9 offset1:25\n"
    "ds_write2st64_b32 v162, v188, v204 offset0:12 offset1:28\n"
    "ds_write2st64_b32 v163, v189, v205 offset0:12 offset1:28\n"
    "ds_write2st64_b32 v162, v190, v206 offset0:13 offset1:29\n"
    "ds_write2st64_b32 v163, v191, v207 offset0:13 offset1:29\n"
    "s_branch LSPAr_%=\n"
    "LPAD14_%=:\n"
    "s_cbranch_execz LPADS_%=\n"
    "LSPB_%=:\n"
    "s_waitcnt lgkmcnt(0)\n"
    "ds_read_b32 v120, v161 offset:0\n"
    "ds_read_b32 v136, v161 offset:4096\n"
    "ds_read_b32 v121, v160 offset:3328\n"
    "ds_read_b32 v137, v160 offset:7424\n"
    "ds_read_b32 v122, v160 offset:3200\n"
    "ds_read_b32 v138, v160 offset:7296\n"
    "ds_read_b32 v123, v160 offset:3072\n"
    "ds_read_b32 v139, v160 offset:7168\n"
    "ds_read_b32 v124, v160 offset:2432\n"
    "ds_read_b32 v140, v160 offset:6528\n"
    "ds_read_b32 v125, v160 offset:2304\n"
    "ds_read_b32 v141, v160 offset:6400\n"
    "ds_read_b32 v126, v160 offset:2176\n"
    "ds_read_b32 v142, v160 offset:6272\n"
    "ds_read_b32 v127, v160 offset:2048\n"
    "ds_read_b32 v143, v160 offset:6144\n"
    "ds_read_b32 v128, v160 offset:1408\n"
    "ds_read_b32 v144, v160 offset:5504\n"
    "ds_read_b32 v129, v160 offset:1280\n"
    "ds_read_b32 v145, v160 offset:5376\n"
    "ds_read_b32 v130, v160 offset:1152\n"
    "ds_read_b32 v146, v160 offset:5248\n"
    "ds_read_b32 v131, v160 offset:1024\n"
    "ds_read_b32 v147, v160 offset:5120\n"
    "ds_read_b32 v132, v160 offset:384\n"
    "ds_read_b32 v148, v160 offset:4480\n"
    "ds_read_b32 v133, v160 offset:256\n"
    "ds_read_b32 v149, v160 offset:4352\n"
    "ds_read_b32 v134, v160 offset:128\n"
    "ds_read_b32 v150, v160 offset:4224\n"
    "ds_read_b32 v135, v160 offset:0\n"
    "ds_read_b32 v151, v160 offset:4096\n"
    "v_lshlrev_b32 v240, 16, v224\n"
    "v_and_b32 v241, 0xffff0000, v224\n"
    "v_lshlrev_b32 v242, 16, v226\n"
    "v_and_b32 v243, 0xffff0000, v226\n"
    "s_waitcnt lgkmcnt(15)\n"
    "v_pk_fma_f32 v[176:177], v[120:121], v[240:241], v[176:177]\n"
    "v_pk_fma_f32 v[192:193], v[120:121], v[242:243], v[192:193]\n"
    "v_pk_fma_f32 v[176:177], v[136:137], v[242:243], v[176:177]\n"
    "v_pk_fma_f32 v[192:193], v[136:137], v[240:241], v[192:193] neg_lo:[1,0,0] neg_hi:[1,0,0]\n"
    "v_lshlrev_b32 v244, 16, v225\n"
    "v_and_b32 v245, 0xffff0000, v225\n"
    "v_lshlrev_b32 v246, 16, v227\n"
    "v_and_b32 v247, 0xffff0000, v227\n"
    "v_pk_fma_f32 v[178:179], v[122:123], v[244:245], v[178:179]\n"
    "v_pk_fma_f32 v[194:195], v[122:123], v[246:247], v[194:195]\n"
    "v_pk_fma_f32 v[178:179], v[138:139], v[246:247], v[178:179]\n"
    "v_pk_fma_f32 v[194:195], v[138:139], v[244:245], v[194:195] neg_lo:[1,0,0] neg_hi:[1,0,0]\n"
    "v_lshlrev_b32 v248, 16, v228\n"
    "v_and_b32 v249, 0xffff0000, v228\n"
    "v_lshlrev_b32 v250, 16, v230\n"
    "v_and_b32 v251, 0xffff0000, v230\n"
    "v_pk_fma_f32 v[180:181], v[124:125], v[248:249], v[180:181]\n"
    "v_pk_fma_f32 v[196:197], v[124:125], v[250:251], v[196:197]\n"
    "v_pk_fma_f32 v[180:181], v[140:141], v[250:251], v[180:181]\n"
    "v_pk_fma_f32 v[196:197], v[140:141], v[248:249], v[196:197] neg_lo:[1,0,0] neg_hi:[1,0,0]\n"
    "v_lshlrev_b32 v252, 16, v229\n"
    "v_and_b32 v253, 0xffff0000, v229\n"
    "v_lshlrev_b32 v254, 16, v231\n"
    "v_and_b32 v255, 0xffff0000, v231\n"
    "v_pk_fma_f32 v[182:183], v[126:127], v[252:253], v[182:183]\n"
    "v_pk_fma_f32 v[198:199], v[126:127], v[254:255], v[198:199]\n"
    "v_pk_fma_f32 v[182:183], v[142:143], v[254:255], v[182:183]\n"
    "v_pk_fma_f32 v[198:199], v[142:143], v[252:253], v[198:199] neg_lo:[1,0,0] neg_hi:[1,0,0]\n"
    "v_lshlrev_b32 v240, 16, v232\n"
    "v_and_b32 v241, 0xffff0000, v232\n"
    "v_lshlrev_b32 v242, 16, v234\n"
    "v_and_b32 v243, 0xffff0000, v234\n"
    "s_waitcnt lgkmcnt(13)\n"
    "v_pk_fma_f32 v[184:185], v[128:129], v[240:241], v[184:185]\n"
    "v_pk_fma_f32 v[200:201], v[128:129], v[242:243], v[200:201]\n"
    "s_waitcnt lgkmcnt(12)\n"
    "v_pk_fma_f32 v[184:185], v[144:145], v[242:243], v[184:185]\n"
    "v_pk_fma_f32 v[200:201], v[144:145], v[240:241], v[200:201] neg_lo:[1,0,0] neg_hi:[1,0,0]\n"
    "v_lshlrev_b32 v244, 16, v233\n"
    "v_and_b32 v245, 0xffff0000, v233\n"
    "v_lshlrev_b32 v246, 16, v235\n"
    "v_and_b32 v247, 0xffff0000, v235\n"
    "s_waitcnt lgkmcnt(9)\n"
    "v_pk_fma_f32 v[186:187], v[130:131], v[244:245], v[186:187]\n"
    "v_pk_fma_f32 v[202:203], v[130:131], v[246:247], v[202:203]\n"
    "s_waitcnt lgkmcnt(8)\n"
    "v_pk_fma_f32 v[186:187], v[146:147], v[246:247], v[186:187]\n"
    "v_pk_fma_f32 v[202:203], v[146:147], v[244:245], v[202:203] neg_lo:[1,0,0] neg_hi:[1,0,0]\n"
    "v_lshlrev_b32 v248, 16, v236\n"
    "v_and_b32 v249, 0xffff0000, v236\n"
    "v_lshlrev_b32 v250, 16, v238\n"
    "v_and_b32 v251, 0xffff0000, v238\n"
    "s_waitcnt lgkmcnt(5)\n"
    "v_pk_fma_f32 v[188:189], v[132:133], v[248:249], v[188:189]\n"
    "v_pk_fma_f32 v[204:205], v[132:133], v[250:251], v[204:205]\n"
    "s_waitcnt lgkmcnt(4)\n"
    "v_pk_fma_f32 v[188:189], v[148:149], v[250:251], v[188:189]\n"
    "v_pk_fma_f32 v[204:205], v[148:149], v[248:249], v[204:205] neg_lo:[1,0,0] neg_hi:[1,0,0]\n"
    "v_lshlrev_b32 v252, 16, v237\n"
    "v_and_b32 v253, 0xffff0000, v237\n"
    "v_lshlrev_b32 v254, 16, v239\n"
    "v_and_b32 v255, 0xffff0000, v239\n"
    "s_waitcnt lgkmcnt(1)\n"
    "v_pk_fma_f32 v[190:191], v[134:135], v[252:253], v[190:191]\n"
    "v_pk_fma_f32 v[206:207], v[134:135], v[254:255], v[206:207]\n"
    "s_waitcnt lgkmcnt(0)\n"
    "v_pk_fma_f32 v[190:191], v[150:151], v[254:255], v[190:191]\n"
    "v_pk_fma_f32 v[206:207], v[150:151], v[252:253], v[206:207] neg_lo:[1,0,0] neg_hi:[1,0,0]\n"
    "s_branch LSPBr_%=\n"
    "LPLZ_%=s:\n"
    "s_mov_b32 s47, 0\n"
    "LPLZ_%=l:\n"
    "s_sleep 24\n"
    "buffer_load_dwordx2 v[62:63], v25, s[16:19], s87 offen sc1\n"
    "s_waitcnt vmcnt(0)\n"
    "v_cmp_ne_u64 vcc, s[98:99], v[62:63]\n"
    "s_nop 4\n"
    "s_add_u32 s47, s47, 1\n"
    "s_cbranch_vccz LPLZ_%=\n"
    "s_cmp_lt_u32 s47, 0x8000\n"
    "s_cbranch_scc1 LPLZ_%=l\n"
    "s_branch LPLZ_%=\n"
    "LPAD15_%=:\n"
    "s_cbranch_execz LPADS_%=\n"
    "LSPW_%=:\n"
    "s_add_u32 s47, s93, 8192\n"
    "buffer_load_dwordx4 v[224:227], v31, s[16:19], s47 offen offset:0 sc1\n"
    "buffer_load_dwordx4 v[228:231], v31, s[16:19], s47 offen offset:1024 sc1\n"
    "buffer_load_dwordx4 v[232:235], v31, s[16:19], s47 offen offset:2048 sc1\n"
    "buffer_load_dwordx4 v[236:239], v31, s[16:19], s47 offen offset:3072 sc1\n"
    "s_branch LSPWr_%=\n"
    "LPLQ_%=s:\n"
    "s_mov_b32 s47, 0\n"
    "LPLQ_%=l:\n"
    "s_sleep 24\n"
    "buffer_load_dwordx2 v[62:63], v25, s[16:19], s87 offen sc1\n"
    "s_waitcnt vmcnt(0)\n"
    "v_cmp_ne_u64 vcc, s[98:99], v[62:63]\n"
    "s_nop 4\n"
    "s_add_u32 s47, s47, 1\n"
    "s_cbranch_vccz LPLQ_%=\n"
    "s_cmp_lt_u32 s47, 0x8000\n"
    "s_cbranch_scc1 LPLQ_%=l\n"
    "s_branch LPLQ_%=\n"
    "LPLH1_%=s:\n"
    "s_mov_b32 s47, 0\n"
    "LPLH1_%=l:\n"
    "s_sleep 1\n"
    "buffer_load_dwordx2 v[62:63], v24, s[16:19], s87 offen sc1\n"
    "s_waitcnt vmcnt(0)\n"
    "v_cmp_ne_u64 vcc, s[98:99], v[62:63]\n"
    "s_nop 4\n"
    "s_add_u32 s47, s47, 1\n"
    "s_cbranch_vccz LPLH1_%=\n"
    "s_cmp_lt_u32 s47, 0x8000\n"
    "s_cbranch_scc1 LPLH1_%=l\n"
    "s_branch LPLH1_%=\n"
    "LPLH3_%=s:\n"
    "s_mov_b32 s47, 0\n"
    "LPLH3_%=l:\n"
    "s_sleep 1\n"
    "buffer_load_dwordx2 v[62:63], v24, s[16:19], s87 offen sc1\n"
    "s_waitcnt vmcnt(0)\n"
    "v_cmp_ne_u64 vcc, s[98:99], v[62:63]\n"
    "s_nop 4\n"
    "s_add_u32 s47, s47, 1\n"
    "s_cbranch_vccz LPLH3_%=\n"
    "s_cmp_lt_u32 s47, 0x8000\n"
    "s_cbranch_scc1 LPLH3_%=l\n"
    "s_branch LPLH3_%=\n"
    "LPLPA_%=s:\n"
    "s_mov_b32 s47, 0\n"
    "LPLPA_%=l:\n"
    "s_sleep 1\n"
    "buffer_load_dwordx2 v[62:63], v25, s[16:19], s87 offen sc1\n"
    "s_waitcnt vmcnt(0)\n"
    "v_cmp_ne_u64 vcc, s[98:99], v[62:63]\n"
    "s_nop 4\n"
    "s_add_u32 s47, s47, 1\n"
    "s_cbranch_vccz LPLPA_%=\n"
    "s_cmp_lt_u32 s47, 0x8000\n"
    "s_cbranch_scc1 LPLPA_%=l\n"
    "s_branch LPLPA_%=\n"
    "LPLPB_%=s:\n"
    "s_mov_b32 s47, 0\n"
    "LPLPB_%=l:\n"
    "s_sleep 1\n"
    "buffer_load_dwordx2 v[62:63], v25, s[16:19], s87 offen sc1\n"
    "s_waitcnt vmcnt(0)\n"
    "v_cmp_ne_u64 vcc, s[98:99], v[62:63]\n"
    "s_nop 4\n"
    "s_add_u32 s47, s47, 1\n"
    "s_cbranch_vccz LPLPB_%=\n"
    "s_cmp_lt_u32 s47, 0x8000\n"
    "s_cbranch_scc1 LPLPB_%=l\n"
    "s_branch LPLPB_%=\n"
    "LPLS_%=s:\n"
    "s_mov_b32 s47, 0\n"
    "LPLS_%=l:\n"
    "s_sleep 1\n"
    "buffer_load_dwordx2 v[62:63], v27, s[16:19], s87 offen sc1\n"
    "s_waitcnt vmcnt(0)\n"
    "v_cmp_ne_u64 vcc, s[98:99], v[62:63]\n"
    "s_nop 4\n"
    "s_add_u32 s47, s47, 1\n"
    "s_cbranch_vccz LPLS_%=\n"
    "s_cmp_lt_u32 s47, 0x8000\n"
    "s_cbranch_scc1 LPLS_%=l\n"
    "s_branch LPLS_%=\n"
    "LPLPY_%=s:\n"
    "s_mov_b32 s47, 0\n"
    "LPLPY_%=l:\n"
    "s_sleep 1\n"
    "buffer_load_dwordx2 v[62:63], v25, s[16:19], s87 offen sc1\n"
    "s_waitcnt vmcnt(0)\n"
    "v_cmp_ne_u64 vcc, s[98:99], v[62:63]\n"
    "s_nop 4\n"
    "s_add_u32 s47, s47, 1\n"
    "s_cbranch_vccz LPLPY_%=\n"
    "s_cmp_lt_u32 s47, 0x8000\n"
    "s_cbranch_scc1 LPLPY_%=l\n"
    "s_branch LPLPY_%=\n"
    "LPLXN_%=s:\n"
    "s_mov_b32 s47, 0\n"
    "LPLXN_%=l:\n"
    "s_sleep 1\n"
    "buffer_load_dwordx2 v[62:63], v27, s[16:19], s87 offen sc1\n"
    "s_waitcnt vmcnt(0)\n"
    "v_cmp_ne_u64 vcc, s[98:99], v[62:63]\n"
    "s_nop 4\n"
    "s_add_u32 s47, s47, 1\n"
    "s_cbranch_vccz LPLXN_%=\n"
    "s_cmp_lt_u32 s47, 0x8000\n"
    "s_cbranch_scc1 LPLXN_%=l\n"
    "s_branch LPLXN_%=\n"
    "LEND_%=:\n"
    :: "{s[98:99]}"(ka), "{s[68:69]}"(tb), "{s100}"(blockIdx.x), "{v0}"(threadIdx.x)
    : "memory","scc","vcc","v1","v2","v3","v4","v5","v6","v7","v8","v9","v10","v11","v12","v13","v14","v15","v16","v17","v18","v19","v20","v21","v22","v23","v24","v25","v26","v27","v28","v29","v30","v31","v32","v33","v34","v35","v36","v37","v38","v39","v40","v41","v42","v43","v44","v45","v46","v47","v48","v49","v50","v51","v52","v53","v54","v55","v56","v57","v58","v59","v60","v61","v62","v63","v64","v65","v66","v67","v68","v69","v70","v71","v72","v73","v74","v75","v76","v77","v78","v79","v80","v81","v82","v83","v84","v85","v86","v87","v88","v89","v90","v91","v92","v93","v94","v95","v96","v97","v98","v99","v100","v101","v102","v103","v104","v105","v106","v107","v108","v109","v110","v111","v112","v113","v114","v115","v116","v117","v118","v119","v120","v121","v122","v123","v124","v125","v126","v127","v128","v129","v130","v131","v132","v133","v134","v135","v136","v137","v138","v139","v140","v141","v142","v143","v144","v145","v146","v147","v148","v149","v150","v151","v152","v153","v154","v155","v156","v157","v158","v159","v160","v161","v162","v163","v164","v165","v166","v167","v168","v169","v170","v171","v172","v173","v174","v175","v176","v177","v178","v179","v180","v181","v182","v183","v184","v185","v186","v187","v188","v189","v190","v191","v192","v193","v194","v195","v196","v197","v198","v199","v200","v201","v202","v203","v204","v205","v206","v207","v208","v209","v210","v211","v212","v213","v214","v215","v216","v217","v218","v219","v220","v221","v222","v223","v224","v225","v226","v227","v228","v229","v230","v231","v232","v233","v234","v235","v236","v237","v238","v239","v240","v241","v242","v243","v244","v245","v246","v247","v248","v249","v250","v251","v252","v253","v254","v255","a0","a1","a2","a3","a4","a5","a6","a7","a8","a9","a10","a11","a12","a13","a14","a15","a16","a17","a18","a19","a20","a21","a22","a23","a24","a25","a26","a27","a28","a29","a30","a31","a32","a33","a34","a35","a36","a37","a38","a39","a40","a41","a42","a43","a44","a45","a46","a47","a48","a49","a50","a51","a52","a53","a54","a55","a56","a57","a58","a59","a60","a61","a62","a63","a64","a65","a66","a67","a68","a69","a70","a71","a72","a73","a74","a75","a76","a77","a78","a79","a80","a81","a82","a83","a84","a85","a86","a87","a88","a89","a90","a91","a92","a93","a94","a95","a96","a97","a98","a99","a100","a101","a102","a103","a104","a105","a106","a107","a108","a109","a110","a111","a112","a113","a114","a115","a116","a117","a118","a119","a120","a121","a122","a123","a124","a125","a126","a127","a128","a129","a130","a131","a132","a133","a134","a135","a136","a137","a138","a139","a140","a141","a142","a143","a144","a145","a146","a147","a148","a149","a150","a151","a152","a153","a154","a155","a156","a157","a158","a159","a160","a161","a162","a163","a164","a165","a166","a167","a168","a169","a170","a171","a172","a173","a174","a175","a176","a177","a178","a179","a180","a181","a182","a183","a184","a185","a186","a187","a188","a189","a190","a191","a192","a193","a194","a195","a196","a197","a198","a199","a200","a201","a202","a203","a204","a205","a206","a207","a208","a209","a210","a211","a212","a213","a214","a215","a216","a217","a218","a219","a220","a221","a222","a223","a224","a225","a226","a227","a228","a229","a230","a231","a232","a233","a234","a235","a236","a237","a238","a239","a240","a241","a242","a243","a244","a245","a246","a247","a248","a249","a250","a251","a252","a253","a254","a255","s0","s1","s2","s3","s4","s5","s6","s7","s8","s9","s10","s11","s12","s13","s14","s15","s16","s17","s18","s19","s20","s21","s22","s23","s24","s25","s26","s27","s28","s29","s30","s31","s32","s33","s34","s35","s36","s37","s38","s39","s40","s41","s42","s43","s44","s45","s46","s47","s48","s49","s50","s51","s52","s53","s54","s55","s56","s57","s58","s59","s60","s61","s62","s63","s64","s65","s66","s67","s70","s71","s72","s73","s74","s75","s76","s77","s78","s79","s80","s81","s82","s83","s84","s85","s86","s87","s88","s89","s90","s91","s92","s93","s94","s95","s96","s97","s101");
}

extern "C" void kernel_launch(void* const* d_in, const int* in_sizes, int n_in, void* d_out, int out_size, void* d_ws, size_t ws_size, hipStream_t stream) {
  (void)hipFuncSetAttribute((const void*)k_fused, hipFuncAttributeMaxDynamicSharedMemorySize, 163840);
  k_fused<<<256, 256, 163840, stream>>>(d_in[0], d_in[1], d_in[2], d_in[4], d_in[5], d_in[6], d_in[7], d_ws, d_out);
}
```
